```python
import torch

def _softmax(x, axis=-1):
    return torch.softmax(x, dim=axis)
def _gelu(x):
    return torch.nn.functional.gelu(x, approximate="tanh")
def _silu(x):
    return torch.nn.functional.silu(x)
def _sigmoid(x):
    return torch.sigmoid(x)
def _layernorm(x, axis=-1, eps=1e-5):
    m = x.mean(dim=axis, keepdim=True)
    v = x.var(dim=axis, keepdim=True, unbiased=False)
    return (x - m) / torch.sqrt(v + eps)
def _scatter_add(tgt, idx, src, axis=0):
    return tgt.clone().index_add_(axis, idx, src)
def _segment_sum(src, seg, n):
    out = torch.zeros((n,) + src.shape[1:], dtype=src.dtype)
    return out.index_add_(0, seg, src)
def _pad(x, pw):
    flat = tuple(v for pair in reversed(pw) for v in pair)
    return torch.nn.functional.pad(x, flat)

kernel_name = 'sequential_preLN__linear_attn_block__dense_activation_relu__prompt'

def setup_inputs():

    _w = (lambda a: (lambda t: t.to(torch.bfloat16) if t.is_floating_point() else t)(torch.as_tensor(a)))
    def _A_setup():
        B, S, D = 1, 2048, 640
        return {
            "x": _w((lambda *s: torch.randn(*s, dtype=torch.bfloat16) * 0.05)(B, S, D)),
            "wqkv": _w((lambda *s: torch.randn(*s, dtype=torch.bfloat16) * 0.05)(D, 3 * D)),
            "wo": _w((lambda *s: torch.randn(*s, dtype=torch.bfloat16) * 0.05)(D, D)),
        }
    _A_in = _A_setup()

    def _B_setup():
        B, S, D, Dff = 1, 2048, 640, 1728
        return {
            "x": _w((lambda *s: torch.randn(*s, dtype=torch.bfloat16) * 0.05)(B, S, D)),
            "w1": _w((lambda *s: torch.randn(*s, dtype=torch.bfloat16) * 0.05)(D, Dff)),
            "w2": _w((lambda *s: torch.randn(*s, dtype=torch.bfloat16) * 0.05)(Dff, D)),
        }
    _B_in = _B_setup()

    return {
        "A_x": _A_in["x"],
        "A_wqkv": _A_in["wqkv"],
        "A_wo": _A_in["wo"],
        "B_x": _B_in["x"],
        "B_w1": _B_in["w1"],
        "B_w2": _B_in["w2"],
    }

def reference(A_x, A_wqkv, A_wo, B_x, B_w1, B_w2):
    def _A_ref(x, wqkv, wo):
        B, S, D = x.shape
        H = 32; Dh = D // H
        qkv = (lambda x, *d: x.permute(*d))((x @ wqkv).reshape(B, S, 3, H, Dh), 2, 0, 3, 1, 4)
        q, k, v = qkv[0], qkv[1], qkv[2]
        phi = lambda t: torch.where(t > 0, t + 1, torch.exp(t)) + 1e-6
        pq, pk = phi(q), phi(k)
        kv = torch.einsum("bhsd,bhse->bhde", pk, v)
        z = torch.einsum("bhsd->bhd", pk)
        num = torch.einsum("bhqd,bhde->bhqe", pq, kv)
        den = torch.einsum("bhqd,bhd->bhq", pq, z)[..., None] + 1e-6
        o = num / den
        return (lambda x, *d: x.permute(*d))(o, 0, 2, 1, 3).reshape(B, S, D) @ wo

    def _B_ref(x, w1, w2):
        h = torch.einsum("bsd,df->bsf", x, w1)
        zero = torch.zeros_like(h)
        h = torch.maximum(h, zero)
        return torch.einsum("bsf,fd->bsd", h, w2)

    x = A_x
    h = x + _A_ref(_layernorm(x, axis=-1), A_wqkv, A_wo)
    h2 = h + _B_ref(_layernorm(h, axis=-1), B_w1, B_w2)
    return h2
```

```cpp
#include <hip/hip_runtime.h>
#include <hip/hip_bf16.h>
typedef __hip_bfloat16 bf16;
constexpr int S=2048, D=640, H=32, DH=20, FF=1728;

__device__ inline float b2f(bf16 v){ return __bfloat162float(v); }
__device__ inline bf16 f2b(float v){ return __float2bfloat16(v); }

__global__ void ln_k(const bf16* x, bf16* y){
  int r=blockIdx.x; __shared__ float red[256];
  float s=0; for(int i=threadIdx.x;i<D;i+=256) s+=b2f(x[r*D+i]);
  red[threadIdx.x]=s; __syncthreads();
  for(int o=128;o>0;o>>=1){ if(threadIdx.x<o) red[threadIdx.x]+=red[threadIdx.x+o]; __syncthreads(); }
  float m=red[0]/D; __syncthreads();
  s=0; for(int i=threadIdx.x;i<D;i+=256){ float d=b2f(x[r*D+i])-m; s+=d*d; }
  red[threadIdx.x]=s; __syncthreads();
  for(int o=128;o>0;o>>=1){ if(threadIdx.x<o) red[threadIdx.x]+=red[threadIdx.x+o]; __syncthreads(); }
  float rs=rsqrtf(red[0]/D+1e-5f);
  for(int i=threadIdx.x;i<D;i+=256) y[r*D+i]=f2b((b2f(x[r*D+i])-m)*rs);
}
__global__ void gemm_k(const bf16* A,const bf16* B,bf16* C,const bf16* res,int M,int N,int K,int mode){
  __shared__ float As[16][17], Bs[16][17];
  int tx=threadIdx.x&15, ty=threadIdx.x>>4;
  int row=blockIdx.y*16+ty, col=blockIdx.x*16+tx;
  float acc=0;
  for(int k0=0;k0<K;k0+=16){
    As[ty][tx]=b2f(A[row*K+k0+tx]); Bs[ty][tx]=b2f(B[(k0+ty)*N+col]);
    __syncthreads();
    for(int k=0;k<16;k++) acc+=As[ty][k]*Bs[k][tx];
    __syncthreads();
  }
  if(mode==1) acc=fmaxf(acc,0.f);
  if(mode==2) acc=b2f(f2b(acc))+b2f(res[row*N+col]);
  C[row*N+col]=f2b(acc);
}
__device__ inline float phi(float t){ return (t>0.f? t+1.f: __expf(t))+1e-6f; }
__global__ void kv_k(const bf16* qkv,float* kv,float* z){
  int h=blockIdx.x; int t=threadIdx.x;
  if(t<400){ int d=t/20,e=t%20; float s=0; for(int i=0;i<S;i++) s+=phi(b2f(qkv[i*1920+640+h*20+d]))*b2f(qkv[i*1920+1280+h*20+e]); kv[h*400+t]=s; }
  else if(t<420){ int d=t-400; float s=0; for(int i=0;i<S;i++) s+=phi(b2f(qkv[i*1920+640+h*20+d])); z[h*20+d]=s; }
}
__global__ void o_k(const bf16* qkv,const float* kv,const float* z,bf16* o){
  int s=blockIdx.x;
  for(int c=threadIdx.x;c<D;c+=blockDim.x){ int h=c/20,e=c%20; float num=0,den=0;
    for(int d=0;d<20;d++){ float p=phi(b2f(qkv[s*1920+h*20+d])); num+=p*kv[h*400+d*20+e]; den+=p*z[h*20+d]; }
    o[s*D+c]=f2b(num/(den+1e-6f)); }
}
extern "C" void kernel_launch(void* const* d_in, const int* in_sizes, int n_in, void* d_out, int out_size, void* d_ws, size_t ws_size, hipStream_t stream) {
  const bf16* x=(const bf16*)d_in[0]; const bf16* wqkv=(const bf16*)d_in[1]; const bf16* wo=(const bf16*)d_in[2];
  const bf16* w1=(const bf16*)d_in[4]; const bf16* w2=(const bf16*)d_in[5];
  char* ws=(char*)d_ws;
  bf16* ln=(bf16*)ws; bf16* qkv=(bf16*)(ws+(4<<20)); bf16* o=(bf16*)(ws+(16<<20)); bf16* h=(bf16*)(ws+(20<<20)); bf16* hid=(bf16*)(ws+(24<<20));
  float* kv=(float*)(ws+(40<<20)); float* z=(float*)(ws+(41<<20));
  ln_k<<<S,256,0,stream>>>(x,ln);
  gemm_k<<<dim3(1920/16,S/16),256,0,stream>>>(ln,wqkv,qkv,nullptr,S,1920,D,0);
  kv_k<<<H,448,0,stream>>>(qkv,kv,z);
  o_k<<<S,256,0,stream>>>(qkv,kv,z,o);
  gemm_k<<<dim3(D/16,S/16),256,0,stream>>>(o,wo,h,x,S,D,D,2);
  ln_k<<<S,256,0,stream>>>(h,ln);
  gemm_k<<<dim3(FF/16,S/16),256,0,stream>>>(ln,w1,hid,nullptr,S,FF,D,1);
  gemm_k<<<dim3(D/16,S/16),256,0,stream>>>(hid,w2,(bf16*)d_out,h,S,D,FF,2);
}
```

```cpp
#include <hip/hip_runtime.h>
#include <hip/hip_bf16.h>
#include <stdint.h>

typedef unsigned v4u __attribute__((ext_vector_type(4)));
typedef unsigned v3u __attribute__((ext_vector_type(3)));
typedef unsigned v2u __attribute__((ext_vector_type(2)));
typedef float f32x4 __attribute__((ext_vector_type(4)));
typedef short bf16x8 __attribute__((ext_vector_type(8)));
typedef short s16x4 __attribute__((ext_vector_type(4)));
typedef __bf16 bf2 __attribute__((ext_vector_type(2)));
typedef __amdgpu_buffer_rsrc_t rsrc_t;
#define GAS __attribute__((address_space(1)))
#define LAS __attribute__((address_space(3)))
#define DI __device__ __forceinline__
#define RLX __ATOMIC_RELAXED, __HIP_MEMORY_SCOPE_AGENT
typedef GAS unsigned gu32;

#define TS 0

constexpr unsigned OFF_EPOCH = 0;
constexpr unsigned OFF_FLAG = 4096;
constexpr unsigned OFF_FLAGB = OFF_FLAG + 5 * 256 * 128;
constexpr unsigned OFF_TS = 65 * 1024 * 1024;
constexpr unsigned OFF_TS2 = 64 * 1024 * 1024;
constexpr unsigned OFF_XN = 256 * 1024;
constexpr unsigned WQKV_STR = 153600;
constexpr unsigned OFF_WQKV = OFF_XN + 128 * 20 * 1024;
constexpr unsigned OFF_WO = OFF_WQKV + 16 * WQKV_STR;
constexpr unsigned W1_STR = 20 * 7 * 1024;
constexpr unsigned OFF_W1 = OFF_WO + 8 * 102400;
constexpr unsigned W2_STR = 54 * 5 * 1024;
constexpr unsigned OFF_W2 = OFF_W1 + 16 * W1_STR;
constexpr unsigned PART_STR = 3456;
constexpr unsigned OFF_PART = OFF_W2 + 8 * W2_STR;
constexpr unsigned OFF_O = OFF_PART + 256 * PART_STR;
constexpr unsigned OFF_H = OFF_O + 128 * 20 * 1024;
constexpr unsigned OFF_ST = OFF_H + 128 * 20 * 1024;
constexpr unsigned OFF_HID = OFF_ST + 2048 * 64;
constexpr unsigned WS_BYTES = OFF_HID + 128 * 54 * 1024;

constexpr unsigned L_A = 0;
constexpr unsigned L_B = 102400;
constexpr unsigned L_M = 153600;
constexpr unsigned L_IMG = L_M;
constexpr unsigned L_CS = L_M + 4096;
constexpr unsigned LDS_BYTES = 163840;
constexpr unsigned EP = 272;

constexpr int AUX_SC1 = 16;
constexpr unsigned SPIN_CAP = 1u << 20;

DI f32x4 mfma(v4u a, v4u b, f32x4 c) {
  return __builtin_amdgcn_mfma_f32_16x16x32_bf16(__builtin_bit_cast(bf16x8, a), __builtin_bit_cast(bf16x8, b), c, 0, 0, 0);
}
DI unsigned pk2(float a, float b) { bf2 r = {(__bf16)a, (__bf16)b}; return __builtin_bit_cast(unsigned, r); }
DI float asf(unsigned u) { return __builtin_bit_cast(float, u); }
DI unsigned asu(float f) { return __builtin_bit_cast(unsigned, f); }
DI float blo(unsigned u) { return asf(u << 16); }
DI float bhi(unsigned u) { return asf(u & 0xffff0000u); }
DI float phi(float t) { return (t > 0.f ? t + 1.f : __expf(t)) + 1e-6f; }
template <int C> DI float dpp_add(float v) { return v + asf((unsigned)__builtin_amdgcn_update_dpp(0, (int)asu(v), C, 0xF, 0xF, true)); }
DI float sum16(float v) { v = dpp_add<0xB1>(v); v = dpp_add<0x4E>(v); v = dpp_add<0x141>(v); v = dpp_add<0x140>(v); return v; }
DI float sum_x16(float v) { const unsigned u = asu(v); auto r = __builtin_amdgcn_permlane16_swap(u, u, false, false); return asf(r[0]) + asf(r[1]); }
DI float sum_x32(float v) { const unsigned u = asu(v); auto r = __builtin_amdgcn_permlane32_swap(u, u, false, false); return asf(r[0]) + asf(r[1]); }
DI float sum_g(float v) { return sum_x32(sum_x16(v)); }
#define SCHED() __builtin_amdgcn_sched_barrier(0)
#define SGB(mask, n) __builtin_amdgcn_sched_group_barrier(mask, n, 0)
#define DRAIN() asm volatile("s_waitcnt vmcnt(0)" ::: "memory")
#define BAR() do { asm volatile("s_waitcnt lgkmcnt(0)" ::: "memory"); __builtin_amdgcn_s_barrier(); asm volatile("" ::: "memory"); } while (0)

DI v4u ld128(rsrc_t rs, unsigned voff, unsigned soff) { return __builtin_amdgcn_raw_buffer_load_b128(rs, voff, soff, AUX_SC1); }
DI void st128(v4u v, rsrc_t rs, unsigned voff, unsigned soff) { __builtin_amdgcn_raw_buffer_store_b128(v, rs, voff, soff, AUX_SC1); }
DI void st64(v2u v, rsrc_t rs, unsigned voff, unsigned soff) { __builtin_amdgcn_raw_buffer_store_b64(v, rs, voff, soff, AUX_SC1); }

DI void dma4(rsrc_t rs, unsigned voff, unsigned soff, unsigned m0v) {
  asm volatile("s_nop 4\n\ts_mov_b32 m0, %0\n\ts_nop 0\n\t"
               "buffer_load_dwordx4 %1, %2, %3 offen sc1 lds\n\t"
               "buffer_load_dwordx4 %1, %2, %3 offen offset:1024 sc1 lds\n\t"
               "buffer_load_dwordx4 %1, %2, %3 offen offset:2048 sc1 lds\n\t"
               "buffer_load_dwordx4 %1, %2, %3 offen offset:3072 sc1 lds"
               :: "s"(m0v), "v"(voff), "s"(rs), "s"(soff) : "m0", "memory");
}
DI void dma1(rsrc_t rs, unsigned voff, unsigned soff, unsigned m0v) {
  asm volatile("s_nop 4\n\ts_mov_b32 m0, %0\n\ts_nop 0\n\t"
               "buffer_load_dwordx4 %1, %2, %3 offen sc1 lds"
               :: "s"(m0v), "v"(voff), "s"(rs), "s"(soff) : "m0", "memory");
}
DI void dma_item(rsrc_t rs, unsigned voff, unsigned m0v) {
  asm volatile("s_nop 4\n\ts_mov_b32 m0, %0\n\ts_nop 0\n\t"
               "buffer_load_dwordx4 %1, %2, 0 offen lds"
               :: "s"(m0v), "v"(voff), "s"(rs) : "m0", "memory");
}
DI v4u item_read(LAS char* img, int lane) {
  LAS char* p = img + ((lane >> 2) & 3) * 128 + (lane >> 4) * 32 + (lane & 3) * 8;
  const s16x4 a = __builtin_amdgcn_ds_read_tr16_b64_v4i16((LAS s16x4*)p);
  const s16x4 b = __builtin_amdgcn_ds_read_tr16_b64_v4i16((LAS s16x4*)(p + 512));
  const v2u x = __builtin_bit_cast(v2u, a), y = __builtin_bit_cast(v2u, b);
  return v4u{x.x, x.y, y.x, y.y};
}
#define LDF(sbase, i) ld128(ws, l16 + ((i) & 3) * 1024, (sbase) + ((i) >> 2) * 4096)
#define LDSF(off) (*(LAS v4u*)(((off) < 65536 ? lb0 : ((off) < 131072 ? lb1 : lb2)) + ((off) & 65535)))
#define W1A(ks, n) (((ks) < 7 ? L_B + (ks) * 7168 : L_A + ((ks) - 7) * 7168) + (n) * 1024)

DI void wait_flag(gu32* f, unsigned tag, bool active) {
  unsigned sp = 0;
  for (;;) {
    unsigned v = active ? __hip_atomic_load(f, RLX) : tag;
    if (__all(v == tag)) break;
    if (++sp > SPIN_CAP) break;
  }
  __builtin_amdgcn_fence(__ATOMIC_ACQUIRE, "wavefront");
}

#define STAMP(k) do { } while (0)

__global__ __launch_bounds__(256, 1) void fused_block(const unsigned short* __restrict__ x, const unsigned short* __restrict__ wqkv,
                                                      const unsigned short* __restrict__ wo, const unsigned short* __restrict__ w1,
                                                      const unsigned short* __restrict__ w2, unsigned short* __restrict__ out,
                                                      char* __restrict__ wsp) {
  extern __shared__ __attribute__((aligned(16))) char smem[];
  LAS char* lds = (LAS char*)smem;
  const unsigned ldsb = (unsigned)(size_t)lds;
  const int tid = threadIdx.x, lane = tid & 63;
  const int w = __builtin_amdgcn_readfirstlane(tid >> 6);
  const int g = lane >> 4, t16 = lane & 15;
  const int b = blockIdx.x, xcd = b & 7, sl = b >> 3;
  const int c2 = xcd & 1, P = 4 * (xcd >> 1) + (sl >> 3), m = 8 * c2 + (sl & 7);
  const int cuid = P * 16 + m;
  const int jl = (m & 7) >> 2, ct = 4 * c2 + (m & 3);
  const unsigned l16 = lane * 16;
  const rsrc_t ws = __builtin_amdgcn_make_buffer_rsrc(wsp, 0, WS_BYTES, 0x00020000);
  gu32* flg = (gu32*)(wsp + OFF_FLAG);
  STAMP(0);
  const unsigned epoch = __hip_atomic_load((gu32*)(wsp + OFF_EPOCH), RLX);
  LAS char* lb0 = lds + l16; LAS char* lb1 = lb0 + 65536; LAS char* lb2 = lb0 + 131072;
  asm volatile("" : "+v"(lb1), "+v"(lb2));

  v2u xv[5];
  {
    const GAS char* xp = (const GAS char*)(x + (size_t)(128 * P + 8 * m + 2 * w + (lane >> 5)) * 640) + (lane & 31) * 8;
    asm volatile("global_load_dwordx2 %0, %5, off\n\tglobal_load_dwordx2 %1, %5, off offset:256\n\tglobal_load_dwordx2 %2, %5, off offset:512\n\t"
                 "global_load_dwordx2 %3, %5, off offset:768\n\tglobal_load_dwordx2 %4, %5, off offset:1024"
                 : "=&v"(xv[0]), "=&v"(xv[1]), "=&v"(xv[2]), "=&v"(xv[3]), "=&v"(xv[4]) : "v"(xp) : "memory");
  }
  const int r8 = lane >> 3, ch8 = lane & 7;
  {
    const rsrc_t rq = __builtin_amdgcn_make_buffer_rsrc((void*)wqkv, 0, 640 * 1920 * 2, 0x00020000);
    const int col0 = ch8 < 5 ? 640 + 40 * m + 8 * ch8 : 1280 + 40 * m + 8 * (ch8 - 5);
    const int col1 = ch8 < 2 ? 1280 + 40 * m + 24 + 8 * ch8 : 40 * m + 8 * ((ch8 < 7 ? ch8 : 6) - 2);
#pragma unroll
    for (int k = 0; k < 3; ++k) {
      const int q = w + 4 * k;
      if (q < 10) dma_item(rq, (unsigned)((40 * P + 8 * (q >> 1) + r8) * 1920 + ((q & 1) ? col1 : col0)) * 2, ldsb + L_A + 16384 + q * 1024);
    }
  }
  STAMP(22);
  unsigned dxa[3], dwa[3];
#pragma unroll
  for (int i = 0; i < 3; ++i) {
    const int L = 160 * w + 64 * i + lane, c = L >> 3, tt = 8 * (m & 1) + (L & 7);
    dxa[i] = OFF_XN + ((8 * P + (m >> 1)) * 20 + (c >> 2)) * 1024 + ((c & 3) * 16 + tt) * 16;
    const int q = w + 4 * i, pos = 64 * (q & 1) + lane, nb = pos >> 4, n = pos & 15;
    const int rg = 5 * P + (q >> 1), ks = rg >> 2, gg = rg & 3;
    dwa[i] = OFF_WQKV + m * WQKV_STR + (nb < 7 ? (nb * 20 + ks) * 1024 + (gg * 16 + n) * 16 : 143360 + ks * 512 + (gg * 8 + n) * 16);
  }
  const int gw = cuid * 4 + w;
  const int it0k = gw < 800 ? 0 : (gw < 912 ? 1 : 2), it0 = gw < 800 ? gw : (gw < 912 ? 2048 + gw - 800 : 2048 + gw - 912);
  unsigned srcb[5], dstb[5];
  {
    const int rp = r8 < 4 ? r8 : 12 + r8;
    const unsigned v1 = (unsigned)(rp * 1728 + 8 * ch8) * 2, v2 = (unsigned)(rp * 640 + 8 * ch8) * 2;
    auto src_w1 = [&](int u) { const int rg = u / 27, cg = u - 27 * rg; return v1 + (unsigned)((32 * (rg >> 2) + 4 * (rg & 3)) * 1728 + 64 * cg) * 2; };
    auto src_w2 = [&](int u) { const int rg = u / 10, cg = u - 10 * rg; return v2 + (unsigned)((32 * (rg >> 2) + 4 * (rg & 3)) * 640 + 64 * cg) * 2; };
    auto src_wo = [&](int u) {
      const int rg = u / 10, cg = u - 10 * rg, ks = rg >> 2, gg = rg & 3;
      const int idx = 8 * (ks % 5) + r8, row = 160 * (ks / 5) + 20 * (idx / 5) + 5 * gg + idx % 5;
      return (unsigned)(row * 640 + 64 * cg + 8 * ch8) * 2;
    };
    auto dst_w1 = [&](int u) {
      const int rg = u / 27, cg = u - 27 * rg, ks = rg >> 2, gg = rg & 3, n = 64 * cg + lane;
      const int nb = n >> 4, h2 = nb >= 54 ? 1 : 0, nbp = nb - 54 * h2;
      const int tt = nbp < 42 ? nbp / 7 : 6 + (nbp - 42) / 6, nbl = nbp - (tt < 6 ? 7 * tt : 42 + 6 * (tt - 6));
      return OFF_W1 + (8 * h2 + tt) * W1_STR + (ks * 7 + nbl) * 1024 + (gg * 16 + (n & 15)) * 16;
    };
    auto dst_5 = [&](int u, unsigned base, unsigned str) {
      const int rg = u / 10, cg = u - 10 * rg, ks = rg >> 2, gg = rg & 3, n = 64 * cg + lane;
      const int c = n / 80, nl = n - 80 * c;
      return base + c * str + (ks * 5 + (nl >> 4)) * 1024 + (gg * 16 + (nl & 15)) * 16;
    };
    srcb[0] = it0k == 0 ? src_wo(it0) : (it0k == 1 ? src_w1(it0) : src_w2(it0));
    dstb[0] = it0k == 1 ? dst_w1(it0) : dst_5(it0, it0k == 0 ? OFF_WO : OFF_W2, it0k == 0 ? 102400u : W2_STR);
    srcb[1] = src_w1(gw); srcb[2] = src_w1(gw + 1024); srcb[3] = src_w2(gw); srcb[4] = src_w2(gw + 1024);
    dstb[1] = dst_w1(gw); dstb[2] = dst_w1(gw + 1024); dstb[3] = dst_5(gw, OFF_W2, W2_STR); dstb[4] = dst_5(gw + 1024, OFF_W2, W2_STR);
  }
  STAMP(23);
  asm volatile("s_waitcnt vmcnt(2)" : "+v"(xv[0]), "+v"(xv[1]), "+v"(xv[2]), "+v"(xv[3]), "+v"(xv[4]) :: "memory");
  {
    float f[20];
#pragma unroll
    for (int i = 0; i < 5; ++i) { f[4 * i] = blo(xv[i].x); f[4 * i + 1] = bhi(xv[i].x); f[4 * i + 2] = blo(xv[i].y); f[4 * i + 3] = bhi(xv[i].y); }
    float s4[4];
#pragma unroll
    for (int j = 0; j < 4; ++j) s4[j] = (f[j] + f[4 + j]) + (f[8 + j] + f[12 + j]) + f[16 + j];
    const float mean = sum_x16(sum16((s4[0] + s4[1]) + (s4[2] + s4[3]))) * (1.f / 640.f);
#pragma unroll
    for (int i = 0; i < 20; ++i) f[i] -= mean;
#pragma unroll
    for (int j = 0; j < 4; ++j) s4[j] = (f[j] * f[j] + f[4 + j] * f[4 + j]) + (f[8 + j] * f[8 + j] + f[12 + j] * f[12 + j]) + f[16 + j] * f[16 + j];
    const float rstd = __builtin_amdgcn_rsqf(sum_x16(sum16((s4[0] + s4[1]) + (s4[2] + s4[3]))) * (1.f / 640.f) + 1e-5f);
    LAS char* sc = lds + L_A + ((lane & 31) >> 1) * 128 + (2 * w + (lane >> 5)) * 16 + (lane & 1) * 8;
#pragma unroll
    for (int i = 0; i < 5; ++i)
      *(LAS v2u*)(sc + i * 2048) = v2u{pk2(f[4 * i] * rstd, f[4 * i + 1] * rstd), pk2(f[4 * i + 2] * rstd, f[4 * i + 3] * rstd)};
  }
  STAMP(24);
  DRAIN();
  BAR();
  STAMP(25);
#pragma unroll
  for (int i = 0; i < 3; ++i)
    if (i < 2 || lane < 32) st128(*(LAS v4u*)(lds + L_A + (160 * w + 64 * i + lane) * 16), ws, dxa[i], 0);
#pragma unroll
  for (int k = 0; k < 3; ++k) {
    const int q = w + 4 * k;
    if (q < 10) {
      const v4u o = item_read(lds + L_A + 16384 + q * 1024, lane);
      if (64 * (q & 1) + lane < 120) st128(o, ws, dwa[k], 0);
    }
  }
  {
    const rsrc_t ro = __builtin_amdgcn_make_buffer_rsrc((void*)wo, 0, 640 * 640 * 2, 0x00020000);
    const rsrc_t r1 = __builtin_amdgcn_make_buffer_rsrc((void*)w1, 0, 640 * 1728 * 2, 0x00020000);
    const rsrc_t r2 = __builtin_amdgcn_make_buffer_rsrc((void*)w2, 0, 1728 * 640 * 2, 0x00020000);
    const unsigned sb = ldsb + L_B + w * 5120;
    if (it0k == 0) dma_item(ro, srcb[0], sb); else if (it0k == 1) dma_item(r1, srcb[0], sb); else dma_item(r2, srcb[0], sb);
    dma_item(r1, srcb[1], sb + 1024); dma_item(r1, srcb[2], sb + 2048); dma_item(r2, srcb[3], sb + 3072); dma_item(r2, srcb[4], sb + 4096);
  }
  const unsigned tag = __builtin_amdgcn_readfirstlane(epoch) * 2654435761u + 0x9E3779B9u;
  STAMP(26);
  asm volatile("s_waitcnt vmcnt(5)" ::: "memory");
  BAR();
  if (tid == 0) __hip_atomic_store(flg + (0 * 256 + cuid) * 32, tag, RLX);
  STAMP(1);
  *(LAS v4u*)(lds + L_IMG + tid * 16) = v4u{0u, 0u, 0u, 0u};
  DRAIN();
#pragma unroll
  for (int k = 0; k < 5; ++k) st128(item_read(lds + L_B + w * 5120 + k * 1024, lane), ws, dstb[k], 0);
  STAMP(2);

  if (w == 0) {
    const int l = lane & 15;
    wait_flag(flg + (0 * 256 + (lane < 16 ? P * 16 + l : l * 16 + m)) * 32, tag, lane < 32);
  }
  BAR();
  STAMP(3);
  v4u fb;
  v4u xn[2][20];
  f32x4 akv[2][5];
  v4u wqs[13];
  {
    constexpr int D1 = 5;
    for (int q = w; q < 25; q += 4) dma4(ws, l16, OFF_WQKV + m * WQKV_STR + q * 4096, ldsb + L_A + q * 4096);
    const unsigned xb = OFF_XN + (8 * P + 2 * w) * 20 * 1024;
#pragma unroll
    for (int ks = 0; ks < D1; ++ks) {
#pragma unroll
      for (int u = 0; u < 2; ++u) xn[u][ks] = LDF(xb, u * 20 + ks);
      SCHED();
    }
    asm volatile("s_waitcnt vmcnt(10)" ::: "memory");
    BAR();
    if (tid == 0) __hip_atomic_store((gu32*)(wsp + OFF_FLAGB) + cuid, tag, RLX);
    STAMP(17);
#pragma unroll
    for (int u = 0; u < 2; ++u)
#pragma unroll
      for (int n = 0; n < 5; ++n) akv[u][n] = f32x4{0.f, 0.f, 0.f, 0.f};
    v4u wf[2][5];
#pragma unroll
    for (int n = 0; n < 5; ++n) wf[0][n] = LDSF(L_A + (n * 20) * 1024);
    SCHED();
    const unsigned wqb = OFF_WQKV + m * WQKV_STR + 102400 + w * 1024;
#pragma unroll
    for (int ks = 0; ks < 20; ++ks) {
      if (ks + D1 < 20) {
#pragma unroll
        for (int u = 0; u < 2; ++u) xn[u][ks + D1] = LDF(xb, u * 20 + ks + D1);
      } else {
#pragma unroll
        for (int i = (ks + D1 - 20) * 3; i < (ks + D1 - 20) * 3 + 3 && i < 13; ++i) wqs[i] = ld128(ws, l16, wqb + (i < 12 || w < 2 ? i : 11) * 4096);
        if (ks == 19) fb = __builtin_amdgcn_raw_buffer_load_b128(ws, OFF_FLAGB + l16, 0, AUX_SC1);
      }
      if (ks + 1 < 20) {
#pragma unroll
        for (int n = 0; n < 5; ++n) wf[(ks + 1) & 1][n] = LDSF(L_A + (n * 20 + ks + 1) * 1024);
      }
#pragma unroll
      for (int n = 0; n < 5; ++n)
#pragma unroll
        for (int u = 0; u < 2; ++u) akv[u][n] = mfma(xn[u][ks], wf[ks & 1][n], akv[u][n]);
#pragma unroll
      for (int n = 0; n < 5; ++n) { SGB(0x100, 1); SGB(0x8, 1); if (n < 3) SGB(0x20, 1); SGB(0x8, 1); }
      SCHED();
    }
  }
  STAMP(4);
  BAR();
#pragma unroll
  for (int u = 0; u < 2; ++u)
#pragma unroll
    for (int n = 0; n < 5; ++n) {
      f32x4 a = akv[u][n];
      if (n < 2) { a[0] = phi(a[0]); a[1] = phi(a[1]); a[2] = phi(a[2]); a[3] = phi(a[3]); }
      else if (n == 2) { if (t16 < 8) { a[0] = phi(a[0]); a[1] = phi(a[1]); a[2] = phi(a[2]); a[3] = phi(a[3]); } }
      *(LAS v2u*)(lds + L_A + (16 * n + t16) * EP + (32 * w + 16 * u + 4 * g) * 2) = v2u{pk2(a[0], a[1]), pk2(a[2], a[3])};
    }
  if (tid < 16) *(LAS v4u*)(lds + L_A + 80 * EP + tid * 16) = v4u{0x3F803F80u, 0x3F803F80u, 0x3F803F80u, 0x3F803F80u};
  BAR();
  STAMP(27);
  {
    const int hd = w >> 1, dt = w & 1;
    f32x4 t0 = {0.f, 0.f, 0.f, 0.f}, t1 = t0;
    const unsigned ra = L_A + (20 * hd + 16 * dt + t16) * EP + g * 16;
    const unsigned rb0 = L_A + (40 + 20 * hd + t16) * EP + g * 16;
    const unsigned rb1 = L_A + (t16 < 4 ? 56 + 20 * hd + t16 : 80) * EP + g * 16;
    v4u fa[4], f0[4], f1[4];
#pragma unroll
    for (int s = 0; s < 4; ++s) { fa[s] = *(LAS v4u*)(lds + ra + s * 64); f0[s] = *(LAS v4u*)(lds + rb0 + s * 64); f1[s] = *(LAS v4u*)(lds + rb1 + s * 64); }
#pragma unroll
    for (int s = 0; s < 4; ++s) { t0 = mfma(fa[s], f0[s], t0); t1 = mfma(fa[s], f1[s], t1); }
    const unsigned pb = OFF_PART + (m * 16 + P) * PART_STR + hd * 1680;
    if (dt == 0) {
      st128(__builtin_bit_cast(v4u, t0), ws, l16, pb);
      if (t16 < 5) st128(__builtin_bit_cast(v4u, t1), ws, 1024 + (g * 5 + t16) * 16, pb);
    } else if (g == 0) {
      st128(__builtin_bit_cast(v4u, t0), ws, 1344 + t16 * 16, pb);
      if (t16 < 5) st128(__builtin_bit_cast(v4u, t1), ws, 1600 + t16 * 16, pb);
    }
  }
  STAMP(28);
  DRAIN();
  BAR();
  if (tid == 0) __hip_atomic_store(flg + (1 * 256 + cuid) * 32, tag, RLX);
#pragma unroll
  for (int i = 0; i < 13; ++i)
    if (i < 12 || w < 2) *(LAS v4u*)(lds + L_B + (w + 4 * i) * 1024 + l16) = wqs[i];
  if (!__all(fb.x == tag && fb.y == tag && fb.z == tag && fb.w == tag)) {
    unsigned sp = 0;
    for (;;) {
      asm volatile("" ::: "memory");
      fb = __builtin_amdgcn_raw_buffer_load_b128(ws, OFF_FLAGB + l16, 0, AUX_SC1);
      if (__all(fb.x == tag && fb.y == tag && fb.z == tag && fb.w == tag)) break;
      if (++sp > SPIN_CAP) break;
    }
  }
  BAR();
  STAMP(5);

  unsigned qp[2][3][2];
  {
    f32x4 aq[2][3];
#pragma unroll
    for (int u = 0; u < 2; ++u)
#pragma unroll
      for (int n = 0; n < 3; ++n) aq[u][n] = f32x4{0.f, 0.f, 0.f, 0.f};
    LAS char* lq7 = lds + L_B + 40960 + g * 128 + (t16 & 7) * 16;
    v4u wf[3][3];
#pragma unroll
    for (int k = 0; k < 2; ++k) { wf[k][0] = LDSF(L_B + k * 1024); wf[k][1] = LDSF(L_B + (20 + k) * 1024); wf[k][2] = *(LAS v4u*)(lq7 + k * 512); }
    SCHED();
#pragma unroll
    for (int ks = 0; ks < 20; ++ks) {
      if (ks % 3 == 0) {
        const int q = w + 4 * (ks / 3);
        if (q < 25) dma4(ws, l16, OFF_WO + ct * 102400 + q * 4096, ldsb + L_A + q * 4096);
      }
      if (ks + 2 < 20) {
        wf[(ks + 2) % 3][0] = LDSF(L_B + (ks + 2) * 1024);
        wf[(ks + 2) % 3][1] = LDSF(L_B + (20 + ks + 2) * 1024);
        wf[(ks + 2) % 3][2] = *(LAS v4u*)(lq7 + (ks + 2) * 512);
      }
#pragma unroll
      for (int n = 0; n < 3; ++n)
#pragma unroll
        for (int u = 0; u < 2; ++u) aq[u][n] = mfma(wf[ks % 3][n], xn[u][ks], aq[u][n]);
#pragma unroll
      for (int n = 0; n < 3; ++n) { SGB(0x100, 1); SGB(0x8, 2); }
      SCHED();
    }
#pragma unroll
    for (int u = 0; u < 2; ++u)
#pragma unroll
      for (int n = 0; n < 3; ++n) {
        qp[u][n][0] = pk2(phi(aq[u][n][0]), phi(aq[u][n][1]));
        qp[u][n][1] = pk2(phi(aq[u][n][2]), phi(aq[u][n][3]));
      }
  }
  v4u w2f[14][5];
  const int k4s = w < 2 ? 14 * w : 28 + 13 * (w - 2);
  const unsigned w2b = OFF_W2 + ct * W2_STR + k4s * 5 * 1024;
#pragma unroll
  for (int k = 0; k < 2; ++k) {
#pragma unroll
    for (int n = 0; n < 5; ++n) w2f[k][n] = LDF(w2b, k * 5 + n);
    SCHED();
  }
  unsigned imgoff, imgoffz;
  {
    const int hd = tid >= 105 ? 1 : 0, u = tid - 105 * hd;
    int dq, e;
    if (u < 64) { dq = u >> 4; e = u & 15; }
    else if (u < 84) { const int v = u - 64; dq = v / 5; e = 16 + v - 5 * dq; }
    else if (u < 100) { dq = 4; e = u - 84; }
    else { dq = 4; e = 16 + u - 100; }
    int gq;
    if (hd == 0) gq = dq < 4 ? dq : 0; else gq = dq < 3 ? dq + 1 : (dq == 3 ? 0 : 1);
    const int j0 = dq < 4 ? 0 : 4, ge = e / 5, re = e - 5 * ge;
    const unsigned ib = L_IMG + hd * 2048 + gq * 256 + j0 * 2;
    imgoff = ib + (re < 4 ? (4 * ge + re) * 16 : 1024 + (4 * ge) * 16);
    imgoffz = e == 20 ? ib + 1024 + 16 : 0u;
  }
  STAMP(6);

  if (w == 0) wait_flag(flg + (1 * 256 + (lane & 15) * 16 + m) * 32, tag, lane < 16);
  BAR();
  STAMP(7);
  if (tid < 210) {
    v4u pv[16];
#pragma unroll
    for (int i = 0; i < 16; ++i) pv[i] = ld128(ws, tid * 16, OFF_PART + (m * 16 + i) * PART_STR);
    f32x4 s[4];
#pragma unroll
    for (int i = 0; i < 4; ++i)
      s[i] = (__builtin_bit_cast(f32x4, pv[4 * i]) + __builtin_bit_cast(f32x4, pv[4 * i + 1])) +
             (__builtin_bit_cast(f32x4, pv[4 * i + 2]) + __builtin_bit_cast(f32x4, pv[4 * i + 3]));
    const f32x4 st = (s[0] + s[1]) + (s[2] + s[3]);
    const v2u val = v2u{pk2(st[0], st[1]), pk2(st[2], st[3])};
    if (imgoffz == 0u) *(LAS v2u*)(lds + imgoff) = val;
    else {
#pragma unroll
      for (int ge = 0; ge < 4; ++ge) *(LAS v2u*)(lds + imgoffz + ge * 64) = val;
    }
  }
  BAR();
  STAMP(18);
  {
    v4u ka[2][2];
#pragma unroll
    for (int hd = 0; hd < 2; ++hd)
#pragma unroll
      for (int bb = 0; bb < 2; ++bb) ka[hd][bb] = *(LAS v4u*)(lds + L_IMG + hd * 2048 + bb * 1024 + l16);
    const int i4 = m & 3;
#pragma unroll
    for (int u = 0; u < 2; ++u) {
      v4u qa, qb;
      qa.x = qp[u][0][0]; qa.y = qp[u][0][1]; qa.z = g == 0 ? qp[u][1][0] : 0u; qa.w = g == 0 ? qp[u][1][1] : 0u;
      qb.x = g == 0 ? qp[u][2][0] : qp[u][1][0]; qb.y = g == 0 ? qp[u][2][1] : qp[u][1][1];
      qb.z = g == 1 ? qp[u][2][0] : 0u; qb.w = g == 1 ? qp[u][2][1] : 0u;
      const f32x4 z4 = {0.f, 0.f, 0.f, 0.f};
      const f32x4 a0 = mfma(ka[0][0], qa, z4), a1 = mfma(ka[0][1], qa, z4);
      const f32x4 b0 = mfma(ka[1][0], qb, z4), b1 = mfma(ka[1][1], qb, z4);
      const float ra = __builtin_amdgcn_rcpf(a1[1] + 1e-6f), rb = __builtin_amdgcn_rcpf(b1[1] + 1e-6f);
      const unsigned d0 = pk2(a0[0] * ra, a0[1] * ra), d1 = pk2(a0[2] * ra, a0[3] * ra), d2 = pk2(a1[0] * ra, b0[0] * rb),
                     d3 = pk2(b0[1] * rb, b0[2] * rb), d4 = pk2(b0[3] * rb, b1[0] * rb);
      const unsigned ob = OFF_O + ((8 * P + 2 * w + u) * 20 + 5 * (m >> 2)) * 1024;
      if (i4 == 0) {
        st128(v4u{d0, d1, d2, d3}, ws, l16, ob);
        __builtin_amdgcn_raw_buffer_store_b32(d4, ws, l16 + 1024, ob, AUX_SC1);
      } else if (i4 == 1) {
        __builtin_amdgcn_raw_buffer_store_b96(v3u{d0, d1, d2}, ws, l16 + 1024 + 4, ob, AUX_SC1);
        st64(v2u{d3, d4}, ws, l16 + 2048, ob);
      } else if (i4 == 2) {
        st64(v2u{d0, d1}, ws, l16 + 2048 + 8, ob);
        __builtin_amdgcn_raw_buffer_store_b96(v3u{d2, d3, d4}, ws, l16 + 3072, ob, AUX_SC1);
      } else {
        __builtin_amdgcn_raw_buffer_store_b32(d0, ws, l16 + 3072 + 12, ob, AUX_SC1);
        st128(v4u{d1, d2, d3, d4}, ws, l16 + 4096, ob);
      }
    }
  }
  DRAIN();
  BAR();
  if (tid == 0) __hip_atomic_store(flg + (2 * 256 + cuid) * 32, tag, RLX);
  STAMP(8);
  for (int q = w; q < 12; q += 4) dma4(ws, l16, OFF_W1 + m * W1_STR + q * 4096, ldsb + L_B + q * 4096);
  if (w == 0) dma1(ws, l16, OFF_W1 + m * W1_STR + 49152, ldsb + L_B + 49152);
  v2u xr[5];
  {
    const GAS char* xp = (const GAS char*)x + (size_t)(64 * (2 * P + jl) + 16 * w + t16) * 1280 + (80 * ct + 4 * g) * 2;
#pragma unroll
    for (int n = 0; n < 5; ++n) xr[n] = *(const GAS v2u*)(xp + n * 32);
  }
#pragma unroll
  for (int k = 2; k < 4; ++k) {
#pragma unroll
    for (int n = 0; n < 5; ++n) w2f[k][n] = LDF(w2b, k * 5 + n);
    SCHED();
  }

  STAMP(29);
  if (w == 0) wait_flag(flg + (2 * 256 + P * 16 + (lane & 15)) * 32, tag, lane < 16);
  STAMP(30);
  DRAIN();
  BAR();
  STAMP(9);
  unsigned hp[5][2];
  {
    constexpr int D2 = 8;
    v4u of[20];
    const unsigned ob = OFF_O + (8 * P + 4 * jl + w) * 20 * 1024;
#pragma unroll
    for (int ks = 0; ks < D2; ++ks) { of[ks] = LDF(ob, ks); SCHED(); }
    f32x4 ah[5];
#pragma unroll
    for (int n = 0; n < 5; ++n) ah[n] = f32x4{0.f, 0.f, 0.f, 0.f};
    v4u wf[3][5];
#pragma unroll
    for (int n = 0; n < 5; ++n) { wf[0][n] = LDSF(L_A + n * 1024); wf[1][n] = LDSF(L_A + (5 + n) * 1024); }
    SCHED();
#pragma unroll
    for (int ks = 0; ks < 20; ++ks) {
      if (ks + D2 < 20) of[ks + D2] = LDF(ob, ks + D2);
      if (ks + 2 < 20) {
#pragma unroll
        for (int n = 0; n < 5; ++n) wf[(ks + 2) % 3][n] = LDSF(L_A + ((ks + 2) * 5 + n) * 1024);
      }
#pragma unroll
      for (int n = 0; n < 5; ++n) ah[n] = mfma(wf[ks % 3][n], of[ks], ah[n]);
#pragma unroll
      for (int n = 0; n < 5; ++n) { SGB(0x100, 1); SGB(0x8, 1); if (n == 0) SGB(0x20, 1); }
      SCHED();
    }
    STAMP(19);
    float hv[20];
#pragma unroll
    for (int n = 0; n < 5; ++n) {
      hp[n][0] = pk2(blo(xr[n].x) + ah[n][0], bhi(xr[n].x) + ah[n][1]);
      hp[n][1] = pk2(blo(xr[n].y) + ah[n][2], bhi(xr[n].y) + ah[n][3]);
    }
    const unsigned hb = OFF_H + ((8 * P + 4 * jl + w) * 20 + ((5 * ct) >> 1)) * 1024;
    if ((ct & 1) == 0) {
      st128(v4u{hp[0][0], hp[0][1], hp[1][0], hp[1][1]}, ws, l16, hb);
      st128(v4u{hp[2][0], hp[2][1], hp[3][0], hp[3][1]}, ws, l16 + 1024, hb);
      st64(v2u{hp[4][0], hp[4][1]}, ws, l16 + 2048, hb);
    } else {
      st64(v2u{hp[0][0], hp[0][1]}, ws, l16 + 8, hb);
      st128(v4u{hp[1][0], hp[1][1], hp[2][0], hp[2][1]}, ws, l16 + 1024, hb);
      st128(v4u{hp[3][0], hp[3][1], hp[4][0], hp[4][1]}, ws, l16 + 2048, hb);
    }
    float s4[5];
#pragma unroll
    for (int n = 0; n < 5; ++n) {
      hv[4 * n] = blo(hp[n][0]); hv[4 * n + 1] = bhi(hp[n][0]); hv[4 * n + 2] = blo(hp[n][1]); hv[4 * n + 3] = bhi(hp[n][1]);
      s4[n] = (hv[4 * n] + hv[4 * n + 1]) + (hv[4 * n + 2] + hv[4 * n + 3]);
    }
    const float mi = sum_g((s4[0] + s4[1]) + (s4[2] + s4[3]) + s4[4]) * (1.f / 80.f);
#pragma unroll
    for (int n = 0; n < 5; ++n) {
      const float a = hv[4 * n] - mi, bq = hv[4 * n + 1] - mi, c = hv[4 * n + 2] - mi, d = hv[4 * n + 3] - mi;
      s4[n] = (a * a + bq * bq) + (c * c + d * d);
    }
    const float q = sum_g((s4[0] + s4[1]) + (s4[2] + s4[3]) + s4[4]);
    if (g == 0) st64(v2u{asu(mi), asu(q)}, ws, (64 * (2 * P + jl) + 16 * w + t16) * 64 + ct * 8, OFF_ST);
  }
  DRAIN();
  BAR();
  if (tid == 0) __hip_atomic_store(flg + (3 * 256 + cuid) * 32, tag, RLX);
  STAMP(10);
  for (int q = w; q < 22; q += 4) dma4(ws, l16, OFF_W1 + m * W1_STR + 50176 + q * 4096, ldsb + L_A + q * 4096);
  if (w < 3) dma1(ws, l16, OFF_W1 + m * W1_STR + 50176 + 90112 + w * 1024, ldsb + L_A + 90112 + w * 1024);
  {
    f32x4 cs[7];
#pragma unroll
    for (int n = 0; n < 7; ++n) cs[n] = f32x4{0.f, 0.f, 0.f, 0.f};
    const v4u ones = {0x3F803F80u, 0x3F803F80u, 0x3F803F80u, 0x3F803F80u};
    {
      v4u wf[7];
#pragma unroll
      for (int n = 0; n < 7; ++n) wf[n] = *(LAS v4u*)(lb0 + L_B + w * 7168 + n * 1024);
#pragma unroll
      for (int n = 0; n < 7; ++n) cs[n] = mfma(wf[n], ones, cs[n]);
      if (w < 3) {
#pragma unroll
        for (int n = 0; n < 7; ++n) wf[n] = *(LAS v4u*)(lb0 + L_B + (w + 4) * 7168 + n * 1024);
#pragma unroll
        for (int n = 0; n < 7; ++n) cs[n] = mfma(wf[n], ones, cs[n]);
      }
    }
    DRAIN();
    BAR();
#pragma unroll
    for (int i = 0; i < 4; ++i) {
      const int j = ((w + 1) & 3) + 4 * i;
      if (j < 13) {
        v4u wf[7];
#pragma unroll
        for (int n = 0; n < 7; ++n) wf[n] = *(LAS v4u*)(lb0 + L_A + j * 7168 + n * 1024);
#pragma unroll
        for (int n = 0; n < 7; ++n) cs[n] = mfma(wf[n], ones, cs[n]);
      }
    }
    if (t16 == 0) {
#pragma unroll
      for (int n = 0; n < 7; ++n) *(LAS f32x4*)(lds + L_CS + (w * 7 + n) * 64 + g * 16) = cs[n];
    }
  }
  STAMP(20);

  if (w == 0) wait_flag(flg + (3 * 256 + P * 16 + (lane & 15)) * 32, tag, lane < 16);
  BAR();
  STAMP(11);
  {
    constexpr int D3 = 5;
    v4u hf[2][20];
    const unsigned hb = OFF_H + (8 * P + 2 * w) * 20 * 1024;
    v4u stv[2];
#pragma unroll
    for (int ks = 0; ks < D3; ++ks) {
#pragma unroll
      for (int u = 0; u < 2; ++u) hf[u][ks] = LDF(hb, u * 20 + ks);
      SCHED();
    }
#pragma unroll
    for (int u = 0; u < 2; ++u) stv[u] = ld128(ws, (128 * P + 32 * w + 16 * u + t16) * 64 + g * 16, OFF_ST);
    f32x4 a3[2][7];
#pragma unroll
    for (int u = 0; u < 2; ++u)
#pragma unroll
      for (int n = 0; n < 7; ++n) a3[u][n] = f32x4{0.f, 0.f, 0.f, 0.f};
    v4u wf[2][7];
#pragma unroll
    for (int n = 0; n < 7; ++n) wf[0][n] = LDSF(W1A(0, n));
    SCHED();
#pragma unroll
    for (int ks = 0; ks < 20; ++ks) {
      if (ks + D3 < 20) {
#pragma unroll
        for (int u = 0; u < 2; ++u) hf[u][ks + D3] = LDF(hb, u * 20 + ks + D3);
      }
      w2f[4 + ks / 5][ks % 5] = LDF(w2b, 20 + ks);
      if (ks >= 15) w2f[8][ks - 15] = LDF(w2b, 40 + ks - 15);
      if (ks + 1 < 20) {
#pragma unroll
        for (int n = 0; n < 7; ++n) wf[(ks + 1) & 1][n] = LDSF(W1A(ks + 1, n));
      }
#pragma unroll
      for (int n = 0; n < 7; ++n)
#pragma unroll
        for (int u = 0; u < 2; ++u) a3[u][n] = mfma(wf[ks & 1][n], hf[u][ks], a3[u][n]);
#pragma unroll
      for (int n = 0; n < 7; ++n) { SGB(0x100, 1); SGB(0x8, 1); if (n < 3) SGB(0x20, 1); SGB(0x8, 1); }
      SCHED();
    }
    STAMP(12);
#pragma unroll
    for (int k = 9; k < 11; ++k) {
#pragma unroll
      for (int n = 0; n < 5; ++n) w2f[k][n] = LDF(w2b, k * 5 + n);
    }
    SCHED();
    float mean[2], rstd[2];
#pragma unroll
    for (int u = 0; u < 2; ++u) {
      const float m0 = asf(stv[u].x), q0 = asf(stv[u].y), m1 = asf(stv[u].z), q1 = asf(stv[u].w);
      const float mu = sum_g(m0 + m1) * 0.125f;
      const float M2 = sum_g(q0 + q1 + 80.f * ((m0 - mu) * (m0 - mu) + (m1 - mu) * (m1 - mu)));
      rstd[u] = __builtin_amdgcn_rsqf(M2 * (1.f / 640.f) + 1e-5f);
      mean[u] = mu * rstd[u];
    }
    const int tt = m & 7;
    const int nb0 = 54 * c2 + (tt < 6 ? 7 * tt : 42 + 6 * (tt - 6));
    const bool seven = tt < 6;
    unsigned hq[2][7][2];
#pragma unroll
    for (int n = 0; n < 7; ++n) {
      f32x4 cw[4];
#pragma unroll
      for (int ww = 0; ww < 4; ++ww) cw[ww] = *(LAS f32x4*)(lds + L_CS + (ww * 7 + n) * 64 + g * 16);
      const f32x4 cs = (cw[0] + cw[1]) + (cw[2] + cw[3]);
#pragma unroll
      for (int u = 0; u < 2; ++u) {
        const float v0 = fmaxf(a3[u][n][0] * rstd[u] - mean[u] * cs[0], 0.f), v1 = fmaxf(a3[u][n][1] * rstd[u] - mean[u] * cs[1], 0.f);
        const float v2 = fmaxf(a3[u][n][2] * rstd[u] - mean[u] * cs[2], 0.f), v3 = fmaxf(a3[u][n][3] * rstd[u] - mean[u] * cs[3], 0.f);
        hq[u][n][0] = pk2(v0, v1); hq[u][n][1] = pk2(v2, v3);
      }
    }
#pragma unroll
    for (int u = 0; u < 2; ++u) {
      const unsigned db = OFF_HID + ((8 * P + 2 * w + u) * 54 + (nb0 >> 1)) * 1024;
      if ((nb0 & 1) == 0) {
        st128(v4u{hq[u][0][0], hq[u][0][1], hq[u][1][0], hq[u][1][1]}, ws, l16, db);
        st128(v4u{hq[u][2][0], hq[u][2][1], hq[u][3][0], hq[u][3][1]}, ws, l16 + 1024, db);
        st128(v4u{hq[u][4][0], hq[u][4][1], hq[u][5][0], hq[u][5][1]}, ws, l16 + 2048, db);
        if (seven) st64(v2u{hq[u][6][0], hq[u][6][1]}, ws, l16 + 3072, db);
      } else {
        st64(v2u{hq[u][0][0], hq[u][0][1]}, ws, l16 + 8, db);
        st128(v4u{hq[u][1][0], hq[u][1][1], hq[u][2][0], hq[u][2][1]}, ws, l16 + 1024, db);
        st128(v4u{hq[u][3][0], hq[u][3][1], hq[u][4][0], hq[u][4][1]}, ws, l16 + 2048, db);
        st128(v4u{hq[u][5][0], hq[u][5][1], hq[u][6][0], hq[u][6][1]}, ws, l16 + 3072, db);
      }
    }
  }
  STAMP(21);
  DRAIN();
  BAR();
  if (tid == 0) {
    __hip_atomic_store(flg + (4 * 256 + cuid) * 32, tag, RLX);
    if (cuid == 0) __hip_atomic_store((gu32*)(wsp + OFF_EPOCH), epoch + 1u, RLX);
  }
  STAMP(13);
#pragma unroll
  for (int k = 11; k < 14; ++k) {
#pragma unroll
    for (int n = 0; n < 5; ++n) w2f[k][n] = LDF(w2b, k * 5 + n);
    SCHED();
  }

  if (w == 0) wait_flag(flg + (4 * 256 + P * 16 + (lane & 15)) * 32, tag, lane < 16);
  BAR();
  STAMP(14);
  {
    f32x4 a4[4][5];
#pragma unroll
    for (int u = 0; u < 4; ++u)
#pragma unroll
      for (int n = 0; n < 5; ++n) a4[u][n] = f32x4{0.f, 0.f, 0.f, 0.f};
    const unsigned hb = OFF_HID + ((8 * P + 4 * jl) * 54 + k4s) * 1024;
    constexpr int DP = 3;
    v4u af[DP + 1][4];
#pragma unroll
    for (int k = 0; k < DP; ++k)
#pragma unroll
      for (int u = 0; u < 4; ++u) { af[k][u] = ld128(ws, l16 + (k & 3) * 1024, hb + u * 54 * 1024 + (k >> 2) * 4096); SCHED(); }
#pragma unroll
    for (int k = 0; k < 14; ++k) {
      if (k + DP < 14) {
        if (k + DP < 13 || w < 2) {
#pragma unroll
          for (int u = 0; u < 4; ++u) af[(k + DP) % (DP + 1)][u] = ld128(ws, l16 + ((k + DP) & 3) * 1024, hb + u * 54 * 1024 + ((k + DP) >> 2) * 4096);
        }
      }
      if (k < 13 || w < 2) {
#pragma unroll
        for (int u = 0; u < 4; ++u)
#pragma unroll
          for (int n = 0; n < 5; ++n) a4[u][n] = mfma(w2f[k][n], af[k % (DP + 1)][u], a4[u][n]);
      }
#pragma unroll
      for (int u = 0; u < 4; ++u) { SGB(0x20, 1); SGB(0x8, 5); }
      SCHED();
    }
    STAMP(15);
#pragma unroll
    for (int u = 0; u < 4; ++u) {
      if (u != w) {
        const int si = w < u ? w : w - 1;
#pragma unroll
        for (int n = 0; n < 5; ++n) *(LAS f32x4*)(lds + L_A + ((u * 3 + si) * 5 + n) * 1024 + l16) = a4[u][n];
      }
    }
    BAR();
    GAS char* op = (GAS char*)out + (size_t)(64 * (2 * P + jl) + 16 * w + t16) * 1280 + (80 * ct + 4 * g) * 2;
#pragma unroll
    for (int n = 0; n < 5; ++n) {
      f32x4 v = w == 0 ? a4[0][n] : (w == 1 ? a4[1][n] : (w == 2 ? a4[2][n] : a4[3][n]));
      f32x4 p[3];
#pragma unroll
      for (int si = 0; si < 3; ++si) p[si] = *(LAS f32x4*)(lds + L_A + ((w * 3 + si) * 5 + n) * 1024 + l16);
      v = (v + p[0]) + (p[1] + p[2]);
      const v2u o = {pk2(blo(hp[n][0]) + v[0], bhi(hp[n][0]) + v[1]), pk2(blo(hp[n][1]) + v[2], bhi(hp[n][1]) + v[3])};
      *(GAS v2u*)(op + n * 32) = o;
    }
  }
  STAMP(16);
}

extern "C" void kernel_launch(void* const* d_in, const int* in_sizes, int n_in, void* d_out, int out_size, void* d_ws, size_t ws_size,
                              hipStream_t stream) {
  (void)in_sizes; (void)n_in; (void)out_size; (void)ws_size;
  hipFuncSetAttribute((const void*)fused_block, hipFuncAttributeMaxDynamicSharedMemorySize, LDS_BYTES);
  fused_block<<<256, 256, LDS_BYTES, stream>>>((const unsigned short*)d_in[0], (const unsigned short*)d_in[1], (const unsigned short*)d_in[2],
                                               (const unsigned short*)d_in[4], (const unsigned short*)d_in[5], (unsigned short*)d_out,
                                               (char*)d_ws);
}
```

```cpp
#include <hip/hip_runtime.h>
#include <hip/hip_bf16.h>
#include <stdint.h>

typedef unsigned v4u __attribute__((ext_vector_type(4)));
typedef unsigned v3u __attribute__((ext_vector_type(3)));
typedef unsigned v2u __attribute__((ext_vector_type(2)));
typedef float f32x4 __attribute__((ext_vector_type(4)));
typedef short bf16x8 __attribute__((ext_vector_type(8)));
typedef short s16x4 __attribute__((ext_vector_type(4)));
typedef __bf16 bf2 __attribute__((ext_vector_type(2)));
typedef __amdgpu_buffer_rsrc_t rsrc_t;
#define GAS __attribute__((address_space(1)))
#define LAS __attribute__((address_space(3)))
#define DI __device__ __forceinline__
#define RLX __ATOMIC_RELAXED, __HIP_MEMORY_SCOPE_AGENT
typedef GAS unsigned gu32;

#define TS 0
#define CODEPF 1
#define PRETOUCH 0
constexpr int CODE_LINES = 290;

constexpr unsigned OFF_EPOCH = 0;
constexpr unsigned OFF_FLAG = 4096;
constexpr unsigned OFF_FLAGB = OFF_FLAG + 5 * 256 * 128;
constexpr unsigned OFF_TS = 65 * 1024 * 1024;
constexpr unsigned OFF_TS2 = 64 * 1024 * 1024;
constexpr unsigned OFF_XN = 256 * 1024;
constexpr unsigned WQKV_STR = 153600;
constexpr unsigned OFF_WQKV = OFF_XN + 128 * 20 * 1024;
constexpr unsigned OFF_WO = OFF_WQKV + 16 * WQKV_STR;
constexpr unsigned W1_STR = 20 * 7 * 1024;
constexpr unsigned OFF_W1 = OFF_WO + 8 * 102400;
constexpr unsigned W2_STR = 54 * 5 * 1024;
constexpr unsigned OFF_W2 = OFF_W1 + 16 * W1_STR;
constexpr unsigned PART_STR = 3456;
constexpr unsigned OFF_PART = OFF_W2 + 8 * W2_STR;
constexpr unsigned OFF_O = OFF_PART + 256 * PART_STR;
constexpr unsigned OFF_H = OFF_O + 128 * 20 * 1024;
constexpr unsigned OFF_ST = OFF_H + 128 * 20 * 1024;
constexpr unsigned OFF_HID = OFF_ST + 2048 * 64;
constexpr unsigned WS_BYTES = OFF_HID + 128 * 54 * 1024;

constexpr unsigned L_A = 0;
constexpr unsigned L_B = 102400;
constexpr unsigned L_M = 153600;
constexpr unsigned L_IMG = L_M;
constexpr unsigned L_CS = L_M + 4096;
constexpr unsigned LDS_BYTES = 163840;
constexpr unsigned EP = 272;

constexpr int AUX_SC1 = 16;
constexpr unsigned SPIN_CAP = 1u << 20;

DI f32x4 mfma(v4u a, v4u b, f32x4 c) {
  return __builtin_amdgcn_mfma_f32_16x16x32_bf16(__builtin_bit_cast(bf16x8, a), __builtin_bit_cast(bf16x8, b), c, 0, 0, 0);
}
DI unsigned pk2(float a, float b) { bf2 r = {(__bf16)a, (__bf16)b}; return __builtin_bit_cast(unsigned, r); }
DI float asf(unsigned u) { return __builtin_bit_cast(float, u); }
DI unsigned asu(float f) { return __builtin_bit_cast(unsigned, f); }
DI float blo(unsigned u) { return asf(u << 16); }
DI float bhi(unsigned u) { return asf(u & 0xffff0000u); }
DI float phi(float t) { return (t > 0.f ? t + 1.f : __expf(t)) + 1e-6f; }
template <int C> DI float dpp_add(float v) { return v + asf((unsigned)__builtin_amdgcn_update_dpp(0, (int)asu(v), C, 0xF, 0xF, true)); }
DI float sum16(float v) { v = dpp_add<0xB1>(v); v = dpp_add<0x4E>(v); v = dpp_add<0x141>(v); v = dpp_add<0x140>(v); return v; }
DI float sum_x16(float v) { const unsigned u = asu(v); auto r = __builtin_amdgcn_permlane16_swap(u, u, false, false); return asf(r[0]) + asf(r[1]); }
DI float sum_x32(float v) { const unsigned u = asu(v); auto r = __builtin_amdgcn_permlane32_swap(u, u, false, false); return asf(r[0]) + asf(r[1]); }
DI float sum_g(float v) { return sum_x32(sum_x16(v)); }
#define SCHED() __builtin_amdgcn_sched_barrier(0)
#define SGB(mask, n) __builtin_amdgcn_sched_group_barrier(mask, n, 0)
#define DRAIN() asm volatile("s_waitcnt vmcnt(0)" ::: "memory")
#define BAR() do { asm volatile("s_waitcnt lgkmcnt(0)" ::: "memory"); __builtin_amdgcn_s_barrier(); asm volatile("" ::: "memory"); } while (0)

DI v4u ld128(rsrc_t rs, unsigned voff, unsigned soff) { return __builtin_amdgcn_raw_buffer_load_b128(rs, voff, soff, AUX_SC1); }
DI void st128(v4u v, rsrc_t rs, unsigned voff, unsigned soff) { __builtin_amdgcn_raw_buffer_store_b128(v, rs, voff, soff, AUX_SC1); }
DI void st64(v2u v, rsrc_t rs, unsigned voff, unsigned soff) { __builtin_amdgcn_raw_buffer_store_b64(v, rs, voff, soff, AUX_SC1); }

DI void dma4(rsrc_t rs, unsigned voff, unsigned soff, unsigned m0v) {
  asm volatile("s_nop 4\n\ts_mov_b32 m0, %0\n\ts_nop 0\n\t"
               "buffer_load_dwordx4 %1, %2, %3 offen sc1 lds\n\t"
               "buffer_load_dwordx4 %1, %2, %3 offen offset:1024 sc1 lds\n\t"
               "buffer_load_dwordx4 %1, %2, %3 offen offset:2048 sc1 lds\n\t"
               "buffer_load_dwordx4 %1, %2, %3 offen offset:3072 sc1 lds"
               :: "s"(m0v), "v"(voff), "s"(rs), "s"(soff) : "m0", "memory");
}
DI void dma1(rsrc_t rs, unsigned voff, unsigned soff, unsigned m0v) {
  asm volatile("s_nop 4\n\ts_mov_b32 m0, %0\n\ts_nop 0\n\t"
               "buffer_load_dwordx4 %1, %2, %3 offen sc1 lds"
               :: "s"(m0v), "v"(voff), "s"(rs), "s"(soff) : "m0", "memory");
}
DI void dma_item(rsrc_t rs, unsigned voff, unsigned m0v) {
  asm volatile("s_nop 4\n\ts_mov_b32 m0, %0\n\ts_nop 0\n\t"
               "buffer_load_dwordx4 %1, %2, 0 offen lds"
               :: "s"(m0v), "v"(voff), "s"(rs) : "m0", "memory");
}
DI v4u item_read(LAS char* img, int lane) {
  LAS char* p = img + ((lane >> 2) & 3) * 128 + (lane >> 4) * 32 + (lane & 3) * 8;
  const s16x4 a = __builtin_amdgcn_ds_read_tr16_b64_v4i16((LAS s16x4*)p);
  const s16x4 b = __builtin_amdgcn_ds_read_tr16_b64_v4i16((LAS s16x4*)(p + 512));
  const v2u x = __builtin_bit_cast(v2u, a), y = __builtin_bit_cast(v2u, b);
  return v4u{x.x, x.y, y.x, y.y};
}
#define LDF(sbase, i) ld128(ws, l16 + ((i) & 3) * 1024, (sbase) + ((i) >> 2) * 4096)
#define LDSF(off) (*(LAS v4u*)(((off) < 65536 ? lb0 : ((off) < 131072 ? lb1 : lb2)) + ((off) & 65535)))
#define W1A(ks, n) (((ks) < 7 ? L_B + (ks) * 7168 : L_A + ((ks) - 7) * 7168) + (n) * 1024)

DI void wait_flag(gu32* f, unsigned tag, bool active) {
  unsigned sp = 0;
  for (;;) {
    unsigned v = active ? __hip_atomic_load(f, RLX) : tag;
    if (__all(v == tag)) break;
    if (++sp > SPIN_CAP) break;
  }
  __builtin_amdgcn_fence(__ATOMIC_ACQUIRE, "wavefront");
}

#define STAMP(k) do { } while (0)

__global__ __launch_bounds__(256, 1) void fused_block(const unsigned short* __restrict__ x, const unsigned short* __restrict__ wqkv,
                                                      const unsigned short* __restrict__ wo, const unsigned short* __restrict__ w1,
                                                      const unsigned short* __restrict__ w2, unsigned short* __restrict__ out,
                                                      char* __restrict__ wsp) {
  extern __shared__ __attribute__((aligned(16))) char smem[];
  LAS char* lds = (LAS char*)smem;
  const unsigned ldsb = (unsigned)(size_t)lds;
  const int tid = threadIdx.x, lane = tid & 63;
  const int w = __builtin_amdgcn_readfirstlane(tid >> 6);
  const int g = lane >> 4, t16 = lane & 15;
  const int b = blockIdx.x, xcd = b & 7, sl = b >> 3;
  const int c2 = xcd & 1, P = 4 * (xcd >> 1) + (sl >> 3), m = 8 * c2 + (sl & 7);
  const int cuid = P * 16 + m;
  const int jl = (m & 7) >> 2, ct = 4 * c2 + (m & 3);
  const unsigned l16 = lane * 16;
  const rsrc_t ws = __builtin_amdgcn_make_buffer_rsrc(wsp, 0, WS_BYTES, 0x00020000);
  gu32* flg = (gu32*)(wsp + OFF_FLAG);
  STAMP(0);
  unsigned warm0 = 0, warm1 = 0;
  if (w == 3) {
    unsigned long long pc;
    asm volatile("s_getpc_b64 %0" : "=s"(pc));
    const GAS char* cp = (const GAS char*)((pc & ~127ull) + (unsigned long long)(sl * 10 + (lane < 10 ? lane : 0)) * 128ull);
    if (sl * 10 + lane < CODE_LINES && lane < 10) asm volatile("global_load_dword %0, %1, off" : "=v"(warm0) : "v"(cp) : "memory");
  }
  const unsigned epoch = __hip_atomic_load((gu32*)(wsp + OFF_EPOCH), RLX);
  LAS char* lb0 = lds + l16; LAS char* lb1 = lb0 + 65536; LAS char* lb2 = lb0 + 131072;
  asm volatile("" : "+v"(lb1), "+v"(lb2));

  v2u xv[5];
  {
    const GAS char* xp = (const GAS char*)(x + (size_t)(128 * P + 8 * m + 2 * w + (lane >> 5)) * 640) + (lane & 31) * 8;
    asm volatile("global_load_dwordx2 %0, %5, off\n\tglobal_load_dwordx2 %1, %5, off offset:256\n\tglobal_load_dwordx2 %2, %5, off offset:512\n\t"
                 "global_load_dwordx2 %3, %5, off offset:768\n\tglobal_load_dwordx2 %4, %5, off offset:1024"
                 : "=&v"(xv[0]), "=&v"(xv[1]), "=&v"(xv[2]), "=&v"(xv[3]), "=&v"(xv[4]) : "v"(xp) : "memory");
  }
  const int r8 = lane >> 3, ch8 = lane & 7;
  {
    const rsrc_t rq = __builtin_amdgcn_make_buffer_rsrc((void*)wqkv, 0, 640 * 1920 * 2, 0x00020000);
    const int col0 = ch8 < 5 ? 640 + 40 * m + 8 * ch8 : 1280 + 40 * m + 8 * (ch8 - 5);
    const int col1 = ch8 < 2 ? 1280 + 40 * m + 24 + 8 * ch8 : 40 * m + 8 * ((ch8 < 7 ? ch8 : 6) - 2);
#pragma unroll
    for (int k = 0; k < 3; ++k) {
      const int q = w + 4 * k;
      if (q < 10) dma_item(rq, (unsigned)((40 * P + 8 * (q >> 1) + r8) * 1920 + ((q & 1) ? col1 : col0)) * 2, ldsb + L_A + 16384 + q * 1024);
    }
  }
  STAMP(22);
  unsigned dxa[3], dwa[3];
#pragma unroll
  for (int i = 0; i < 3; ++i) {
    const int L = 160 * w + 64 * i + lane, c = L >> 3, tt = 8 * (m & 1) + (L & 7);
    dxa[i] = OFF_XN + ((8 * P + (m >> 1)) * 20 + (c >> 2)) * 1024 + ((c & 3) * 16 + tt) * 16;
    const int q = w + 4 * i, pos = 64 * (q & 1) + lane, nb = pos >> 4, n = pos & 15;
    const int rg = 5 * P + (q >> 1), ks = rg >> 2, gg = rg & 3;
    dwa[i] = OFF_WQKV + m * WQKV_STR + (nb < 7 ? (nb * 20 + ks) * 1024 + (gg * 16 + n) * 16 : 143360 + ks * 512 + (gg * 8 + n) * 16);
  }
  const int gw = cuid * 4 + w;
  const int it0k = gw < 800 ? 0 : (gw < 912 ? 1 : 2), it0 = gw < 800 ? gw : (gw < 912 ? 2048 + gw - 800 : 2048 + gw - 912);
  unsigned srcb[5], dstb[5];
  {
    const int rp = r8 < 4 ? r8 : 12 + r8;
    const unsigned v1 = (unsigned)(rp * 1728 + 8 * ch8) * 2, v2 = (unsigned)(rp * 640 + 8 * ch8) * 2;
    auto src_w1 = [&](int u) { const int rg = u / 27, cg = u - 27 * rg; return v1 + (unsigned)((32 * (rg >> 2) + 4 * (rg & 3)) * 1728 + 64 * cg) * 2; };
    auto src_w2 = [&](int u) { const int rg = u / 10, cg = u - 10 * rg; return v2 + (unsigned)((32 * (rg >> 2) + 4 * (rg & 3)) * 640 + 64 * cg) * 2; };
    auto src_wo = [&](int u) {
      const int rg = u / 10, cg = u - 10 * rg, ks = rg >> 2, gg = rg & 3;
      const int idx = 8 * (ks % 5) + r8, row = 160 * (ks / 5) + 20 * (idx / 5) + 5 * gg + idx % 5;
      return (unsigned)(row * 640 + 64 * cg + 8 * ch8) * 2;
    };
    auto dst_w1 = [&](int u) {
      const int rg = u / 27, cg = u - 27 * rg, ks = rg >> 2, gg = rg & 3, n = 64 * cg + lane;
      const int nb = n >> 4, h2 = nb >= 54 ? 1 : 0, nbp = nb - 54 * h2;
      const int tt = nbp < 42 ? nbp / 7 : 6 + (nbp - 42) / 6, nbl = nbp - (tt < 6 ? 7 * tt : 42 + 6 * (tt - 6));
      return OFF_W1 + (8 * h2 + tt) * W1_STR + (ks * 7 + nbl) * 1024 + (gg * 16 + (n & 15)) * 16;
    };
    auto dst_5 = [&](int u, unsigned base, unsigned str) {
      const int rg = u / 10, cg = u - 10 * rg, ks = rg >> 2, gg = rg & 3, n = 64 * cg + lane;
      const int c = n / 80, nl = n - 80 * c;
      return base + c * str + (ks * 5 + (nl >> 4)) * 1024 + (gg * 16 + (nl & 15)) * 16;
    };
    srcb[0] = it0k == 0 ? src_wo(it0) : (it0k == 1 ? src_w1(it0) : src_w2(it0));
    dstb[0] = it0k == 1 ? dst_w1(it0) : dst_5(it0, it0k == 0 ? OFF_WO : OFF_W2, it0k == 0 ? 102400u : W2_STR);
    srcb[1] = src_w1(gw); srcb[2] = src_w1(gw + 1024); srcb[3] = src_w2(gw); srcb[4] = src_w2(gw + 1024);
    dstb[1] = dst_w1(gw); dstb[2] = dst_w1(gw + 1024); dstb[3] = dst_5(gw, OFF_W2, W2_STR); dstb[4] = dst_5(gw + 1024, OFF_W2, W2_STR);
  }
  STAMP(23);
  asm volatile("s_waitcnt vmcnt(2)" : "+v"(xv[0]), "+v"(xv[1]), "+v"(xv[2]), "+v"(xv[3]), "+v"(xv[4]) :: "memory");
  {
    float f[20];
#pragma unroll
    for (int i = 0; i < 5; ++i) { f[4 * i] = blo(xv[i].x); f[4 * i + 1] = bhi(xv[i].x); f[4 * i + 2] = blo(xv[i].y); f[4 * i + 3] = bhi(xv[i].y); }
    float s4[4];
#pragma unroll
    for (int j = 0; j < 4; ++j) s4[j] = (f[j] + f[4 + j]) + (f[8 + j] + f[12 + j]) + f[16 + j];
    const float mean = sum_x16(sum16((s4[0] + s4[1]) + (s4[2] + s4[3]))) * (1.f / 640.f);
#pragma unroll
    for (int i = 0; i < 20; ++i) f[i] -= mean;
#pragma unroll
    for (int j = 0; j < 4; ++j) s4[j] = (f[j] * f[j] + f[4 + j] * f[4 + j]) + (f[8 + j] * f[8 + j] + f[12 + j] * f[12 + j]) + f[16 + j] * f[16 + j];
    const float rstd = __builtin_amdgcn_rsqf(sum_x16(sum16((s4[0] + s4[1]) + (s4[2] + s4[3]))) * (1.f / 640.f) + 1e-5f);
    LAS char* sc = lds + L_A + ((lane & 31) >> 1) * 128 + (2 * w + (lane >> 5)) * 16 + (lane & 1) * 8;
#pragma unroll
    for (int i = 0; i < 5; ++i)
      *(LAS v2u*)(sc + i * 2048) = v2u{pk2(f[4 * i] * rstd, f[4 * i + 1] * rstd), pk2(f[4 * i + 2] * rstd, f[4 * i + 3] * rstd)};
  }
  STAMP(24);
  DRAIN();
  asm volatile("" :: "v"(warm0), "v"(warm1));
  BAR();
  STAMP(25);
#pragma unroll
  for (int i = 0; i < 3; ++i)
    if (i < 2 || lane < 32) st128(*(LAS v4u*)(lds + L_A + (160 * w + 64 * i + lane) * 16), ws, dxa[i], 0);
#pragma unroll
  for (int k = 0; k < 3; ++k) {
    const int q = w + 4 * k;
    if (q < 10) {
      const v4u o = item_read(lds + L_A + 16384 + q * 1024, lane);
      if (64 * (q & 1) + lane < 120) st128(o, ws, dwa[k], 0);
    }
  }
  {
    const rsrc_t ro = __builtin_amdgcn_make_buffer_rsrc((void*)wo, 0, 640 * 640 * 2, 0x00020000);
    const rsrc_t r1 = __builtin_amdgcn_make_buffer_rsrc((void*)w1, 0, 640 * 1728 * 2, 0x00020000);
    const rsrc_t r2 = __builtin_amdgcn_make_buffer_rsrc((void*)w2, 0, 1728 * 640 * 2, 0x00020000);
    const unsigned sb = ldsb + L_B + w * 5120;
    if (it0k == 0) dma_item(ro, srcb[0], sb); else if (it0k == 1) dma_item(r1, srcb[0], sb); else dma_item(r2, srcb[0], sb);
    dma_item(r1, srcb[1], sb + 1024); dma_item(r1, srcb[2], sb + 2048); dma_item(r2, srcb[3], sb + 3072); dma_item(r2, srcb[4], sb + 4096);
  }
  const unsigned tag = __builtin_amdgcn_readfirstlane(epoch) * 2654435761u + 0x9E3779B9u;
  STAMP(26);
  asm volatile("s_waitcnt vmcnt(5)" ::: "memory");
  BAR();
  if (tid == 0) __hip_atomic_store(flg + (0 * 256 + cuid) * 32, tag, RLX);
  STAMP(1);
  *(LAS v4u*)(lds + L_IMG + tid * 16) = v4u{0u, 0u, 0u, 0u};
  DRAIN();
#pragma unroll
  for (int k = 0; k < 5; ++k) st128(item_read(lds + L_B + w * 5120 + k * 1024, lane), ws, dstb[k], 0);
  STAMP(2);

  if (w == 0) {
    const int l = lane & 15;
    wait_flag(flg + (0 * 256 + (lane < 16 ? P * 16 + l : l * 16 + m)) * 32, tag, lane < 32);
  }
  BAR();
  STAMP(3);
  v4u fb;
  v4u xn[2][20];
  f32x4 akv[2][5];
  v4u wqs[13];
  {
    constexpr int D1 = 5;
    for (int q = w; q < 25; q += 4) dma4(ws, l16, OFF_WQKV + m * WQKV_STR + q * 4096, ldsb + L_A + q * 4096);
    const unsigned xb = OFF_XN + (8 * P + 2 * w) * 20 * 1024;
#pragma unroll
    for (int ks = 0; ks < D1; ++ks) {
#pragma unroll
      for (int u = 0; u < 2; ++u) xn[u][ks] = LDF(xb, u * 20 + ks);
      SCHED();
    }
    asm volatile("s_waitcnt vmcnt(10)" ::: "memory");
    BAR();
    if (tid == 0) __hip_atomic_store((gu32*)(wsp + OFF_FLAGB) + cuid, tag, RLX);
    STAMP(17);
#pragma unroll
    for (int u = 0; u < 2; ++u)
#pragma unroll
      for (int n = 0; n < 5; ++n) akv[u][n] = f32x4{0.f, 0.f, 0.f, 0.f};
    v4u wf[2][5];
#pragma unroll
    for (int n = 0; n < 5; ++n) wf[0][n] = LDSF(L_A + (n * 20) * 1024);
    SCHED();
    const unsigned wqb = OFF_WQKV + m * WQKV_STR + 102400 + w * 1024;
#pragma unroll
    for (int ks = 0; ks < 20; ++ks) {
      if (ks + D1 < 20) {
#pragma unroll
        for (int u = 0; u < 2; ++u) xn[u][ks + D1] = LDF(xb, u * 20 + ks + D1);
      } else {
#pragma unroll
        for (int i = (ks + D1 - 20) * 3; i < (ks + D1 - 20) * 3 + 3 && i < 13; ++i) wqs[i] = ld128(ws, l16, wqb + (i < 12 || w < 2 ? i : 11) * 4096);
        if (ks == 19) fb = __builtin_amdgcn_raw_buffer_load_b128(ws, OFF_FLAGB + l16, 0, AUX_SC1);
      }
      if (ks + 1 < 20) {
#pragma unroll
        for (int n = 0; n < 5; ++n) wf[(ks + 1) & 1][n] = LDSF(L_A + (n * 20 + ks + 1) * 1024);
      }
#pragma unroll
      for (int n = 0; n < 5; ++n)
#pragma unroll
        for (int u = 0; u < 2; ++u) akv[u][n] = mfma(xn[u][ks], wf[ks & 1][n], akv[u][n]);
#pragma unroll
      for (int n = 0; n < 5; ++n) { SGB(0x100, 1); SGB(0x8, 1); if (n < 3) SGB(0x20, 1); SGB(0x8, 1); }
      SCHED();
    }
  }
  STAMP(4);
  BAR();
#pragma unroll
  for (int u = 0; u < 2; ++u)
#pragma unroll
    for (int n = 0; n < 5; ++n) {
      f32x4 a = akv[u][n];
      if (n < 2) { a[0] = phi(a[0]); a[1] = phi(a[1]); a[2] = phi(a[2]); a[3] = phi(a[3]); }
      else if (n == 2) { if (t16 < 8) { a[0] = phi(a[0]); a[1] = phi(a[1]); a[2] = phi(a[2]); a[3] = phi(a[3]); } }
      *(LAS v2u*)(lds + L_A + (16 * n + t16) * EP + (32 * w + 16 * u + 4 * g) * 2) = v2u{pk2(a[0], a[1]), pk2(a[2], a[3])};
    }
  if (tid < 16) *(LAS v4u*)(lds + L_A + 80 * EP + tid * 16) = v4u{0x3F803F80u, 0x3F803F80u, 0x3F803F80u, 0x3F803F80u};
  BAR();
  STAMP(27);
  {
    const int hd = w >> 1, dt = w & 1;
    f32x4 t0 = {0.f, 0.f, 0.f, 0.f}, t1 = t0;
    const unsigned ra = L_A + (20 * hd + 16 * dt + t16) * EP + g * 16;
    const unsigned rb0 = L_A + (40 + 20 * hd + t16) * EP + g * 16;
    const unsigned rb1 = L_A + (t16 < 4 ? 56 + 20 * hd + t16 : 80) * EP + g * 16;
    v4u fa[4], f0[4], f1[4];
#pragma unroll
    for (int s = 0; s < 4; ++s) { fa[s] = *(LAS v4u*)(lds + ra + s * 64); f0[s] = *(LAS v4u*)(lds + rb0 + s * 64); f1[s] = *(LAS v4u*)(lds + rb1 + s * 64); }
#pragma unroll
    for (int s = 0; s < 4; ++s) { t0 = mfma(fa[s], f0[s], t0); t1 = mfma(fa[s], f1[s], t1); }
    const unsigned pb = OFF_PART + (m * 16 + P) * PART_STR + hd * 1680;
    if (dt == 0) {
      st128(__builtin_bit_cast(v4u, t0), ws, l16, pb);
      if (t16 < 5) st128(__builtin_bit_cast(v4u, t1), ws, 1024 + (g * 5 + t16) * 16, pb);
    } else if (g == 0) {
      st128(__builtin_bit_cast(v4u, t0), ws, 1344 + t16 * 16, pb);
      if (t16 < 5) st128(__builtin_bit_cast(v4u, t1), ws, 1600 + t16 * 16, pb);
    }
  }
  STAMP(28);
  DRAIN();
  BAR();
  if (tid == 0) __hip_atomic_store(flg + (1 * 256 + cuid) * 32, tag, RLX);
#pragma unroll
  for (int i = 0; i < 13; ++i)
    if (i < 12 || w < 2) *(LAS v4u*)(lds + L_B + (w + 4 * i) * 1024 + l16) = wqs[i];
  if (!__all(fb.x == tag && fb.y == tag && fb.z == tag && fb.w == tag)) {
    unsigned sp = 0;
    for (;;) {
      asm volatile("" ::: "memory");
      fb = __builtin_amdgcn_raw_buffer_load_b128(ws, OFF_FLAGB + l16, 0, AUX_SC1);
      if (__all(fb.x == tag && fb.y == tag && fb.z == tag && fb.w == tag)) break;
      if (++sp > SPIN_CAP) break;
    }
  }
  BAR();
  STAMP(5);

  unsigned qp[2][3][2];
  {
    f32x4 aq[2][3];
#pragma unroll
    for (int u = 0; u < 2; ++u)
#pragma unroll
      for (int n = 0; n < 3; ++n) aq[u][n] = f32x4{0.f, 0.f, 0.f, 0.f};
    LAS char* lq7 = lds + L_B + 40960 + g * 128 + (t16 & 7) * 16;
    v4u wf[3][3];
#pragma unroll
    for (int k = 0; k < 2; ++k) { wf[k][0] = LDSF(L_B + k * 1024); wf[k][1] = LDSF(L_B + (20 + k) * 1024); wf[k][2] = *(LAS v4u*)(lq7 + k * 512); }
    SCHED();
#pragma unroll
    for (int ks = 0; ks < 20; ++ks) {
      if (ks % 3 == 0) {
        const int q = w + 4 * (ks / 3);
        if (q < 25) dma4(ws, l16, OFF_WO + ct * 102400 + q * 4096, ldsb + L_A + q * 4096);
      }
      if (ks + 2 < 20) {
        wf[(ks + 2) % 3][0] = LDSF(L_B + (ks + 2) * 1024);
        wf[(ks + 2) % 3][1] = LDSF(L_B + (20 + ks + 2) * 1024);
        wf[(ks + 2) % 3][2] = *(LAS v4u*)(lq7 + (ks + 2) * 512);
      }
#pragma unroll
      for (int n = 0; n < 3; ++n)
#pragma unroll
        for (int u = 0; u < 2; ++u) aq[u][n] = mfma(wf[ks % 3][n], xn[u][ks], aq[u][n]);
#pragma unroll
      for (int n = 0; n < 3; ++n) { SGB(0x100, 1); SGB(0x8, 2); }
      SCHED();
    }
#pragma unroll
    for (int u = 0; u < 2; ++u)
#pragma unroll
      for (int n = 0; n < 3; ++n) {
        qp[u][n][0] = pk2(phi(aq[u][n][0]), phi(aq[u][n][1]));
        qp[u][n][1] = pk2(phi(aq[u][n][2]), phi(aq[u][n][3]));
      }
  }
  v4u w2f[14][5];
  const int k4s = w < 2 ? 14 * w : 28 + 13 * (w - 2);
  const unsigned w2b = OFF_W2 + ct * W2_STR + k4s * 5 * 1024;
#pragma unroll
  for (int k = 0; k < 2; ++k) {
#pragma unroll
    for (int n = 0; n < 5; ++n) w2f[k][n] = LDF(w2b, k * 5 + n);
    SCHED();
  }
  unsigned imgoff, imgoffz;
  {
    const int hd = tid >= 105 ? 1 : 0, u = tid - 105 * hd;
    int dq, e;
    if (u < 64) { dq = u >> 4; e = u & 15; }
    else if (u < 84) { const int v = u - 64; dq = v / 5; e = 16 + v - 5 * dq; }
    else if (u < 100) { dq = 4; e = u - 84; }
    else { dq = 4; e = 16 + u - 100; }
    int gq;
    if (hd == 0) gq = dq < 4 ? dq : 0; else gq = dq < 3 ? dq + 1 : (dq == 3 ? 0 : 1);
    const int j0 = dq < 4 ? 0 : 4, ge = e / 5, re = e - 5 * ge;
    const unsigned ib = L_IMG + hd * 2048 + gq * 256 + j0 * 2;
    imgoff = ib + (re < 4 ? (4 * ge + re) * 16 : 1024 + (4 * ge) * 16);
    imgoffz = e == 20 ? ib + 1024 + 16 : 0u;
  }
  STAMP(6);

  if (w == 0) wait_flag(flg + (1 * 256 + (lane & 15) * 16 + m) * 32, tag, lane < 16);
  BAR();
  STAMP(7);
  if (tid < 210) {
    v4u pv[16];
#pragma unroll
    for (int i = 0; i < 16; ++i) pv[i] = ld128(ws, tid * 16, OFF_PART + (m * 16 + i) * PART_STR);
    f32x4 s[4];
#pragma unroll
    for (int i = 0; i < 4; ++i)
      s[i] = (__builtin_bit_cast(f32x4, pv[4 * i]) + __builtin_bit_cast(f32x4, pv[4 * i + 1])) +
             (__builtin_bit_cast(f32x4, pv[4 * i + 2]) + __builtin_bit_cast(f32x4, pv[4 * i + 3]));
    const f32x4 st = (s[0] + s[1]) + (s[2] + s[3]);
    const v2u val = v2u{pk2(st[0], st[1]), pk2(st[2], st[3])};
    if (imgoffz == 0u) *(LAS v2u*)(lds + imgoff) = val;
    else {
#pragma unroll
      for (int ge = 0; ge < 4; ++ge) *(LAS v2u*)(lds + imgoffz + ge * 64) = val;
    }
  }
  BAR();
  STAMP(18);
  {
    v4u ka[2][2];
#pragma unroll
    for (int hd = 0; hd < 2; ++hd)
#pragma unroll
      for (int bb = 0; bb < 2; ++bb) ka[hd][bb] = *(LAS v4u*)(lds + L_IMG + hd * 2048 + bb * 1024 + l16);
    const int i4 = m & 3;
#pragma unroll
    for (int u = 0; u < 2; ++u) {
      v4u qa, qb;
      qa.x = qp[u][0][0]; qa.y = qp[u][0][1]; qa.z = g == 0 ? qp[u][1][0] : 0u; qa.w = g == 0 ? qp[u][1][1] : 0u;
      qb.x = g == 0 ? qp[u][2][0] : qp[u][1][0]; qb.y = g == 0 ? qp[u][2][1] : qp[u][1][1];
      qb.z = g == 1 ? qp[u][2][0] : 0u; qb.w = g == 1 ? qp[u][2][1] : 0u;
      const f32x4 z4 = {0.f, 0.f, 0.f, 0.f};
      const f32x4 a0 = mfma(ka[0][0], qa, z4), a1 = mfma(ka[0][1], qa, z4);
      const f32x4 b0 = mfma(ka[1][0], qb, z4), b1 = mfma(ka[1][1], qb, z4);
      const float ra = __builtin_amdgcn_rcpf(a1[1] + 1e-6f), rb = __builtin_amdgcn_rcpf(b1[1] + 1e-6f);
      const unsigned d0 = pk2(a0[0] * ra, a0[1] * ra), d1 = pk2(a0[2] * ra, a0[3] * ra), d2 = pk2(a1[0] * ra, b0[0] * rb),
                     d3 = pk2(b0[1] * rb, b0[2] * rb), d4 = pk2(b0[3] * rb, b1[0] * rb);
      const unsigned ob = OFF_O + ((8 * P + 2 * w + u) * 20 + 5 * (m >> 2)) * 1024;
      if (i4 == 0) {
        st128(v4u{d0, d1, d2, d3}, ws, l16, ob);
        __builtin_amdgcn_raw_buffer_store_b32(d4, ws, l16 + 1024, ob, AUX_SC1);
      } else if (i4 == 1) {
        __builtin_amdgcn_raw_buffer_store_b96(v3u{d0, d1, d2}, ws, l16 + 1024 + 4, ob, AUX_SC1);
        st64(v2u{d3, d4}, ws, l16 + 2048, ob);
      } else if (i4 == 2) {
        st64(v2u{d0, d1}, ws, l16 + 2048 + 8, ob);
        __builtin_amdgcn_raw_buffer_store_b96(v3u{d2, d3, d4}, ws, l16 + 3072, ob, AUX_SC1);
      } else {
        __builtin_amdgcn_raw_buffer_store_b32(d0, ws, l16 + 3072 + 12, ob, AUX_SC1);
        st128(v4u{d1, d2, d3, d4}, ws, l16 + 4096, ob);
      }
    }
  }
  DRAIN();
  BAR();
  if (tid == 0) __hip_atomic_store(flg + (2 * 256 + cuid) * 32, tag, RLX);
  STAMP(8);
  for (int q = w; q < 12; q += 4) dma4(ws, l16, OFF_W1 + m * W1_STR + q * 4096, ldsb + L_B + q * 4096);
  if (w == 0) dma1(ws, l16, OFF_W1 + m * W1_STR + 49152, ldsb + L_B + 49152);
  v2u xr[5];
  {
    const GAS char* xp = (const GAS char*)x + (size_t)(64 * (2 * P + jl) + 16 * w + t16) * 1280 + (80 * ct + 4 * g) * 2;
#pragma unroll
    for (int n = 0; n < 5; ++n) xr[n] = *(const GAS v2u*)(xp + n * 32);
  }
#pragma unroll
  for (int k = 2; k < 4; ++k) {
#pragma unroll
    for (int n = 0; n < 5; ++n) w2f[k][n] = LDF(w2b, k * 5 + n);
    SCHED();
  }

  STAMP(29);
  if (w == 0) wait_flag(flg + (2 * 256 + P * 16 + (lane & 15)) * 32, tag, lane < 16);
  STAMP(30);
  DRAIN();
  BAR();
  STAMP(9);
  unsigned hp[5][2];
  {
    constexpr int D2 = 8;
    v4u of[20];
    const unsigned ob = OFF_O + (8 * P + 4 * jl + w) * 20 * 1024;
#pragma unroll
    for (int ks = 0; ks < D2; ++ks) { of[ks] = LDF(ob, ks); SCHED(); }
    f32x4 ah[5];
#pragma unroll
    for (int n = 0; n < 5; ++n) ah[n] = f32x4{0.f, 0.f, 0.f, 0.f};
    v4u wf[3][5];
#pragma unroll
    for (int n = 0; n < 5; ++n) { wf[0][n] = LDSF(L_A + n * 1024); wf[1][n] = LDSF(L_A + (5 + n) * 1024); }
    SCHED();
#pragma unroll
    for (int ks = 0; ks < 20; ++ks) {
      if (ks + D2 < 20) of[ks + D2] = LDF(ob, ks + D2);
      if (ks + 2 < 20) {
#pragma unroll
        for (int n = 0; n < 5; ++n) wf[(ks + 2) % 3][n] = LDSF(L_A + ((ks + 2) * 5 + n) * 1024);
      }
#pragma unroll
      for (int n = 0; n < 5; ++n) ah[n] = mfma(wf[ks % 3][n], of[ks], ah[n]);
#pragma unroll
      for (int n = 0; n < 5; ++n) { SGB(0x100, 1); SGB(0x8, 1); if (n == 0) SGB(0x20, 1); }
      SCHED();
    }
    STAMP(19);
    float hv[20];
#pragma unroll
    for (int n = 0; n < 5; ++n) {
      hp[n][0] = pk2(blo(xr[n].x) + ah[n][0], bhi(xr[n].x) + ah[n][1]);
      hp[n][1] = pk2(blo(xr[n].y) + ah[n][2], bhi(xr[n].y) + ah[n][3]);
    }
    const unsigned hb = OFF_H + ((8 * P + 4 * jl + w) * 20 + ((5 * ct) >> 1)) * 1024;
    if ((ct & 1) == 0) {
      st128(v4u{hp[0][0], hp[0][1], hp[1][0], hp[1][1]}, ws, l16, hb);
      st128(v4u{hp[2][0], hp[2][1], hp[3][0], hp[3][1]}, ws, l16 + 1024, hb);
      st64(v2u{hp[4][0], hp[4][1]}, ws, l16 + 2048, hb);
    } else {
      st64(v2u{hp[0][0], hp[0][1]}, ws, l16 + 8, hb);
      st128(v4u{hp[1][0], hp[1][1], hp[2][0], hp[2][1]}, ws, l16 + 1024, hb);
      st128(v4u{hp[3][0], hp[3][1], hp[4][0], hp[4][1]}, ws, l16 + 2048, hb);
    }
    float s4[5];
#pragma unroll
    for (int n = 0; n < 5; ++n) {
      hv[4 * n] = blo(hp[n][0]); hv[4 * n + 1] = bhi(hp[n][0]); hv[4 * n + 2] = blo(hp[n][1]); hv[4 * n + 3] = bhi(hp[n][1]);
      s4[n] = (hv[4 * n] + hv[4 * n + 1]) + (hv[4 * n + 2] + hv[4 * n + 3]);
    }
    const float mi = sum_g((s4[0] + s4[1]) + (s4[2] + s4[3]) + s4[4]) * (1.f / 80.f);
#pragma unroll
    for (int n = 0; n < 5; ++n) {
      const float a = hv[4 * n] - mi, bq = hv[4 * n + 1] - mi, c = hv[4 * n + 2] - mi, d = hv[4 * n + 3] - mi;
      s4[n] = (a * a + bq * bq) + (c * c + d * d);
    }
    const float q = sum_g((s4[0] + s4[1]) + (s4[2] + s4[3]) + s4[4]);
    if (g == 0) st64(v2u{asu(mi), asu(q)}, ws, (64 * (2 * P + jl) + 16 * w + t16) * 64 + ct * 8, OFF_ST);
  }
  DRAIN();
  BAR();
  if (tid == 0) __hip_atomic_store(flg + (3 * 256 + cuid) * 32, tag, RLX);
  STAMP(10);
  for (int q = w; q < 22; q += 4) dma4(ws, l16, OFF_W1 + m * W1_STR + 50176 + q * 4096, ldsb + L_A + q * 4096);
  if (w < 3) dma1(ws, l16, OFF_W1 + m * W1_STR + 50176 + 90112 + w * 1024, ldsb + L_A + 90112 + w * 1024);
  {
    f32x4 cs[7];
#pragma unroll
    for (int n = 0; n < 7; ++n) cs[n] = f32x4{0.f, 0.f, 0.f, 0.f};
    const v4u ones = {0x3F803F80u, 0x3F803F80u, 0x3F803F80u, 0x3F803F80u};
    {
      v4u wf[7];
#pragma unroll
      for (int n = 0; n < 7; ++n) wf[n] = *(LAS v4u*)(lb0 + L_B + w * 7168 + n * 1024);
#pragma unroll
      for (int n = 0; n < 7; ++n) cs[n] = mfma(wf[n], ones, cs[n]);
      if (w < 3) {
#pragma unroll
        for (int n = 0; n < 7; ++n) wf[n] = *(LAS v4u*)(lb0 + L_B + (w + 4) * 7168 + n * 1024);
#pragma unroll
        for (int n = 0; n < 7; ++n) cs[n] = mfma(wf[n], ones, cs[n]);
      }
    }
    DRAIN();
    BAR();
#pragma unroll
    for (int i = 0; i < 4; ++i) {
      const int j = ((w + 1) & 3) + 4 * i;
      if (j < 13) {
        v4u wf[7];
#pragma unroll
        for (int n = 0; n < 7; ++n) wf[n] = *(LAS v4u*)(lb0 + L_A + j * 7168 + n * 1024);
#pragma unroll
        for (int n = 0; n < 7; ++n) cs[n] = mfma(wf[n], ones, cs[n]);
      }
    }
    if (t16 == 0) {
#pragma unroll
      for (int n = 0; n < 7; ++n) *(LAS f32x4*)(lds + L_CS + (w * 7 + n) * 64 + g * 16) = cs[n];
    }
  }
  STAMP(20);

  if (w == 0) wait_flag(flg + (3 * 256 + P * 16 + (lane & 15)) * 32, tag, lane < 16);
  BAR();
  STAMP(11);
  {
    constexpr int D3 = 5;
    v4u hf[2][20];
    const unsigned hb = OFF_H + (8 * P + 2 * w) * 20 * 1024;
    v4u stv[2];
#pragma unroll
    for (int ks = 0; ks < D3; ++ks) {
#pragma unroll
      for (int u = 0; u < 2; ++u) hf[u][ks] = LDF(hb, u * 20 + ks);
      SCHED();
    }
#pragma unroll
    for (int u = 0; u < 2; ++u) stv[u] = ld128(ws, (128 * P + 32 * w + 16 * u + t16) * 64 + g * 16, OFF_ST);
    f32x4 a3[2][7];
#pragma unroll
    for (int u = 0; u < 2; ++u)
#pragma unroll
      for (int n = 0; n < 7; ++n) a3[u][n] = f32x4{0.f, 0.f, 0.f, 0.f};
    v4u wf[2][7];
#pragma unroll
    for (int n = 0; n < 7; ++n) wf[0][n] = LDSF(W1A(0, n));
    SCHED();
#pragma unroll
    for (int ks = 0; ks < 20; ++ks) {
      if (ks + D3 < 20) {
#pragma unroll
        for (int u = 0; u < 2; ++u) hf[u][ks + D3] = LDF(hb, u * 20 + ks + D3);
      }
      w2f[4 + ks / 5][ks % 5] = LDF(w2b, 20 + ks);
      if (ks >= 15) w2f[8][ks - 15] = LDF(w2b, 40 + ks - 15);
      if (ks + 1 < 20) {
#pragma unroll
        for (int n = 0; n < 7; ++n) wf[(ks + 1) & 1][n] = LDSF(W1A(ks + 1, n));
      }
#pragma unroll
      for (int n = 0; n < 7; ++n)
#pragma unroll
        for (int u = 0; u < 2; ++u) a3[u][n] = mfma(wf[ks & 1][n], hf[u][ks], a3[u][n]);
#pragma unroll
      for (int n = 0; n < 7; ++n) { SGB(0x100, 1); SGB(0x8, 1); if (n < 3) SGB(0x20, 1); SGB(0x8, 1); }
      SCHED();
    }
    STAMP(12);
#pragma unroll
    for (int k = 9; k < 11; ++k) {
#pragma unroll
      for (int n = 0; n < 5; ++n) w2f[k][n] = LDF(w2b, k * 5 + n);
    }
    SCHED();
    float mean[2], rstd[2];
#pragma unroll
    for (int u = 0; u < 2; ++u) {
      const float m0 = asf(stv[u].x), q0 = asf(stv[u].y), m1 = asf(stv[u].z), q1 = asf(stv[u].w);
      const float mu = sum_g(m0 + m1) * 0.125f;
      const float M2 = sum_g(q0 + q1 + 80.f * ((m0 - mu) * (m0 - mu) + (m1 - mu) * (m1 - mu)));
      rstd[u] = __builtin_amdgcn_rsqf(M2 * (1.f / 640.f) + 1e-5f);
      mean[u] = mu * rstd[u];
    }
    const int tt = m & 7;
    const int nb0 = 54 * c2 + (tt < 6 ? 7 * tt : 42 + 6 * (tt - 6));
    const bool seven = tt < 6;
    unsigned hq[2][7][2];
#pragma unroll
    for (int n = 0; n < 7; ++n) {
      f32x4 cw[4];
#pragma unroll
      for (int ww = 0; ww < 4; ++ww) cw[ww] = *(LAS f32x4*)(lds + L_CS + (ww * 7 + n) * 64 + g * 16);
      const f32x4 cs = (cw[0] + cw[1]) + (cw[2] + cw[3]);
#pragma unroll
      for (int u = 0; u < 2; ++u) {
        const float v0 = fmaxf(a3[u][n][0] * rstd[u] - mean[u] * cs[0], 0.f), v1 = fmaxf(a3[u][n][1] * rstd[u] - mean[u] * cs[1], 0.f);
        const float v2 = fmaxf(a3[u][n][2] * rstd[u] - mean[u] * cs[2], 0.f), v3 = fmaxf(a3[u][n][3] * rstd[u] - mean[u] * cs[3], 0.f);
        hq[u][n][0] = pk2(v0, v1); hq[u][n][1] = pk2(v2, v3);
      }
    }
#pragma unroll
    for (int u = 0; u < 2; ++u) {
      const unsigned db = OFF_HID + ((8 * P + 2 * w + u) * 54 + (nb0 >> 1)) * 1024;
      if ((nb0 & 1) == 0) {
        st128(v4u{hq[u][0][0], hq[u][0][1], hq[u][1][0], hq[u][1][1]}, ws, l16, db);
        st128(v4u{hq[u][2][0], hq[u][2][1], hq[u][3][0], hq[u][3][1]}, ws, l16 + 1024, db);
        st128(v4u{hq[u][4][0], hq[u][4][1], hq[u][5][0], hq[u][5][1]}, ws, l16 + 2048, db);
        if (seven) st64(v2u{hq[u][6][0], hq[u][6][1]}, ws, l16 + 3072, db);
      } else {
        st64(v2u{hq[u][0][0], hq[u][0][1]}, ws, l16 + 8, db);
        st128(v4u{hq[u][1][0], hq[u][1][1], hq[u][2][0], hq[u][2][1]}, ws, l16 + 1024, db);
        st128(v4u{hq[u][3][0], hq[u][3][1], hq[u][4][0], hq[u][4][1]}, ws, l16 + 2048, db);
        st128(v4u{hq[u][5][0], hq[u][5][1], hq[u][6][0], hq[u][6][1]}, ws, l16 + 3072, db);
      }
    }
  }
  STAMP(21);
  DRAIN();
  BAR();
  if (tid == 0) {
    __hip_atomic_store(flg + (4 * 256 + cuid) * 32, tag, RLX);
    if (cuid == 0) __hip_atomic_store((gu32*)(wsp + OFF_EPOCH), epoch + 1u, RLX);
  }
  STAMP(13);
#pragma unroll
  for (int k = 11; k < 14; ++k) {
#pragma unroll
    for (int n = 0; n < 5; ++n) w2f[k][n] = LDF(w2b, k * 5 + n);
    SCHED();
  }

  if (w == 0) wait_flag(flg + (4 * 256 + P * 16 + (lane & 15)) * 32, tag, lane < 16);
  BAR();
  STAMP(14);
  {
    f32x4 a4[4][5];
#pragma unroll
    for (int u = 0; u < 4; ++u)
#pragma unroll
      for (int n = 0; n < 5; ++n) a4[u][n] = f32x4{0.f, 0.f, 0.f, 0.f};
    const unsigned hb = OFF_HID + ((8 * P + 4 * jl) * 54 + k4s) * 1024;
    constexpr int DP = 3;
    v4u af[DP + 1][4];
#pragma unroll
    for (int k = 0; k < DP; ++k)
#pragma unroll
      for (int u = 0; u < 4; ++u) { af[k][u] = ld128(ws, l16 + (k & 3) * 1024, hb + u * 54 * 1024 + (k >> 2) * 4096); SCHED(); }
#pragma unroll
    for (int k = 0; k < 14; ++k) {
      if (k + DP < 14) {
        if (k + DP < 13 || w < 2) {
#pragma unroll
          for (int u = 0; u < 4; ++u) af[(k + DP) % (DP + 1)][u] = ld128(ws, l16 + ((k + DP) & 3) * 1024, hb + u * 54 * 1024 + ((k + DP) >> 2) * 4096);
        }
      }
      if (k < 13 || w < 2) {
#pragma unroll
        for (int u = 0; u < 4; ++u)
#pragma unroll
          for (int n = 0; n < 5; ++n) a4[u][n] = mfma(w2f[k][n], af[k % (DP + 1)][u], a4[u][n]);
      }
#pragma unroll
      for (int u = 0; u < 4; ++u) { SGB(0x20, 1); SGB(0x8, 5); }
      SCHED();
    }
    STAMP(15);
#pragma unroll
    for (int u = 0; u < 4; ++u) {
      if (u != w) {
        const int si = w < u ? w : w - 1;
#pragma unroll
        for (int n = 0; n < 5; ++n) *(LAS f32x4*)(lds + L_A + ((u * 3 + si) * 5 + n) * 1024 + l16) = a4[u][n];
      }
    }
    BAR();
    GAS char* op = (GAS char*)out + (size_t)(64 * (2 * P + jl) + 16 * w + t16) * 1280 + (80 * ct + 4 * g) * 2;
#pragma unroll
    for (int n = 0; n < 5; ++n) {
      f32x4 v = w == 0 ? a4[0][n] : (w == 1 ? a4[1][n] : (w == 2 ? a4[2][n] : a4[3][n]));
      f32x4 p[3];
#pragma unroll
      for (int si = 0; si < 3; ++si) p[si] = *(LAS f32x4*)(lds + L_A + ((w * 3 + si) * 5 + n) * 1024 + l16);
      v = (v + p[0]) + (p[1] + p[2]);
      const v2u o = {pk2(blo(hp[n][0]) + v[0], bhi(hp[n][0]) + v[1]), pk2(blo(hp[n][1]) + v[2], bhi(hp[n][1]) + v[3])};
      *(GAS v2u*)(op + n * 32) = o;
    }
  }
  STAMP(16);
}

extern "C" void kernel_launch(void* const* d_in, const int* in_sizes, int n_in, void* d_out, int out_size, void* d_ws, size_t ws_size,
                              hipStream_t stream) {
  (void)in_sizes; (void)n_in; (void)out_size; (void)ws_size;
  hipFuncSetAttribute((const void*)fused_block, hipFuncAttributeMaxDynamicSharedMemorySize, LDS_BYTES);
  fused_block<<<256, 256, LDS_BYTES, stream>>>((const unsigned short*)d_in[0], (const unsigned short*)d_in[1], (const unsigned short*)d_in[2],
                                               (const unsigned short*)d_in[4], (const unsigned short*)d_in[5], (unsigned short*)d_out,
                                               (char*)d_ws);
}
```

```cpp
#include <hip/hip_runtime.h>
#include <hip/hip_bf16.h>
#include <stdint.h>

typedef unsigned v4u __attribute__((ext_vector_type(4)));
typedef unsigned v3u __attribute__((ext_vector_type(3)));
typedef unsigned v2u __attribute__((ext_vector_type(2)));
typedef float f32x4 __attribute__((ext_vector_type(4)));
typedef short bf16x8 __attribute__((ext_vector_type(8)));
typedef short s16x4 __attribute__((ext_vector_type(4)));
typedef __bf16 bf2 __attribute__((ext_vector_type(2)));
typedef __amdgpu_buffer_rsrc_t rsrc_t;
#define GAS __attribute__((address_space(1)))
#define LAS __attribute__((address_space(3)))
#define DI __device__ __forceinline__
#define RLX __ATOMIC_RELAXED, __HIP_MEMORY_SCOPE_AGENT
typedef GAS unsigned gu32;

#define TS 0
#define CODEPF 1
#define PRETOUCH 1
constexpr int CODE_LINES = 290;

constexpr unsigned OFF_EPOCH = 0;
constexpr unsigned OFF_FLAG = 4096;
constexpr unsigned OFF_FLAGB = OFF_FLAG + 5 * 256 * 128;
constexpr unsigned OFF_TS = 65 * 1024 * 1024;
constexpr unsigned OFF_TS2 = 64 * 1024 * 1024;
constexpr unsigned OFF_XN = 256 * 1024;
constexpr unsigned WQKV_STR = 153600;
constexpr unsigned OFF_WQKV = OFF_XN + 128 * 20 * 1024;
constexpr unsigned OFF_WO = OFF_WQKV + 16 * WQKV_STR;
constexpr unsigned W1_STR = 20 * 7 * 1024;
constexpr unsigned OFF_W1 = OFF_WO + 8 * 102400;
constexpr unsigned W2_STR = 54 * 5 * 1024;
constexpr unsigned OFF_W2 = OFF_W1 + 16 * W1_STR;
constexpr unsigned PART_STR = 3456;
constexpr unsigned OFF_PART = OFF_W2 + 8 * W2_STR;
constexpr unsigned OFF_OM = OFF_PART + 256 * PART_STR;
constexpr unsigned OFF_OR = OFF_OM + 128 * 16 * 1024;
constexpr unsigned OFF_HF = OFF_OR + 128 * 16 * 256;
constexpr unsigned OFF_HH = OFF_HF + 128 * 16 * 1024;
constexpr unsigned OFF_ST = OFF_HH + 128 * 8 * 512;
constexpr unsigned OFF_DF = OFF_ST + 128 * 8 * 128;
constexpr unsigned OFF_DH = OFF_DF + 128 * 48 * 1024;
constexpr unsigned WS_BYTES = OFF_DH + 128 * 16 * 512;

constexpr unsigned L_A = 0;
constexpr unsigned L_B = 102400;
constexpr unsigned L_M = 153600;
constexpr unsigned L_IMG = L_M;
constexpr unsigned L_CS = L_M + 4096;
constexpr unsigned L_PT = L_M + 6144;
constexpr unsigned LDS_BYTES = 163840;
constexpr unsigned EP = 272;

constexpr int AUX_SC1 = 16;
constexpr unsigned SPIN_CAP = 1u << 20;

DI f32x4 mfma(v4u a, v4u b, f32x4 c) {
  return __builtin_amdgcn_mfma_f32_16x16x32_bf16(__builtin_bit_cast(bf16x8, a), __builtin_bit_cast(bf16x8, b), c, 0, 0, 0);
}
DI unsigned pk2(float a, float b) { bf2 r = {(__bf16)a, (__bf16)b}; return __builtin_bit_cast(unsigned, r); }
DI float asf(unsigned u) { return __builtin_bit_cast(float, u); }
DI unsigned asu(float f) { return __builtin_bit_cast(unsigned, f); }
DI float blo(unsigned u) { return asf(u << 16); }
DI float bhi(unsigned u) { return asf(u & 0xffff0000u); }
DI float phi(float t) { return (t > 0.f ? t + 1.f : __expf(t)) + 1e-6f; }
template <int C> DI float dpp_add(float v) { return v + asf((unsigned)__builtin_amdgcn_update_dpp(0, (int)asu(v), C, 0xF, 0xF, true)); }
DI float sum16(float v) { v = dpp_add<0xB1>(v); v = dpp_add<0x4E>(v); v = dpp_add<0x141>(v); v = dpp_add<0x140>(v); return v; }
DI float sum_x16(float v) { const unsigned u = asu(v); auto r = __builtin_amdgcn_permlane16_swap(u, u, false, false); return asf(r[0]) + asf(r[1]); }
DI float sum_x32(float v) { const unsigned u = asu(v); auto r = __builtin_amdgcn_permlane32_swap(u, u, false, false); return asf(r[0]) + asf(r[1]); }
DI float sum_g(float v) { return sum_x32(sum_x16(v)); }
#define SCHED() __builtin_amdgcn_sched_barrier(0)
#define SGB(mask, n) __builtin_amdgcn_sched_group_barrier(mask, n, 0)
#define DRAIN() asm volatile("s_waitcnt vmcnt(0)" ::: "memory")
#define BAR() do { asm volatile("s_waitcnt lgkmcnt(0)" ::: "memory"); __builtin_amdgcn_s_barrier(); asm volatile("" ::: "memory"); } while (0)

DI v4u ld128(rsrc_t rs, unsigned voff, unsigned soff) { return __builtin_amdgcn_raw_buffer_load_b128(rs, voff, soff, AUX_SC1); }
DI void st128(v4u v, rsrc_t rs, unsigned voff, unsigned soff) { __builtin_amdgcn_raw_buffer_store_b128(v, rs, voff, soff, AUX_SC1); }
DI v2u ld64(rsrc_t rs, unsigned voff, unsigned soff) { return __builtin_amdgcn_raw_buffer_load_b64(rs, voff, soff, AUX_SC1); }
DI unsigned ld32(rsrc_t rs, unsigned voff, unsigned soff) { return __builtin_amdgcn_raw_buffer_load_b32(rs, voff, soff, AUX_SC1); }
DI void st64(v2u v, rsrc_t rs, unsigned voff, unsigned soff) { __builtin_amdgcn_raw_buffer_store_b64(v, rs, voff, soff, AUX_SC1); }

DI void dma4(rsrc_t rs, unsigned voff, unsigned soff, unsigned m0v) {
  asm volatile("s_nop 4\n\ts_mov_b32 m0, %0\n\ts_nop 0\n\t"
               "buffer_load_dwordx4 %1, %2, %3 offen sc1 lds\n\t"
               "buffer_load_dwordx4 %1, %2, %3 offen offset:1024 sc1 lds\n\t"
               "buffer_load_dwordx4 %1, %2, %3 offen offset:2048 sc1 lds\n\t"
               "buffer_load_dwordx4 %1, %2, %3 offen offset:3072 sc1 lds"
               :: "s"(m0v), "v"(voff), "s"(rs), "s"(soff) : "m0", "memory");
}
DI void dma1(rsrc_t rs, unsigned voff, unsigned soff, unsigned m0v) {
  asm volatile("s_nop 4\n\ts_mov_b32 m0, %0\n\ts_nop 0\n\t"
               "buffer_load_dwordx4 %1, %2, %3 offen sc1 lds"
               :: "s"(m0v), "v"(voff), "s"(rs), "s"(soff) : "m0", "memory");
}
DI void dma_item(rsrc_t rs, unsigned voff, unsigned m0v) {
  asm volatile("s_nop 4\n\ts_mov_b32 m0, %0\n\ts_nop 0\n\t"
               "buffer_load_dwordx4 %1, %2, 0 offen lds"
               :: "s"(m0v), "v"(voff), "s"(rs) : "m0", "memory");
}
DI v4u item_read(LAS char* img, int lane) {
  LAS char* p = img + ((lane >> 2) & 3) * 128 + (lane >> 4) * 32 + (lane & 3) * 8;
  const s16x4 a = __builtin_amdgcn_ds_read_tr16_b64_v4i16((LAS s16x4*)p);
  const s16x4 b = __builtin_amdgcn_ds_read_tr16_b64_v4i16((LAS s16x4*)(p + 512));
  const v2u x = __builtin_bit_cast(v2u, a), y = __builtin_bit_cast(v2u, b);
  return v4u{x.x, x.y, y.x, y.y};
}
#define LDF(sbase, i) ld128(ws, l16 + ((i) & 3) * 1024, (sbase) + ((i) >> 2) * 4096)
#define LDSF(off) (*(LAS v4u*)(((off) < 65536 ? lb0 : ((off) < 131072 ? lb1 : lb2)) + ((off) & 65535)))
#define W1A(ks, n) (((ks) < 7 ? L_B + (ks) * 7168 : L_A + ((ks) - 7) * 7168) + (n) * 1024)

DI void wait_flag(gu32* f, unsigned tag, bool active) {
  unsigned sp = 0;
  for (;;) {
    unsigned v = active ? __hip_atomic_load(f, RLX) : tag;
    if (__all(v == tag)) break;
    if (++sp > SPIN_CAP) break;
  }
  __builtin_amdgcn_fence(__ATOMIC_ACQUIRE, "wavefront");
}

#define STAMP(k) do { } while (0)

__global__ __launch_bounds__(256, 1) void fused_block(const unsigned short* __restrict__ x, const unsigned short* __restrict__ wqkv,
                                                      const unsigned short* __restrict__ wo, const unsigned short* __restrict__ w1,
                                                      const unsigned short* __restrict__ w2, unsigned short* __restrict__ out,
                                                      char* __restrict__ wsp) {
  extern __shared__ __attribute__((aligned(16))) char smem[];
  LAS char* lds = (LAS char*)smem;
  const unsigned ldsb = (unsigned)(size_t)lds;
  const int tid = threadIdx.x, lane = tid & 63;
  const int w = __builtin_amdgcn_readfirstlane(tid >> 6);
  const int g = lane >> 4, t16 = lane & 15;
  const int b = blockIdx.x, xcd = b & 7, sl = b >> 3;
  const int c2 = xcd & 1, P = 4 * (xcd >> 1) + (sl >> 3), m = 8 * c2 + (sl & 7);
  const int cuid = P * 16 + m;
  const int jl = (m & 7) >> 2, ct = 4 * c2 + (m & 3);
  const unsigned l16 = lane * 16;
  const rsrc_t ws = __builtin_amdgcn_make_buffer_rsrc(wsp, 0, WS_BYTES, 0x00020000);
  gu32* flg = (gu32*)(wsp + OFF_FLAG);
  STAMP(0);
  unsigned warm0 = 0, warm1 = 0;
  if (w == 3) {
    unsigned long long pc;
    asm volatile("s_getpc_b64 %0" : "=s"(pc));
    const GAS char* cp = (const GAS char*)((pc & ~127ull) + (unsigned long long)(sl * 10 + (lane < 10 ? lane : 0)) * 128ull);
    if (sl * 10 + lane < CODE_LINES && lane < 10) asm volatile("global_load_dword %0, %1, off" : "=v"(warm0) : "v"(cp) : "memory");
  }
  if (w == 2) {
    const GAS char* tp = lane == 0 ? (const GAS char*)wo : lane == 1 ? (const GAS char*)w1 : lane == 2 ? (const GAS char*)w2 : lane == 3 ? (const GAS char*)out
                       : lane == 4 ? (const GAS char*)w1 + 2097152 : lane == 5 ? (const GAS char*)w2 + 2097152 : lane == 6 ? (const GAS char*)out + 2097152
                       : (const GAS char*)wsp + (size_t)(lane - 7) * 2097152;
    if (lane < 19) asm volatile("global_load_dword %0, %1, off" : "=v"(warm1) : "v"(tp) : "memory");
  }
  const unsigned epoch = __hip_atomic_load((gu32*)(wsp + OFF_EPOCH), RLX);
  LAS char* lb0 = lds + l16; LAS char* lb1 = lb0 + 65536; LAS char* lb2 = lb0 + 131072;
  asm volatile("" : "+v"(lb1), "+v"(lb2));

  v2u xv[5];
  {
    const GAS char* xp = (const GAS char*)(x + (size_t)(128 * P + 8 * m + 2 * w + (lane >> 5)) * 640) + (lane & 31) * 8;
    asm volatile("global_load_dwordx2 %0, %5, off\n\tglobal_load_dwordx2 %1, %5, off offset:256\n\tglobal_load_dwordx2 %2, %5, off offset:512\n\t"
                 "global_load_dwordx2 %3, %5, off offset:768\n\tglobal_load_dwordx2 %4, %5, off offset:1024"
                 : "=&v"(xv[0]), "=&v"(xv[1]), "=&v"(xv[2]), "=&v"(xv[3]), "=&v"(xv[4]) : "v"(xp) : "memory");
  }
  const int r8 = lane >> 3, ch8 = lane & 7;
  {
    const rsrc_t rq = __builtin_amdgcn_make_buffer_rsrc((void*)wqkv, 0, 640 * 1920 * 2, 0x00020000);
    const int col0 = ch8 < 5 ? 640 + 40 * m + 8 * ch8 : 1280 + 40 * m + 8 * (ch8 - 5);
    const int col1 = ch8 < 2 ? 1280 + 40 * m + 24 + 8 * ch8 : 40 * m + 8 * ((ch8 < 7 ? ch8 : 6) - 2);
#pragma unroll
    for (int k = 0; k < 3; ++k) {
      const int q = w + 4 * k;
      if (q < 10) dma_item(rq, (unsigned)((40 * P + 8 * (q >> 1) + r8) * 1920 + ((q & 1) ? col1 : col0)) * 2, ldsb + L_A + 16384 + q * 1024);
    }
  }
  STAMP(22);
  unsigned dxa[3], dwa[3];
#pragma unroll
  for (int i = 0; i < 3; ++i) {
    const int L = 160 * w + 64 * i + lane, c = L >> 3, tt = 8 * (m & 1) + (L & 7);
    dxa[i] = OFF_XN + ((8 * P + (m >> 1)) * 20 + (c >> 2)) * 1024 + ((c & 3) * 16 + tt) * 16;
    const int q = w + 4 * i, pos = 64 * (q & 1) + lane, nb = pos >> 4, n = pos & 15;
    const int rg = 5 * P + (q >> 1), ks = rg >> 2, gg = rg & 3;
    dwa[i] = OFF_WQKV + m * WQKV_STR + (nb < 7 ? (nb * 20 + ks) * 1024 + (gg * 16 + n) * 16 : 143360 + ks * 512 + (gg * 8 + n) * 16);
  }
  const int gw = cuid * 4 + w;
  const int it0k = gw < 800 ? 0 : (gw < 912 ? 1 : 2), it0 = gw < 800 ? gw : (gw < 912 ? 2048 + gw - 800 : 2048 + gw - 912);
  unsigned srcb[5], dstb[5];
  {
    auto src_w1 = [&](int u) {
      const int rg = u / 27, cg = u - 27 * rg, ks = rg >> 2, gg = rg & 3;
      const int nA = ks < 16 ? 5 * (ks >> 1) + 2 * (ks & 1) : 10 * (ks - 16) + 4, nB = ks < 16 ? nA + 1 : nA + 5;
      return (unsigned)((16 * (r8 < 4 ? nA : nB) + 4 * gg + (r8 & 3)) * 1728 + 64 * cg + 8 * ch8) * 2;
    };
    auto src_w2 = [&](int u) {
      const int rg = u / 10, cg = u - 10 * rg, sq = rg >> 2, gg = rg & 3;
      const int wq = sq < 28 ? sq / 14 : 2 + (sq - 28) / 13, k = sq - (wq < 2 ? 14 * wq : 28 + 13 * (wq - 2));
      int nA, nB;
      if (k < 12) {
        const int tt = 4 * (wq & 1) + k / 3, i = k % 3;
        nA = 54 * (wq >> 1) + (tt < 6 ? 7 * tt : 42 + 6 * (tt - 6)) + 2 * i; nB = nA + 1;
      } else {
        const int ci = (wq < 2 ? 2 * wq : wq + 2) + (k - 12), h2 = ci / 3, a3 = ci - 3 * h2;
        nA = 54 * h2 + 14 * a3 + 6; nB = nA + 7;
      }
      return (unsigned)((16 * (r8 < 4 ? nA : nB) + 4 * gg + (r8 & 3)) * 640 + 64 * cg + 8 * ch8) * 2;
    };
    auto src_wo = [&](int u) {
      const int rg = u / 10, cg = u - 10 * rg, ks = rg >> 2, gg = rg & 3;
      const int row = ks < 16 ? 40 * ks + 5 * gg + (r8 < 5 ? r8 : 15 + r8) : 40 * (4 * (ks - 16) + (r8 >> 1)) + 23 + 5 * gg + (r8 & 1);
      return (unsigned)(row * 640 + 64 * cg + 8 * ch8) * 2;
    };
    auto dst_w1 = [&](int u) {
      const int rg = u / 27, cg = u - 27 * rg, ks = rg >> 2, gg = rg & 3, n = 64 * cg + lane;
      const int nb = n >> 4, h2 = nb >= 54 ? 1 : 0, nbp = nb - 54 * h2;
      const int tt = nbp < 42 ? nbp / 7 : 6 + (nbp - 42) / 6, nbl = nbp - (tt < 6 ? 7 * tt : 42 + 6 * (tt - 6));
      return OFF_W1 + (8 * h2 + tt) * W1_STR + (ks * 7 + nbl) * 1024 + (gg * 16 + (n & 15)) * 16;
    };
    auto dst_5 = [&](int u, unsigned base, unsigned str) {
      const int rg = u / 10, cg = u - 10 * rg, ks = rg >> 2, gg = rg & 3, n = 64 * cg + lane;
      const int c = n / 80, nl = n - 80 * c;
      return base + c * str + (ks * 5 + (nl >> 4)) * 1024 + (gg * 16 + (nl & 15)) * 16;
    };
    srcb[0] = it0k == 0 ? src_wo(it0) : (it0k == 1 ? src_w1(it0) : src_w2(it0));
    dstb[0] = it0k == 1 ? dst_w1(it0) : dst_5(it0, it0k == 0 ? OFF_WO : OFF_W2, it0k == 0 ? 102400u : W2_STR);
    srcb[1] = src_w1(gw); srcb[2] = src_w1(gw + 1024); srcb[3] = src_w2(gw); srcb[4] = src_w2(gw + 1024);
    dstb[1] = dst_w1(gw); dstb[2] = dst_w1(gw + 1024); dstb[3] = dst_5(gw, OFF_W2, W2_STR); dstb[4] = dst_5(gw + 1024, OFF_W2, W2_STR);
  }
  STAMP(23);
  asm volatile("s_waitcnt vmcnt(2)" : "+v"(xv[0]), "+v"(xv[1]), "+v"(xv[2]), "+v"(xv[3]), "+v"(xv[4]) :: "memory");
  {
    const rsrc_t ro = __builtin_amdgcn_make_buffer_rsrc((void*)wo, 0, 640 * 640 * 2, 0x00020000);
    const rsrc_t r1 = __builtin_amdgcn_make_buffer_rsrc((void*)w1, 0, 640 * 1728 * 2, 0x00020000);
    const rsrc_t r2 = __builtin_amdgcn_make_buffer_rsrc((void*)w2, 0, 1728 * 640 * 2, 0x00020000);
    const unsigned sb = ldsb + L_B + w * 5120;
    if (it0k == 0) dma_item(ro, srcb[0], sb); else if (it0k == 1) dma_item(r1, srcb[0], sb); else dma_item(r2, srcb[0], sb);
    dma_item(r1, srcb[1], sb + 1024); dma_item(r1, srcb[2], sb + 2048); dma_item(r2, srcb[3], sb + 3072); dma_item(r2, srcb[4], sb + 4096);
  }
  {
    float f[20];
#pragma unroll
    for (int i = 0; i < 5; ++i) { f[4 * i] = blo(xv[i].x); f[4 * i + 1] = bhi(xv[i].x); f[4 * i + 2] = blo(xv[i].y); f[4 * i + 3] = bhi(xv[i].y); }
    float s4[4];
#pragma unroll
    for (int j = 0; j < 4; ++j) s4[j] = (f[j] + f[4 + j]) + (f[8 + j] + f[12 + j]) + f[16 + j];
    const float mean = sum_x16(sum16((s4[0] + s4[1]) + (s4[2] + s4[3]))) * (1.f / 640.f);
#pragma unroll
    for (int i = 0; i < 20; ++i) f[i] -= mean;
#pragma unroll
    for (int j = 0; j < 4; ++j) s4[j] = (f[j] * f[j] + f[4 + j] * f[4 + j]) + (f[8 + j] * f[8 + j] + f[12 + j] * f[12 + j]) + f[16 + j] * f[16 + j];
    const float rstd = __builtin_amdgcn_rsqf(sum_x16(sum16((s4[0] + s4[1]) + (s4[2] + s4[3]))) * (1.f / 640.f) + 1e-5f);
    LAS char* sc = lds + L_A + ((lane & 31) >> 1) * 128 + (2 * w + (lane >> 5)) * 16 + (lane & 1) * 8;
#pragma unroll
    for (int i = 0; i < 5; ++i)
      *(LAS v2u*)(sc + i * 2048) = v2u{pk2(f[4 * i] * rstd, f[4 * i + 1] * rstd), pk2(f[4 * i + 2] * rstd, f[4 * i + 3] * rstd)};
  }
  STAMP(24);
  asm volatile("s_waitcnt vmcnt(5)" ::: "memory");
  asm volatile("" :: "v"(warm0), "v"(warm1));
  BAR();
  STAMP(25);
#pragma unroll
  for (int i = 0; i < 3; ++i)
    if (i < 2 || lane < 32) st128(*(LAS v4u*)(lds + L_A + (160 * w + 64 * i + lane) * 16), ws, dxa[i], 0);
  STAMP(32);
#pragma unroll
  for (int k = 0; k < 3; ++k) {
    const int q = w + 4 * k;
    if (q < 10) {
      const v4u o = item_read(lds + L_A + 16384 + q * 1024, lane);
      if (64 * (q & 1) + lane < 120) st128(o, ws, dwa[k], 0);
    }
  }
  STAMP(33);
  const unsigned tag = __builtin_amdgcn_readfirstlane(epoch) * 2654435761u + 0x9E3779B9u;
  STAMP(26);
  DRAIN();
  BAR();
  if (tid == 0) __hip_atomic_store(flg + (0 * 256 + cuid) * 32, tag, RLX);
  STAMP(1);
  *(LAS v4u*)(lds + L_IMG + tid * 16) = v4u{0u, 0u, 0u, 0u};
#pragma unroll
  for (int k = 0; k < 5; ++k) st128(item_read(lds + L_B + w * 5120 + k * 1024, lane), ws, dstb[k], 0);
  STAMP(2);

  if (w == 0) {
    const int l = lane & 15;
    wait_flag(flg + (0 * 256 + (lane < 16 ? P * 16 + l : l * 16 + m)) * 32, tag, lane < 32);
  }
  BAR();
  STAMP(3);
  v4u fb;
  v4u xn[2][20];
  f32x4 akv[2][5];
  v4u wqs[13];
  {
    constexpr int D1 = 5;
    for (int q = w; q < 25; q += 4) dma4(ws, l16, OFF_WQKV + m * WQKV_STR + q * 4096, ldsb + L_A + q * 4096);
    const unsigned xb = OFF_XN + (8 * P + 2 * w) * 20 * 1024;
#pragma unroll
    for (int ks = 0; ks < D1; ++ks) {
#pragma unroll
      for (int u = 0; u < 2; ++u) xn[u][ks] = LDF(xb, u * 20 + ks);
      SCHED();
    }
    asm volatile("s_waitcnt vmcnt(10)" ::: "memory");
    BAR();
    if (tid == 0) __hip_atomic_store((gu32*)(wsp + OFF_FLAGB) + cuid, tag, RLX);
    STAMP(17);
#pragma unroll
    for (int u = 0; u < 2; ++u)
#pragma unroll
      for (int n = 0; n < 5; ++n) akv[u][n] = f32x4{0.f, 0.f, 0.f, 0.f};
    v4u wf[2][5];
#pragma unroll
    for (int n = 0; n < 5; ++n) wf[0][n] = LDSF(L_A + (n * 20) * 1024);
    SCHED();
    const unsigned wqb = OFF_WQKV + m * WQKV_STR + 102400 + w * 1024;
#pragma unroll
    for (int ks = 0; ks < 20; ++ks) {
      if (ks + D1 < 20) {
#pragma unroll
        for (int u = 0; u < 2; ++u) xn[u][ks + D1] = LDF(xb, u * 20 + ks + D1);
      } else {
#pragma unroll
        for (int i = (ks + D1 - 20) * 3; i < (ks + D1 - 20) * 3 + 3 && i < 13; ++i) wqs[i] = ld128(ws, l16, wqb + (i < 12 || w < 2 ? i : 11) * 4096);
        if (ks == 19) fb = __builtin_amdgcn_raw_buffer_load_b128(ws, OFF_FLAGB + l16, 0, AUX_SC1);
      }
      if (ks + 1 < 20) {
#pragma unroll
        for (int n = 0; n < 5; ++n) wf[(ks + 1) & 1][n] = LDSF(L_A + (n * 20 + ks + 1) * 1024);
      }
#pragma unroll
      for (int n = 0; n < 5; ++n)
#pragma unroll
        for (int u = 0; u < 2; ++u) akv[u][n] = mfma(xn[u][ks], wf[ks & 1][n], akv[u][n]);
#pragma unroll
      for (int n = 0; n < 5; ++n) { SGB(0x100, 1); SGB(0x8, 1); if (n < 3) SGB(0x20, 1); SGB(0x8, 1); }
      SCHED();
    }
  }
  STAMP(4);
  if (!__all(fb.x == tag && fb.y == tag && fb.z == tag && fb.w == tag)) {
    unsigned sp = 0;
    for (;;) {
      asm volatile("" ::: "memory");
      fb = __builtin_amdgcn_raw_buffer_load_b128(ws, OFF_FLAGB + l16, 0, AUX_SC1);
      if (__all(fb.x == tag && fb.y == tag && fb.z == tag && fb.w == tag)) break;
      if (++sp > SPIN_CAP) break;
    }
  }
  v4u w2f[14][5];
  const int k4s = w < 2 ? 14 * w : 28 + 13 * (w - 2);
  const unsigned w2b = OFF_W2 + ct * W2_STR + k4s * 5 * 1024;
#define W2LD(f0, f1) _Pragma("unroll") for (int f_ = (f0); f_ < (f1); ++f_) w2f[f_ / 5][f_ % 5] = LDF(w2b, f_); SCHED()
  W2LD(0, 12);
  BAR();
#pragma unroll
  for (int u = 0; u < 2; ++u)
#pragma unroll
    for (int n = 0; n < 5; ++n) {
      f32x4 a = akv[u][n];
      if (n < 2) { a[0] = phi(a[0]); a[1] = phi(a[1]); a[2] = phi(a[2]); a[3] = phi(a[3]); }
      else if (n == 2) { if (t16 < 8) { a[0] = phi(a[0]); a[1] = phi(a[1]); a[2] = phi(a[2]); a[3] = phi(a[3]); } }
      *(LAS v2u*)(lds + L_A + (16 * n + t16) * EP + (32 * w + 16 * u + 4 * g) * 2) = v2u{pk2(a[0], a[1]), pk2(a[2], a[3])};
    }
  if (tid < 16) *(LAS v4u*)(lds + L_A + 80 * EP + tid * 16) = v4u{0x3F803F80u, 0x3F803F80u, 0x3F803F80u, 0x3F803F80u};
  BAR();
  W2LD(12, 24);
  STAMP(27);
  {
    const int hd = w >> 1, dt = w & 1;
    f32x4 t0 = {0.f, 0.f, 0.f, 0.f}, t1 = t0;
    const unsigned ra = L_A + (20 * hd + 16 * dt + t16) * EP + g * 16;
    const unsigned rb0 = L_A + (40 + 20 * hd + t16) * EP + g * 16;
    const unsigned rb1 = L_A + (t16 < 4 ? 56 + 20 * hd + t16 : 80) * EP + g * 16;
    v4u fa[4], f0[4], f1[4];
#pragma unroll
    for (int s = 0; s < 4; ++s) { fa[s] = *(LAS v4u*)(lds + ra + s * 64); f0[s] = *(LAS v4u*)(lds + rb0 + s * 64); f1[s] = *(LAS v4u*)(lds + rb1 + s * 64); }
#pragma unroll
    for (int s = 0; s < 4; ++s) { t0 = mfma(fa[s], f0[s], t0); t1 = mfma(fa[s], f1[s], t1); }
    LAS char* pt = lds + L_PT + hd * 1680;
    if (dt == 0) {
      *(LAS f32x4*)(pt + l16) = t0;
      if (t16 < 5) *(LAS f32x4*)(pt + 1024 + (g * 5 + t16) * 16) = t1;
    } else if (g == 0) {
      *(LAS f32x4*)(pt + 1344 + t16 * 16) = t0;
      if (t16 < 5) *(LAS f32x4*)(pt + 1600 + t16 * 16) = t1;
    }
    if (tid >= 250) *(LAS v4u*)(lds + L_PT + 3360 + (tid - 250) * 16) = v4u{0u, 0u, 0u, 0u};
  }
  BAR();
  if (tid < 216) st128(*(LAS v4u*)(lds + L_PT + tid * 16), ws, tid * 16, OFF_PART + (m * 16 + P) * PART_STR);
#pragma unroll
  for (int i = 0; i < 13; ++i)
    if (i < 12 || w < 2) *(LAS v4u*)(lds + L_B + (w + 4 * i) * 1024 + l16) = wqs[i];
  STAMP(28);
  DRAIN();
  BAR();
  if (tid == 0) __hip_atomic_store(flg + (1 * 256 + cuid) * 32, tag, RLX);
  STAMP(5);

  unsigned qp[2][3][2];
  {
    f32x4 aq[2][3];
#pragma unroll
    for (int u = 0; u < 2; ++u)
#pragma unroll
      for (int n = 0; n < 3; ++n) aq[u][n] = f32x4{0.f, 0.f, 0.f, 0.f};
    LAS char* lq7 = lds + L_B + 40960 + g * 128 + (t16 & 7) * 16;
    v4u wf[3][3];
#pragma unroll
    for (int k = 0; k < 2; ++k) { wf[k][0] = LDSF(L_B + k * 1024); wf[k][1] = LDSF(L_B + (20 + k) * 1024); wf[k][2] = *(LAS v4u*)(lq7 + k * 512); }
    SCHED();
#pragma unroll
    for (int ks = 0; ks < 20; ++ks) {
      if (ks % 3 == 0) {
        const int q = w + 4 * (ks / 3);
        if (q < 25) dma4(ws, l16, OFF_WO + ct * 102400 + q * 4096, ldsb + L_A + q * 4096);
      }
      if (ks + 2 < 20) {
        wf[(ks + 2) % 3][0] = LDSF(L_B + (ks + 2) * 1024);
        wf[(ks + 2) % 3][1] = LDSF(L_B + (20 + ks + 2) * 1024);
        wf[(ks + 2) % 3][2] = *(LAS v4u*)(lq7 + (ks + 2) * 512);
      }
#pragma unroll
      for (int n = 0; n < 3; ++n)
#pragma unroll
        for (int u = 0; u < 2; ++u) aq[u][n] = mfma(wf[ks % 3][n], xn[u][ks], aq[u][n]);
#pragma unroll
      for (int n = 0; n < 3; ++n) { SGB(0x100, 1); SGB(0x8, 2); }
      SCHED();
    }
#pragma unroll
    for (int u = 0; u < 2; ++u)
#pragma unroll
      for (int n = 0; n < 3; ++n) {
        qp[u][n][0] = pk2(phi(aq[u][n][0]), phi(aq[u][n][1]));
        qp[u][n][1] = pk2(phi(aq[u][n][2]), phi(aq[u][n][3]));
      }
  }
  W2LD(24, 34);
  unsigned imgoff, imgoffz;
  {
    const int hd = tid >= 105 ? 1 : 0, u = tid - 105 * hd;
    int dq, e;
    if (u < 64) { dq = u >> 4; e = u & 15; }
    else if (u < 84) { const int v = u - 64; dq = v / 5; e = 16 + v - 5 * dq; }
    else if (u < 100) { dq = 4; e = u - 84; }
    else { dq = 4; e = 16 + u - 100; }
    int gq;
    if (hd == 0) gq = dq < 4 ? dq : 0; else gq = dq < 3 ? dq + 1 : (dq == 3 ? 0 : 1);
    const int j0 = dq < 4 ? 0 : 4, ge = e / 5, re = e - 5 * ge;
    const unsigned ib = L_IMG + hd * 2048 + gq * 256 + j0 * 2;
    imgoff = ib + (re < 4 ? (4 * ge + re) * 16 : 1024 + (4 * ge) * 16);
    imgoffz = e == 20 ? ib + 1024 + 16 : 0u;
  }
  STAMP(6);

  if (w == 0) wait_flag(flg + (1 * 256 + (lane & 15) * 16 + m) * 32, tag, lane < 16);
  BAR();
  STAMP(7);
  if (tid < 210) {
    v4u pv[16];
#pragma unroll
    for (int i = 0; i < 16; ++i) pv[i] = ld128(ws, tid * 16, OFF_PART + (m * 16 + i) * PART_STR);
    f32x4 s[4];
#pragma unroll
    for (int i = 0; i < 4; ++i)
      s[i] = (__builtin_bit_cast(f32x4, pv[4 * i]) + __builtin_bit_cast(f32x4, pv[4 * i + 1])) +
             (__builtin_bit_cast(f32x4, pv[4 * i + 2]) + __builtin_bit_cast(f32x4, pv[4 * i + 3]));
    const f32x4 st = (s[0] + s[1]) + (s[2] + s[3]);
    const v2u val = v2u{pk2(st[0], st[1]), pk2(st[2], st[3])};
    if (imgoffz == 0u) *(LAS v2u*)(lds + imgoff) = val;
    else {
#pragma unroll
      for (int ge = 0; ge < 4; ++ge) *(LAS v2u*)(lds + imgoffz + ge * 64) = val;
    }
  }
  BAR();
  STAMP(18);
  {
    v4u ka[2][2];
#pragma unroll
    for (int hd = 0; hd < 2; ++hd)
#pragma unroll
      for (int bb = 0; bb < 2; ++bb) ka[hd][bb] = *(LAS v4u*)(lds + L_IMG + hd * 2048 + bb * 1024 + l16);
#pragma unroll
    for (int u = 0; u < 2; ++u) {
      v4u qa, qb;
      qa.x = qp[u][0][0]; qa.y = qp[u][0][1]; qa.z = g == 0 ? qp[u][1][0] : 0u; qa.w = g == 0 ? qp[u][1][1] : 0u;
      qb.x = g == 0 ? qp[u][2][0] : qp[u][1][0]; qb.y = g == 0 ? qp[u][2][1] : qp[u][1][1];
      qb.z = g == 1 ? qp[u][2][0] : 0u; qb.w = g == 1 ? qp[u][2][1] : 0u;
      const f32x4 z4 = {0.f, 0.f, 0.f, 0.f};
      const f32x4 a0 = mfma(ka[0][0], qa, z4), a1 = mfma(ka[0][1], qa, z4);
      const f32x4 b0 = mfma(ka[1][0], qb, z4), b1 = mfma(ka[1][1], qb, z4);
      const float ra = __builtin_amdgcn_rcpf(a1[1] + 1e-6f), rb = __builtin_amdgcn_rcpf(b1[1] + 1e-6f);
      const unsigned d0 = pk2(a0[0] * ra, a0[1] * ra), d1 = pk2(a0[2] * ra, a0[3] * ra), d2 = pk2(a1[0] * ra, b0[0] * rb),
                     d3 = pk2(b0[1] * rb, b0[2] * rb), d4 = pk2(b0[3] * rb, b1[0] * rb);
      const unsigned oi = (8 * P + 2 * w + u) * 16 + m;
      st128(v4u{d0, d1, d2, d3}, ws, l16, OFF_OM + oi * 1024);
      __builtin_amdgcn_raw_buffer_store_b32(d4, ws, lane * 4, OFF_OR + oi * 256, AUX_SC1);
    }
  }
  DRAIN();
  BAR();
  if (tid == 0) __hip_atomic_store(flg + (2 * 256 + cuid) * 32, tag, RLX);
  STAMP(8);
  for (int q = w; q < 12; q += 4) dma4(ws, l16, OFF_W1 + m * W1_STR + q * 4096, ldsb + L_B + q * 4096);
  if (w == 0) dma1(ws, l16, OFF_W1 + m * W1_STR + 49152, ldsb + L_B + 49152);
  v2u xr[5];
  {
    const GAS char* xp = (const GAS char*)x + (size_t)(64 * (2 * P + jl) + 16 * w + t16) * 1280 + (80 * ct + 4 * g) * 2;
#pragma unroll
    for (int n = 0; n < 5; ++n) xr[n] = *(const GAS v2u*)(xp + n * 32);
  }
  v4u w1r[23];
  const unsigned w1rb = OFF_W1 + m * W1_STR + 50176 + w * 1024;
#pragma unroll
  for (int i = 0; i < 12; ++i) w1r[i] = ld128(ws, l16, w1rb + i * 4096);
  SCHED();

  STAMP(29);
  if (w == 0) wait_flag(flg + (2 * 256 + P * 16 + (lane & 15)) * 32, tag, lane < 16);
  STAMP(30);
  BAR();
  STAMP(9);
  unsigned hp[5][2];
  {
    constexpr int D2 = 8;
    v4u of[20];
    const unsigned ob = OFF_OM + (8 * P + 4 * jl + w) * 16 * 1024, orb = OFF_OR + (8 * P + 4 * jl + w) * 16 * 256;
#define LDO(ks) ((ks) < 16 ? LDF(ob, ks) : v4u{ld32(ws, lane * 4 + (4 * ((ks) - 16)) * 256, orb), ld32(ws, lane * 4 + (4 * ((ks) - 16) + 1) * 256, orb), \
                                              ld32(ws, lane * 4 + (4 * ((ks) - 16) + 2) * 256, orb), ld32(ws, lane * 4 + (4 * ((ks) - 16) + 3) * 256, orb)})
#pragma unroll
    for (int ks = 0; ks < D2; ++ks) { of[ks] = LDO(ks); SCHED(); }
    f32x4 ah[5];
#pragma unroll
    for (int n = 0; n < 5; ++n) ah[n] = f32x4{0.f, 0.f, 0.f, 0.f};
    v4u wf[3][5];
#pragma unroll
    for (int n = 0; n < 5; ++n) { wf[0][n] = LDSF(L_A + n * 1024); wf[1][n] = LDSF(L_A + (5 + n) * 1024); }
    SCHED();
#pragma unroll
    for (int ks = 0; ks < 20; ++ks) {
      if (ks + D2 < 20) of[ks + D2] = LDO(ks + D2);
      else {
        const int i0 = ks < 15 ? 12 + 2 * (ks - 12) : 15 + (ks - 12);
        w1r[i0] = ld128(ws, l16, w1rb + (i0 < 22 || w < 3 ? i0 : 21) * 4096);
        if (ks < 15) w1r[i0 + 1] = ld128(ws, l16, w1rb + (i0 + 1) * 4096);
      }
      if (ks + 2 < 20) {
#pragma unroll
        for (int n = 0; n < 5; ++n) wf[(ks + 2) % 3][n] = LDSF(L_A + ((ks + 2) * 5 + n) * 1024);
      }
#pragma unroll
      for (int n = 0; n < 5; ++n) ah[n] = mfma(wf[ks % 3][n], of[ks], ah[n]);
#pragma unroll
      for (int n = 0; n < 5; ++n) { SGB(0x100, 1); SGB(0x8, 1); if (n < 4) SGB(0x20, 1); }
      SCHED();
    }
    STAMP(19);
    float hv[20];
#pragma unroll
    for (int n = 0; n < 5; ++n) {
      hp[n][0] = pk2(blo(xr[n].x) + ah[n][0], bhi(xr[n].x) + ah[n][1]);
      hp[n][1] = pk2(blo(xr[n].y) + ah[n][2], bhi(xr[n].y) + ah[n][3]);
    }
    const unsigned hi = (8 * P + 4 * jl + w) * 8 + ct;
    st128(v4u{hp[0][0], hp[0][1], hp[1][0], hp[1][1]}, ws, l16, OFF_HF + hi * 2048);
    st128(v4u{hp[2][0], hp[2][1], hp[3][0], hp[3][1]}, ws, l16 + 1024, OFF_HF + hi * 2048);
    st64(v2u{hp[4][0], hp[4][1]}, ws, lane * 8, OFF_HH + hi * 512);
    float s4[5];
#pragma unroll
    for (int n = 0; n < 5; ++n) {
      hv[4 * n] = blo(hp[n][0]); hv[4 * n + 1] = bhi(hp[n][0]); hv[4 * n + 2] = blo(hp[n][1]); hv[4 * n + 3] = bhi(hp[n][1]);
      s4[n] = (hv[4 * n] + hv[4 * n + 1]) + (hv[4 * n + 2] + hv[4 * n + 3]);
    }
    const float mi = sum_g((s4[0] + s4[1]) + (s4[2] + s4[3]) + s4[4]) * (1.f / 80.f);
#pragma unroll
    for (int n = 0; n < 5; ++n) {
      const float a = hv[4 * n] - mi, bq = hv[4 * n + 1] - mi, c = hv[4 * n + 2] - mi, d = hv[4 * n + 3] - mi;
      s4[n] = (a * a + bq * bq) + (c * c + d * d);
    }
    const float q = sum_g((s4[0] + s4[1]) + (s4[2] + s4[3]) + s4[4]);
    if (g == 0) st64(v2u{asu(mi), asu(q)}, ws, t16 * 8, OFF_ST + ((8 * P + 4 * jl + w) * 8 + ct) * 128);
  }
  DRAIN();
  BAR();
  if (tid == 0) __hip_atomic_store(flg + (3 * 256 + cuid) * 32, tag, RLX);
  STAMP(10);
#pragma unroll
  for (int i = 0; i < 23; ++i)
    if (i < 22 || w < 3) *(LAS v4u*)(lds + L_A + (w + 4 * i) * 1024 + l16) = w1r[i];
  W2LD(34, 46);
  BAR();
  {
    f32x4 cs[7];
#pragma unroll
    for (int n = 0; n < 7; ++n) cs[n] = f32x4{0.f, 0.f, 0.f, 0.f};
    const v4u ones = {0x3F803F80u, 0x3F803F80u, 0x3F803F80u, 0x3F803F80u};
    v4u wf[2][7];
#pragma unroll
    for (int n = 0; n < 7; ++n) wf[0][n] = *(LAS v4u*)(lb0 + L_B + w * 7168 + n * 1024);
#pragma unroll
    for (int i = 0; i < 5; ++i) {
      if (i + 1 < 5) {
        const int ks = w + 4 * (i + 1);
        const unsigned a = ks < 7 ? L_B + ks * 7168 : L_A + (ks - 7) * 7168;
#pragma unroll
        for (int n = 0; n < 7; ++n) wf[(i + 1) & 1][n] = *(LAS v4u*)(lb0 + a + n * 1024);
      }
#pragma unroll
      for (int n = 0; n < 7; ++n) cs[n] = mfma(wf[i & 1][n], ones, cs[n]);
    }
    if (t16 == 0) {
#pragma unroll
      for (int n = 0; n < 7; ++n) *(LAS f32x4*)(lds + L_CS + (w * 7 + n) * 64 + g * 16) = cs[n];
    }
    BAR();
    if (lane < 8 && w + 4 * (lane >> 2) < 7) {
      const int n = w + 4 * (lane >> 2), gq = lane & 3;
      f32x4 cw[4];
#pragma unroll
      for (int ww = 0; ww < 4; ++ww) cw[ww] = *(LAS f32x4*)(lds + L_CS + (ww * 7 + n) * 64 + gq * 16);
      *(LAS f32x4*)(lds + L_IMG + n * 64 + gq * 16) = (cw[0] + cw[1]) + (cw[2] + cw[3]);
    }
  }
  STAMP(20);

  if (w == 0) wait_flag(flg + (3 * 256 + P * 16 + (lane & 15)) * 32, tag, lane < 16);
  BAR();
  STAMP(11);
  {
    constexpr int D3 = 5;
    v4u hf[2][20];
    const unsigned hb = OFF_HF + (8 * P + 2 * w) * 16 * 1024, hhb = OFF_HH + (8 * P + 2 * w) * 8 * 512;
    auto ldh = [&](int u, int ks) -> v4u {
      if (ks < 16) return LDF(hb, u * 16 + ks);
      const v2u a = ld64(ws, lane * 8 + (2 * (ks - 16)) * 512, hhb + u * 4096), c = ld64(ws, lane * 8 + (2 * (ks - 16) + 1) * 512, hhb + u * 4096);
      return v4u{a.x, a.y, c.x, c.y};
    };
    v2u stv[2][2];
#pragma unroll
    for (int ks = 0; ks < D3; ++ks) {
#pragma unroll
      for (int u = 0; u < 2; ++u) hf[u][ks] = ldh(u, ks);
      SCHED();
    }
#pragma unroll
    for (int u = 0; u < 2; ++u)
#pragma unroll
      for (int i = 0; i < 2; ++i) stv[u][i] = ld64(ws, t16 * 8 + (2 * g + i) * 128, OFF_ST + (8 * P + 2 * w + u) * 1024);
    f32x4 a3[2][7];
#pragma unroll
    for (int u = 0; u < 2; ++u)
#pragma unroll
      for (int n = 0; n < 7; ++n) a3[u][n] = f32x4{0.f, 0.f, 0.f, 0.f};
    v4u wf[2][7];
#pragma unroll
    for (int n = 0; n < 7; ++n) wf[0][n] = LDSF(W1A(0, n));
    SCHED();
#pragma unroll
    for (int ks = 0; ks < 20; ++ks) {
      if (ks + D3 < 20) {
#pragma unroll
        for (int u = 0; u < 2; ++u) hf[u][ks + D3] = ldh(u, ks + D3);
      }
      w2f[(46 + ks) / 5][(46 + ks) % 5] = LDF(w2b, 46 + ks);
      if (ks + 1 < 20) {
#pragma unroll
        for (int n = 0; n < 7; ++n) wf[(ks + 1) & 1][n] = LDSF(W1A(ks + 1, n));
      }
#pragma unroll
      for (int n = 0; n < 7; ++n)
#pragma unroll
        for (int u = 0; u < 2; ++u) a3[u][n] = mfma(wf[ks & 1][n], hf[u][ks], a3[u][n]);
#pragma unroll
      for (int n = 0; n < 7; ++n) { SGB(0x100, 1); SGB(0x8, 1); if (n < 5) SGB(0x20, 1); SGB(0x8, 1); }
      SCHED();
    }
    STAMP(12);
    W2LD(66, 70);
    float mean[2], rstd[2];
#pragma unroll
    for (int u = 0; u < 2; ++u) {
      const float m0 = asf(stv[u][0].x), q0 = asf(stv[u][0].y), m1 = asf(stv[u][1].x), q1 = asf(stv[u][1].y);
      const float mu = sum_g(m0 + m1) * 0.125f;
      const float M2 = sum_g(q0 + q1 + 80.f * ((m0 - mu) * (m0 - mu) + (m1 - mu) * (m1 - mu)));
      rstd[u] = __builtin_amdgcn_rsqf(M2 * (1.f / 640.f) + 1e-5f);
      mean[u] = mu * rstd[u];
    }
    unsigned hq[2][7][2];
#pragma unroll
    for (int n = 0; n < 7; ++n) {
      const f32x4 cs = *(LAS f32x4*)(lds + L_IMG + n * 64 + g * 16);
#pragma unroll
      for (int u = 0; u < 2; ++u) {
        const float v0 = fmaxf(a3[u][n][0] * rstd[u] - mean[u] * cs[0], 0.f), v1 = fmaxf(a3[u][n][1] * rstd[u] - mean[u] * cs[1], 0.f);
        const float v2 = fmaxf(a3[u][n][2] * rstd[u] - mean[u] * cs[2], 0.f), v3 = fmaxf(a3[u][n][3] * rstd[u] - mean[u] * cs[3], 0.f);
        hq[u][n][0] = pk2(v0, v1); hq[u][n][1] = pk2(v2, v3);
      }
    }
#pragma unroll
    for (int u = 0; u < 2; ++u) {
      const unsigned di = (8 * P + 2 * w + u) * 16 + m;
      st128(v4u{hq[u][0][0], hq[u][0][1], hq[u][1][0], hq[u][1][1]}, ws, l16, OFF_DF + di * 3072);
      st128(v4u{hq[u][2][0], hq[u][2][1], hq[u][3][0], hq[u][3][1]}, ws, l16 + 1024, OFF_DF + di * 3072);
      st128(v4u{hq[u][4][0], hq[u][4][1], hq[u][5][0], hq[u][5][1]}, ws, l16 + 2048, OFF_DF + di * 3072);
      if ((m & 7) < 6) st64(v2u{hq[u][6][0], hq[u][6][1]}, ws, lane * 8, OFF_DH + di * 512);
    }
  }
  STAMP(21);
  DRAIN();
  BAR();
  if (tid == 0) {
    __hip_atomic_store(flg + (4 * 256 + cuid) * 32, tag, RLX);
    if (cuid == 0) __hip_atomic_store((gu32*)(wsp + OFF_EPOCH), epoch + 1u, RLX);
  }
  STAMP(13);
  if (w == 0) wait_flag(flg + (4 * 256 + P * 16 + (lane & 15)) * 32, tag, lane < 16);
  BAR();
  STAMP(14);
  {
    f32x4 a4[4][5];
#pragma unroll
    for (int u = 0; u < 4; ++u)
#pragma unroll
      for (int n = 0; n < 5; ++n) a4[u][n] = f32x4{0.f, 0.f, 0.f, 0.f};
    const unsigned hb = OFF_DF + ((8 * P + 4 * jl) * 48 + 12 * w) * 1024, dhb = OFF_DH + (8 * P + 4 * jl) * 16 * 512;
    const int ci0 = w < 2 ? 2 * w : w + 2;
    auto lda = [&](int u, int k) -> v4u {
      if (k < 12) return ld128(ws, l16 + (k & 3) * 1024, hb + u * 48 * 1024 + (k >> 2) * 4096);
      const int ci = ci0 + (k - 12), h2 = ci >= 3 ? 1 : 0, tA = 8 * h2 + 2 * (ci - 3 * h2);
      const v2u a = ld64(ws, lane * 8, dhb + (u * 16 + tA) * 512), c = ld64(ws, lane * 8 + 512, dhb + (u * 16 + tA) * 512);
      return v4u{a.x, a.y, c.x, c.y};
    };
    constexpr int DP = 3;
    v4u af[DP + 1][4];
#pragma unroll
    for (int k = 0; k < DP; ++k)
#pragma unroll
      for (int u = 0; u < 4; ++u) { af[k][u] = lda(u, k); SCHED(); }
#pragma unroll
    for (int k = 0; k < 14; ++k) {
      if (k + DP < 14) {
        if (k + DP < 13 || w < 2) {
#pragma unroll
          for (int u = 0; u < 4; ++u) af[(k + DP) % (DP + 1)][u] = lda(u, k + DP);
        }
      }
      if (k < 13 || w < 2) {
#pragma unroll
        for (int u = 0; u < 4; ++u)
#pragma unroll
          for (int n = 0; n < 5; ++n) a4[u][n] = mfma(w2f[k][n], af[k % (DP + 1)][u], a4[u][n]);
      }
#pragma unroll
      for (int u = 0; u < 4; ++u) { SGB(0x20, 2); SGB(0x8, 5); }
      SCHED();
    }
    STAMP(15);
#pragma unroll
    for (int u = 0; u < 4; ++u) {
      if (u != w) {
        const int si = w < u ? w : w - 1;
#pragma unroll
        for (int n = 0; n < 5; ++n) *(LAS f32x4*)(lds + L_A + ((u * 3 + si) * 5 + n) * 1024 + l16) = a4[u][n];
      }
    }
    BAR();
    GAS char* op = (GAS char*)out + (size_t)(64 * (2 * P + jl) + 16 * w + t16) * 1280 + (80 * ct + 4 * g) * 2;
#pragma unroll
    for (int n = 0; n < 5; ++n) {
      f32x4 v = w == 0 ? a4[0][n] : (w == 1 ? a4[1][n] : (w == 2 ? a4[2][n] : a4[3][n]));
      f32x4 p[3];
#pragma unroll
      for (int si = 0; si < 3; ++si) p[si] = *(LAS f32x4*)(lds + L_A + ((w * 3 + si) * 5 + n) * 1024 + l16);
      v = (v + p[0]) + (p[1] + p[2]);
      const v2u o = {pk2(blo(hp[n][0]) + v[0], bhi(hp[n][0]) + v[1]), pk2(blo(hp[n][1]) + v[2], bhi(hp[n][1]) + v[3])};
      *(GAS v2u*)(op + n * 32) = o;
    }
  }
  STAMP(16);
}

extern "C" void kernel_launch(void* const* d_in, const int* in_sizes, int n_in, void* d_out, int out_size, void* d_ws, size_t ws_size,
                              hipStream_t stream) {
  (void)in_sizes; (void)n_in; (void)out_size; (void)ws_size;
  hipFuncSetAttribute((const void*)fused_block, hipFuncAttributeMaxDynamicSharedMemorySize, LDS_BYTES);
  fused_block<<<256, 256, LDS_BYTES, stream>>>((const unsigned short*)d_in[0], (const unsigned short*)d_in[1], (const unsigned short*)d_in[2],
                                               (const unsigned short*)d_in[4], (const unsigned short*)d_in[5], (unsigned short*)d_out,
                                               (char*)d_ws);
}
```

```cpp
#include <hip/hip_runtime.h>
#include <hip/hip_bf16.h>
#include <stdint.h>

typedef unsigned v4u __attribute__((ext_vector_type(4)));
typedef unsigned v3u __attribute__((ext_vector_type(3)));
typedef unsigned v2u __attribute__((ext_vector_type(2)));
typedef float f32x4 __attribute__((ext_vector_type(4)));
typedef short bf16x8 __attribute__((ext_vector_type(8)));
typedef short s16x4 __attribute__((ext_vector_type(4)));
typedef __bf16 bf2 __attribute__((ext_vector_type(2)));
typedef __amdgpu_buffer_rsrc_t rsrc_t;
#define GAS __attribute__((address_space(1)))
#define LAS __attribute__((address_space(3)))
#define DI __device__ __forceinline__
#define RLX __ATOMIC_RELAXED, __HIP_MEMORY_SCOPE_AGENT
typedef GAS unsigned gu32;

#define TS 0
#define CODEPF 1
#define PRETOUCH 1
constexpr int CODE_LINES = 290;

constexpr unsigned OFF_EPOCH = 0;
constexpr unsigned OFF_FLAG = 4096;
constexpr unsigned OFF_FLAGB = OFF_FLAG + 5 * 256 * 128;
constexpr unsigned OFF_TS = 65 * 1024 * 1024;
constexpr unsigned OFF_TS2 = 64 * 1024 * 1024;
constexpr unsigned OFF_XN = 256 * 1024;
constexpr unsigned WQKV_STR = 153600;
constexpr unsigned OFF_WQKV = OFF_XN + 128 * 20 * 1024;
constexpr unsigned OFF_WO = OFF_WQKV + 16 * WQKV_STR;
constexpr unsigned W1_STR = 20 * 7 * 1024;
constexpr unsigned OFF_W1 = OFF_WO + 8 * 102400;
constexpr unsigned W2_STR = 54 * 5 * 1024;
constexpr unsigned OFF_W2 = OFF_W1 + 16 * W1_STR;
constexpr unsigned PART_STR = 3456;
constexpr unsigned OFF_PART = OFF_W2 + 8 * W2_STR;
constexpr unsigned OFF_OM = OFF_PART + 256 * PART_STR;
constexpr unsigned OFF_OR = OFF_OM + 128 * 16 * 1024;
constexpr unsigned OFF_HF = OFF_OR + 128 * 16 * 256;
constexpr unsigned OFF_HH = OFF_HF + 128 * 16 * 1024;
constexpr unsigned OFF_ST = OFF_HH + 128 * 8 * 512;
constexpr unsigned OFF_DF = OFF_ST + 128 * 8 * 128;
constexpr unsigned OFF_DH = OFF_DF + 128 * 48 * 1024;
constexpr unsigned WS_BYTES = OFF_DH + 128 * 16 * 512;

constexpr unsigned L_A = 0;
constexpr unsigned L_B = 102400;
constexpr unsigned L_M = 153600;
constexpr unsigned L_IMG = L_M;
constexpr unsigned L_CS = L_M + 4096;
constexpr unsigned L_PT = L_M + 6144;
constexpr unsigned LDS_BYTES = 163840;
constexpr unsigned EP = 272;

constexpr int AUX_SC1 = 16;
constexpr unsigned SPIN_CAP = 1u << 20;

DI f32x4 mfma(v4u a, v4u b, f32x4 c) {
  return __builtin_amdgcn_mfma_f32_16x16x32_bf16(__builtin_bit_cast(bf16x8, a), __builtin_bit_cast(bf16x8, b), c, 0, 0, 0);
}
DI unsigned pk2(float a, float b) { bf2 r = {(__bf16)a, (__bf16)b}; return __builtin_bit_cast(unsigned, r); }
DI float asf(unsigned u) { return __builtin_bit_cast(float, u); }
DI unsigned asu(float f) { return __builtin_bit_cast(unsigned, f); }
DI float blo(unsigned u) { return asf(u << 16); }
DI float bhi(unsigned u) { return asf(u & 0xffff0000u); }
DI float phi(float t) { return (t > 0.f ? t + 1.f : __expf(t)) + 1e-6f; }
template <int C> DI float dpp_add(float v) { return v + asf((unsigned)__builtin_amdgcn_update_dpp(0, (int)asu(v), C, 0xF, 0xF, true)); }
DI float sum16(float v) { v = dpp_add<0xB1>(v); v = dpp_add<0x4E>(v); v = dpp_add<0x141>(v); v = dpp_add<0x140>(v); return v; }
DI float sum_x16(float v) { const unsigned u = asu(v); auto r = __builtin_amdgcn_permlane16_swap(u, u, false, false); return asf(r[0]) + asf(r[1]); }
DI float sum_x32(float v) { const unsigned u = asu(v); auto r = __builtin_amdgcn_permlane32_swap(u, u, false, false); return asf(r[0]) + asf(r[1]); }
DI float sum_g(float v) { return sum_x32(sum_x16(v)); }
#define SCHED() __builtin_amdgcn_sched_barrier(0)
#define SGB(mask, n) __builtin_amdgcn_sched_group_barrier(mask, n, 0)
#define DRAIN() asm volatile("s_waitcnt vmcnt(0)" ::: "memory")
#define BAR() do { asm volatile("s_waitcnt lgkmcnt(0)" ::: "memory"); __builtin_amdgcn_s_barrier(); asm volatile("" ::: "memory"); } while (0)

DI v4u ld128(rsrc_t rs, unsigned voff, unsigned soff) { return __builtin_amdgcn_raw_buffer_load_b128(rs, voff, soff, AUX_SC1); }
DI void st128(v4u v, rsrc_t rs, unsigned voff, unsigned soff) { __builtin_amdgcn_raw_buffer_store_b128(v, rs, voff, soff, AUX_SC1); }
DI v2u ld64(rsrc_t rs, unsigned voff, unsigned soff) { return __builtin_amdgcn_raw_buffer_load_b64(rs, voff, soff, AUX_SC1); }
DI unsigned ld32(rsrc_t rs, unsigned voff, unsigned soff) { return __builtin_amdgcn_raw_buffer_load_b32(rs, voff, soff, AUX_SC1); }
DI void st64(v2u v, rsrc_t rs, unsigned voff, unsigned soff) { __builtin_amdgcn_raw_buffer_store_b64(v, rs, voff, soff, AUX_SC1); }

DI void dma4(rsrc_t rs, unsigned voff, unsigned soff, unsigned m0v) {
  asm volatile("s_nop 4\n\ts_mov_b32 m0, %0\n\ts_nop 0\n\t"
               "buffer_load_dwordx4 %1, %2, %3 offen sc1 lds\n\t"
               "buffer_load_dwordx4 %1, %2, %3 offen offset:1024 sc1 lds\n\t"
               "buffer_load_dwordx4 %1, %2, %3 offen offset:2048 sc1 lds\n\t"
               "buffer_load_dwordx4 %1, %2, %3 offen offset:3072 sc1 lds"
               :: "s"(m0v), "v"(voff), "s"(rs), "s"(soff) : "m0", "memory");
}
DI void dma1(rsrc_t rs, unsigned voff, unsigned soff, unsigned m0v) {
  asm volatile("s_nop 4\n\ts_mov_b32 m0, %0\n\ts_nop 0\n\t"
               "buffer_load_dwordx4 %1, %2, %3 offen sc1 lds"
               :: "s"(m0v), "v"(voff), "s"(rs), "s"(soff) : "m0", "memory");
}
DI void dma_item(rsrc_t rs, unsigned voff, unsigned m0v) {
  asm volatile("s_nop 4\n\ts_mov_b32 m0, %0\n\ts_nop 0\n\t"
               "buffer_load_dwordx4 %1, %2, 0 offen lds"
               :: "s"(m0v), "v"(voff), "s"(rs) : "m0", "memory");
}
DI v4u item_read(LAS char* img, int lane) {
  LAS char* p = img + ((lane >> 2) & 3) * 128 + (lane >> 4) * 32 + (lane & 3) * 8;
  const s16x4 a = __builtin_amdgcn_ds_read_tr16_b64_v4i16((LAS s16x4*)p);
  const s16x4 b = __builtin_amdgcn_ds_read_tr16_b64_v4i16((LAS s16x4*)(p + 512));
  const v2u x = __builtin_bit_cast(v2u, a), y = __builtin_bit_cast(v2u, b);
  return v4u{x.x, x.y, y.x, y.y};
}
#define LDF(sbase, i) ld128(ws, l16 + ((i) & 3) * 1024, (sbase) + ((i) >> 2) * 4096)
#define LDSF(off) (*(LAS v4u*)(((off) < 65536 ? lb0 : ((off) < 131072 ? lb1 : lb2)) + ((off) & 65535)))
#define W1A(ks, n) (((ks) < 7 ? L_B + (ks) * 7168 : L_A + ((ks) - 7) * 7168) + (n) * 1024)

DI void wait_flag(gu32* f, unsigned tag, bool active) {
  unsigned sp = 0;
  for (;;) {
    unsigned v = active ? __hip_atomic_load(f, RLX) : tag;
    if (__all(v == tag)) break;
    if (++sp > SPIN_CAP) break;
  }
  __builtin_amdgcn_fence(__ATOMIC_ACQUIRE, "wavefront");
}

#define STAMP(k) do { } while (0)

__global__ __launch_bounds__(256, 1) void fused_block(const unsigned short* __restrict__ x, const unsigned short* __restrict__ wqkv,
                                                      const unsigned short* __restrict__ wo, const unsigned short* __restrict__ w1,
                                                      const unsigned short* __restrict__ w2, unsigned short* __restrict__ out,
                                                      char* __restrict__ wsp) {
  extern __shared__ __attribute__((aligned(16))) char smem[];
  LAS char* lds = (LAS char*)smem;
  const unsigned ldsb = (unsigned)(size_t)lds;
  const int tid = threadIdx.x, lane = tid & 63;
  const int w = __builtin_amdgcn_readfirstlane(tid >> 6);
  const int g = lane >> 4, t16 = lane & 15;
  const int b = blockIdx.x, xcd = b & 7, sl = b >> 3;
  const int c2 = xcd & 1, P = 4 * (xcd >> 1) + (sl >> 3), m = 8 * c2 + (sl & 7);
  const int cuid = P * 16 + m;
  const int jl = (m & 7) >> 2, ct = 4 * c2 + (m & 3);
  const unsigned l16 = lane * 16;
  const rsrc_t ws = __builtin_amdgcn_make_buffer_rsrc(wsp, 0, WS_BYTES, 0x00020000);
  gu32* flg = (gu32*)(wsp + OFF_FLAG);
  STAMP(0);
  unsigned warm0 = 0, warm1 = 0;
  unsigned warm2 = 0, warm3 = 0, warm4 = 0, warm5 = 0;
  if (w == 3) {
    unsigned long long pc;
    asm volatile("s_getpc_b64 %0" : "=s"(pc));
    const GAS char* cp = (const GAS char*)((pc & ~127ull) + (unsigned long long)lane * 128ull);
    asm volatile("global_load_dword %0, %5, off\n\tglobal_load_dword %1, %6, off\n\tglobal_load_dword %2, %7, off\n\tglobal_load_dword %3, %8, off\n\tglobal_load_dword %4, %9, off"
      : "=&v"(warm0), "=&v"(warm2), "=&v"(warm3), "=&v"(warm4), "=&v"(warm5) : "v"(cp), "v"(cp + 8192), "v"(cp + 16384), "v"(cp + 24576), "v"(cp + (lane < 40 ? 32768 : 0)) : "memory");
  }
  if (w == 2) {
    const GAS char* tp = lane == 0 ? (const GAS char*)wo : lane == 1 ? (const GAS char*)w1 : lane == 2 ? (const GAS char*)w2 : lane == 3 ? (const GAS char*)out
                       : lane == 4 ? (const GAS char*)w1 + 2097152 : lane == 5 ? (const GAS char*)w2 + 2097152 : lane == 6 ? (const GAS char*)out + 2097152
                       : (const GAS char*)wsp + (size_t)(lane - 7) * 2097152;
    if (lane < 19) asm volatile("global_load_dword %0, %1, off" : "=v"(warm1) : "v"(tp) : "memory");
  }
  const unsigned epoch = __hip_atomic_load((gu32*)(wsp + OFF_EPOCH), RLX);
  LAS char* lb0 = lds + l16; LAS char* lb1 = lb0 + 65536; LAS char* lb2 = lb0 + 131072;
  asm volatile("" : "+v"(lb1), "+v"(lb2));

  v2u xv[5];
  {
    const GAS char* xp = (const GAS char*)(x + (size_t)(128 * P + 8 * m + 2 * w + (lane >> 5)) * 640) + (lane & 31) * 8;
    asm volatile("global_load_dwordx2 %0, %5, off\n\tglobal_load_dwordx2 %1, %5, off offset:256\n\tglobal_load_dwordx2 %2, %5, off offset:512\n\t"
                 "global_load_dwordx2 %3, %5, off offset:768\n\tglobal_load_dwordx2 %4, %5, off offset:1024"
                 : "=&v"(xv[0]), "=&v"(xv[1]), "=&v"(xv[2]), "=&v"(xv[3]), "=&v"(xv[4]) : "v"(xp) : "memory");
  }
  const int r8 = lane >> 3, ch8 = lane & 7;
  {
    const rsrc_t rq = __builtin_amdgcn_make_buffer_rsrc((void*)wqkv, 0, 640 * 1920 * 2, 0x00020000);
    const int col0 = ch8 < 5 ? 640 + 40 * m + 8 * ch8 : 1280 + 40 * m + 8 * (ch8 - 5);
    const int col1 = ch8 < 2 ? 1280 + 40 * m + 24 + 8 * ch8 : 40 * m + 8 * ((ch8 < 7 ? ch8 : 6) - 2);
#pragma unroll
    for (int k = 0; k < 3; ++k) {
      const int q = w + 4 * k;
      if (q < 10) dma_item(rq, (unsigned)((40 * P + 8 * (q >> 1) + r8) * 1920 + ((q & 1) ? col1 : col0)) * 2, ldsb + L_A + 16384 + q * 1024);
    }
  }
  STAMP(22);
  unsigned dxa[3], dwa[3];
#pragma unroll
  for (int i = 0; i < 3; ++i) {
    const int L = 160 * w + 64 * i + lane, c = L >> 3, tt = 8 * (m & 1) + (L & 7);
    dxa[i] = OFF_XN + ((8 * P + (m >> 1)) * 20 + (c >> 2)) * 1024 + ((c & 3) * 16 + tt) * 16;
    const int q = w + 4 * i, pos = 64 * (q & 1) + lane, nb = pos >> 4, n = pos & 15;
    const int rg = 5 * P + (q >> 1), ks = rg >> 2, gg = rg & 3;
    dwa[i] = OFF_WQKV + m * WQKV_STR + (nb < 7 ? (nb * 20 + ks) * 1024 + (gg * 16 + n) * 16 : 143360 + ks * 512 + (gg * 8 + n) * 16);
  }
  const int gw = cuid * 4 + w;
  const int it0k = gw < 800 ? 0 : (gw < 912 ? 1 : 2), it0 = gw < 800 ? gw : (gw < 912 ? 2048 + gw - 800 : 2048 + gw - 912);
  unsigned srcb[5], dstb[5];
  {
    auto src_w1 = [&](int u) {
      const int rg = u / 27, cg = u - 27 * rg, ks = rg >> 2, gg = rg & 3;
      const int nA = ks < 16 ? 5 * (ks >> 1) + 2 * (ks & 1) : 10 * (ks - 16) + 4, nB = ks < 16 ? nA + 1 : nA + 5;
      return (unsigned)((16 * (r8 < 4 ? nA : nB) + 4 * gg + (r8 & 3)) * 1728 + 64 * cg + 8 * ch8) * 2;
    };
    auto src_w2 = [&](int u) {
      const int rg = u / 10, cg = u - 10 * rg, sq = rg >> 2, gg = rg & 3;
      const int wq = sq < 28 ? sq / 14 : 2 + (sq - 28) / 13, k = sq - (wq < 2 ? 14 * wq : 28 + 13 * (wq - 2));
      int nA, nB;
      if (k < 12) {
        const int tt = 4 * (wq & 1) + k / 3, i = k % 3;
        nA = 54 * (wq >> 1) + (tt < 6 ? 7 * tt : 42 + 6 * (tt - 6)) + 2 * i; nB = nA + 1;
      } else {
        const int ci = (wq < 2 ? 2 * wq : wq + 2) + (k - 12), h2 = ci / 3, a3 = ci - 3 * h2;
        nA = 54 * h2 + 14 * a3 + 6; nB = nA + 7;
      }
      return (unsigned)((16 * (r8 < 4 ? nA : nB) + 4 * gg + (r8 & 3)) * 640 + 64 * cg + 8 * ch8) * 2;
    };
    auto src_wo = [&](int u) {
      const int rg = u / 10, cg = u - 10 * rg, ks = rg >> 2, gg = rg & 3;
      const int row = ks < 16 ? 40 * ks + 5 * gg + (r8 < 5 ? r8 : 15 + r8) : 40 * (4 * (ks - 16) + (r8 >> 1)) + 23 + 5 * gg + (r8 & 1);
      return (unsigned)(row * 640 + 64 * cg + 8 * ch8) * 2;
    };
    auto dst_w1 = [&](int u) {
      const int rg = u / 27, cg = u - 27 * rg, ks = rg >> 2, gg = rg & 3, n = 64 * cg + lane;
      const int nb = n >> 4, h2 = nb >= 54 ? 1 : 0, nbp = nb - 54 * h2;
      const int tt = nbp < 42 ? nbp / 7 : 6 + (nbp - 42) / 6, nbl = nbp - (tt < 6 ? 7 * tt : 42 + 6 * (tt - 6));
      return OFF_W1 + (8 * h2 + tt) * W1_STR + (ks * 7 + nbl) * 1024 + (gg * 16 + (n & 15)) * 16;
    };
    auto dst_5 = [&](int u, unsigned base, unsigned str) {
      const int rg = u / 10, cg = u - 10 * rg, ks = rg >> 2, gg = rg & 3, n = 64 * cg + lane;
      const int c = n / 80, nl = n - 80 * c;
      return base + c * str + (ks * 5 + (nl >> 4)) * 1024 + (gg * 16 + (nl & 15)) * 16;
    };
    srcb[0] = it0k == 0 ? src_wo(it0) : (it0k == 1 ? src_w1(it0) : src_w2(it0));
    dstb[0] = it0k == 1 ? dst_w1(it0) : dst_5(it0, it0k == 0 ? OFF_WO : OFF_W2, it0k == 0 ? 102400u : W2_STR);
    srcb[1] = src_w1(gw); srcb[2] = src_w1(gw + 1024); srcb[3] = src_w2(gw); srcb[4] = src_w2(gw + 1024);
    dstb[1] = dst_w1(gw); dstb[2] = dst_w1(gw + 1024); dstb[3] = dst_5(gw, OFF_W2, W2_STR); dstb[4] = dst_5(gw + 1024, OFF_W2, W2_STR);
  }
  STAMP(23);
  asm volatile("s_waitcnt vmcnt(2)" : "+v"(xv[0]), "+v"(xv[1]), "+v"(xv[2]), "+v"(xv[3]), "+v"(xv[4]) :: "memory");
  {
    const rsrc_t ro = __builtin_amdgcn_make_buffer_rsrc((void*)wo, 0, 640 * 640 * 2, 0x00020000);
    const rsrc_t r1 = __builtin_amdgcn_make_buffer_rsrc((void*)w1, 0, 640 * 1728 * 2, 0x00020000);
    const rsrc_t r2 = __builtin_amdgcn_make_buffer_rsrc((void*)w2, 0, 1728 * 640 * 2, 0x00020000);
    const unsigned sb = ldsb + L_B + w * 5120;
    if (it0k == 0) dma_item(ro, srcb[0], sb); else if (it0k == 1) dma_item(r1, srcb[0], sb); else dma_item(r2, srcb[0], sb);
    dma_item(r1, srcb[1], sb + 1024); dma_item(r1, srcb[2], sb + 2048); dma_item(r2, srcb[3], sb + 3072); dma_item(r2, srcb[4], sb + 4096);
  }
  {
    float f[20];
#pragma unroll
    for (int i = 0; i < 5; ++i) { f[4 * i] = blo(xv[i].x); f[4 * i + 1] = bhi(xv[i].x); f[4 * i + 2] = blo(xv[i].y); f[4 * i + 3] = bhi(xv[i].y); }
    float s4[4];
#pragma unroll
    for (int j = 0; j < 4; ++j) s4[j] = (f[j] + f[4 + j]) + (f[8 + j] + f[12 + j]) + f[16 + j];
    const float mean = sum_x16(sum16((s4[0] + s4[1]) + (s4[2] + s4[3]))) * (1.f / 640.f);
#pragma unroll
    for (int i = 0; i < 20; ++i) f[i] -= mean;
#pragma unroll
    for (int j = 0; j < 4; ++j) s4[j] = (f[j] * f[j] + f[4 + j] * f[4 + j]) + (f[8 + j] * f[8 + j] + f[12 + j] * f[12 + j]) + f[16 + j] * f[16 + j];
    const float rstd = __builtin_amdgcn_rsqf(sum_x16(sum16((s4[0] + s4[1]) + (s4[2] + s4[3]))) * (1.f / 640.f) + 1e-5f);
    LAS char* sc = lds + L_A + ((lane & 31) >> 1) * 128 + (2 * w + (lane >> 5)) * 16 + (lane & 1) * 8;
#pragma unroll
    for (int i = 0; i < 5; ++i)
      *(LAS v2u*)(sc + i * 2048) = v2u{pk2(f[4 * i] * rstd, f[4 * i + 1] * rstd), pk2(f[4 * i + 2] * rstd, f[4 * i + 3] * rstd)};
  }
  STAMP(24);
  asm volatile("s_waitcnt vmcnt(5)" ::: "memory");
  asm volatile("" :: "v"(warm0), "v"(warm1), "v"(warm2), "v"(warm3), "v"(warm4), "v"(warm5));
  BAR();
  STAMP(25);
#pragma unroll
  for (int i = 0; i < 3; ++i)
    if (i < 2 || lane < 32) st128(*(LAS v4u*)(lds + L_A + (160 * w + 64 * i + lane) * 16), ws, dxa[i], 0);
  STAMP(32);
#pragma unroll
  for (int k = 0; k < 3; ++k) {
    const int q = w + 4 * k;
    if (q < 10) {
      const v4u o = item_read(lds + L_A + 16384 + q * 1024, lane);
      if (64 * (q & 1) + lane < 120) st128(o, ws, dwa[k], 0);
    }
  }
  STAMP(33);
  const unsigned tag = __builtin_amdgcn_readfirstlane(epoch) * 2654435761u + 0x9E3779B9u;
  STAMP(26);
  DRAIN();
  BAR();
  if (tid == 0) __hip_atomic_store(flg + (0 * 256 + cuid) * 32, tag, RLX);
  STAMP(1);
  *(LAS v4u*)(lds + L_IMG + tid * 16) = v4u{0u, 0u, 0u, 0u};
#pragma unroll
  for (int k = 0; k < 5; ++k) st128(item_read(lds + L_B + w * 5120 + k * 1024, lane), ws, dstb[k], 0);
  STAMP(2);

  if (w == 0) {
    const int l = lane & 15;
    wait_flag(flg + (0 * 256 + (lane < 16 ? P * 16 + l : l * 16 + m)) * 32, tag, lane < 32);
  }
  BAR();
  STAMP(3);
  v4u fb;
  v4u xn[2][20];
  f32x4 akv[2][5];
  v4u wqs[13];
  {
    constexpr int D1 = 5;
    for (int q = w; q < 25; q += 4) dma4(ws, l16, OFF_WQKV + m * WQKV_STR + q * 4096, ldsb + L_A + q * 4096);
    const unsigned xb = OFF_XN + (8 * P + 2 * w) * 20 * 1024;
#pragma unroll
    for (int ks = 0; ks < D1; ++ks) {
#pragma unroll
      for (int u = 0; u < 2; ++u) xn[u][ks] = LDF(xb, u * 20 + ks);
      SCHED();
    }
    asm volatile("s_waitcnt vmcnt(10)" ::: "memory");
    BAR();
    if (tid == 0) __hip_atomic_store((gu32*)(wsp + OFF_FLAGB) + cuid, tag, RLX);
    STAMP(17);
#pragma unroll
    for (int u = 0; u < 2; ++u)
#pragma unroll
      for (int n = 0; n < 5; ++n) akv[u][n] = f32x4{0.f, 0.f, 0.f, 0.f};
    v4u wf[2][5];
#pragma unroll
    for (int n = 0; n < 5; ++n) wf[0][n] = LDSF(L_A + (n * 20) * 1024);
    SCHED();
    const unsigned wqb = OFF_WQKV + m * WQKV_STR + 102400 + w * 1024;
#pragma unroll
    for (int ks = 0; ks < 20; ++ks) {
      if (ks + D1 < 20) {
#pragma unroll
        for (int u = 0; u < 2; ++u) xn[u][ks + D1] = LDF(xb, u * 20 + ks + D1);
      } else {
#pragma unroll
        for (int i = (ks + D1 - 20) * 3; i < (ks + D1 - 20) * 3 + 3 && i < 13; ++i) wqs[i] = ld128(ws, l16, wqb + (i < 12 || w < 2 ? i : 11) * 4096);
        if (ks == 19) fb = __builtin_amdgcn_raw_buffer_load_b128(ws, OFF_FLAGB + l16, 0, AUX_SC1);
      }
      if (ks + 1 < 20) {
#pragma unroll
        for (int n = 0; n < 5; ++n) wf[(ks + 1) & 1][n] = LDSF(L_A + (n * 20 + ks + 1) * 1024);
      }
#pragma unroll
      for (int n = 0; n < 5; ++n)
#pragma unroll
        for (int u = 0; u < 2; ++u) akv[u][n] = mfma(xn[u][ks], wf[ks & 1][n], akv[u][n]);
#pragma unroll
      for (int n = 0; n < 5; ++n) { SGB(0x100, 1); SGB(0x8, 1); if (n < 3) SGB(0x20, 1); SGB(0x8, 1); }
      SCHED();
    }
  }
  STAMP(4);
  if (!__all(fb.x == tag && fb.y == tag && fb.z == tag && fb.w == tag)) {
    unsigned sp = 0;
    for (;;) {
      asm volatile("" ::: "memory");
      fb = __builtin_amdgcn_raw_buffer_load_b128(ws, OFF_FLAGB + l16, 0, AUX_SC1);
      if (__all(fb.x == tag && fb.y == tag && fb.z == tag && fb.w == tag)) break;
      if (++sp > SPIN_CAP) break;
    }
  }
  v4u w2f[14][5];
  const int k4s = w < 2 ? 14 * w : 28 + 13 * (w - 2);
  const unsigned w2b = OFF_W2 + ct * W2_STR + k4s * 5 * 1024;
#define W2LD(f0, f1) _Pragma("unroll") for (int f_ = (f0); f_ < (f1); ++f_) w2f[f_ / 5][f_ % 5] = LDF(w2b, f_); SCHED()
  W2LD(0, 12);
  BAR();
#pragma unroll
  for (int u = 0; u < 2; ++u)
#pragma unroll
    for (int n = 0; n < 5; ++n) {
      f32x4 a = akv[u][n];
      if (n < 2) { a[0] = phi(a[0]); a[1] = phi(a[1]); a[2] = phi(a[2]); a[3] = phi(a[3]); }
      else if (n == 2) { if (t16 < 8) { a[0] = phi(a[0]); a[1] = phi(a[1]); a[2] = phi(a[2]); a[3] = phi(a[3]); } }
      *(LAS v2u*)(lds + L_A + (16 * n + t16) * EP + (32 * w + 16 * u + 4 * g) * 2) = v2u{pk2(a[0], a[1]), pk2(a[2], a[3])};
    }
  if (tid < 16) *(LAS v4u*)(lds + L_A + 80 * EP + tid * 16) = v4u{0x3F803F80u, 0x3F803F80u, 0x3F803F80u, 0x3F803F80u};
  BAR();
  W2LD(12, 24);
  STAMP(27);
  {
    const int hd = w >> 1, dt = w & 1;
    f32x4 t0 = {0.f, 0.f, 0.f, 0.f}, t1 = t0;
    const unsigned ra = L_A + (20 * hd + 16 * dt + t16) * EP + g * 16;
    const unsigned rb0 = L_A + (40 + 20 * hd + t16) * EP + g * 16;
    const unsigned rb1 = L_A + (t16 < 4 ? 56 + 20 * hd + t16 : 80) * EP + g * 16;
    v4u fa[4], f0[4], f1[4];
#pragma unroll
    for (int s = 0; s < 4; ++s) { fa[s] = *(LAS v4u*)(lds + ra + s * 64); f0[s] = *(LAS v4u*)(lds + rb0 + s * 64); f1[s] = *(LAS v4u*)(lds + rb1 + s * 64); }
#pragma unroll
    for (int s = 0; s < 4; ++s) { t0 = mfma(fa[s], f0[s], t0); t1 = mfma(fa[s], f1[s], t1); }
    LAS char* pt = lds + L_PT + hd * 1680;
    if (dt == 0) {
      *(LAS f32x4*)(pt + l16) = t0;
      if (t16 < 5) *(LAS f32x4*)(pt + 1024 + (g * 5 + t16) * 16) = t1;
    } else if (g == 0) {
      *(LAS f32x4*)(pt + 1344 + t16 * 16) = t0;
      if (t16 < 5) *(LAS f32x4*)(pt + 1600 + t16 * 16) = t1;
    }
    if (tid >= 250) *(LAS v4u*)(lds + L_PT + 3360 + (tid - 250) * 16) = v4u{0u, 0u, 0u, 0u};
  }
  BAR();
  if (tid < 216) st128(*(LAS v4u*)(lds + L_PT + tid * 16), ws, tid * 16, OFF_PART + (m * 16 + P) * PART_STR);
#pragma unroll
  for (int i = 0; i < 13; ++i)
    if (i < 12 || w < 2) *(LAS v4u*)(lds + L_B + (w + 4 * i) * 1024 + l16) = wqs[i];
  STAMP(28);
  DRAIN();
  BAR();
  if (tid == 0) __hip_atomic_store(flg + (1 * 256 + cuid) * 32, tag, RLX);
  STAMP(5);

  unsigned qp[2][3][2];
  {
    f32x4 aq[2][3];
#pragma unroll
    for (int u = 0; u < 2; ++u)
#pragma unroll
      for (int n = 0; n < 3; ++n) aq[u][n] = f32x4{0.f, 0.f, 0.f, 0.f};
    LAS char* lq7 = lds + L_B + 40960 + g * 128 + (t16 & 7) * 16;
    v4u wf[3][3];
#pragma unroll
    for (int k = 0; k < 2; ++k) { wf[k][0] = LDSF(L_B + k * 1024); wf[k][1] = LDSF(L_B + (20 + k) * 1024); wf[k][2] = *(LAS v4u*)(lq7 + k * 512); }
    SCHED();
#pragma unroll
    for (int ks = 0; ks < 20; ++ks) {
      if (ks % 3 == 0) {
        const int q = w + 4 * (ks / 3);
        if (q < 25) dma4(ws, l16, OFF_WO + ct * 102400 + q * 4096, ldsb + L_A + q * 4096);
      }
      if (ks + 2 < 20) {
        wf[(ks + 2) % 3][0] = LDSF(L_B + (ks + 2) * 1024);
        wf[(ks + 2) % 3][1] = LDSF(L_B + (20 + ks + 2) * 1024);
        wf[(ks + 2) % 3][2] = *(LAS v4u*)(lq7 + (ks + 2) * 512);
      }
#pragma unroll
      for (int n = 0; n < 3; ++n)
#pragma unroll
        for (int u = 0; u < 2; ++u) aq[u][n] = mfma(wf[ks % 3][n], xn[u][ks], aq[u][n]);
#pragma unroll
      for (int n = 0; n < 3; ++n) { SGB(0x100, 1); SGB(0x8, 2); }
      SCHED();
    }
#pragma unroll
    for (int u = 0; u < 2; ++u)
#pragma unroll
      for (int n = 0; n < 3; ++n) {
        qp[u][n][0] = pk2(phi(aq[u][n][0]), phi(aq[u][n][1]));
        qp[u][n][1] = pk2(phi(aq[u][n][2]), phi(aq[u][n][3]));
      }
  }
  W2LD(24, 34);
  unsigned imgoff, imgoffz;
  {
    const int hd = tid >= 105 ? 1 : 0, u = tid - 105 * hd;
    int dq, e;
    if (u < 64) { dq = u >> 4; e = u & 15; }
    else if (u < 84) { const int v = u - 64; dq = v / 5; e = 16 + v - 5 * dq; }
    else if (u < 100) { dq = 4; e = u - 84; }
    else { dq = 4; e = 16 + u - 100; }
    int gq;
    if (hd == 0) gq = dq < 4 ? dq : 0; else gq = dq < 3 ? dq + 1 : (dq == 3 ? 0 : 1);
    const int j0 = dq < 4 ? 0 : 4, ge = e / 5, re = e - 5 * ge;
    const unsigned ib = L_IMG + hd * 2048 + gq * 256 + j0 * 2;
    imgoff = ib + (re < 4 ? (4 * ge + re) * 16 : 1024 + (4 * ge) * 16);
    imgoffz = e == 20 ? ib + 1024 + 16 : 0u;
  }
  STAMP(6);

  if (w == 0) wait_flag(flg + (1 * 256 + (lane & 15) * 16 + m) * 32, tag, lane < 16);
  BAR();
  STAMP(7);
  if (tid < 210) {
    v4u pv[16];
#pragma unroll
    for (int i = 0; i < 16; ++i) pv[i] = ld128(ws, tid * 16, OFF_PART + (m * 16 + i) * PART_STR);
    f32x4 s[4];
#pragma unroll
    for (int i = 0; i < 4; ++i)
      s[i] = (__builtin_bit_cast(f32x4, pv[4 * i]) + __builtin_bit_cast(f32x4, pv[4 * i + 1])) +
             (__builtin_bit_cast(f32x4, pv[4 * i + 2]) + __builtin_bit_cast(f32x4, pv[4 * i + 3]));
    const f32x4 st = (s[0] + s[1]) + (s[2] + s[3]);
    const v2u val = v2u{pk2(st[0], st[1]), pk2(st[2], st[3])};
    if (imgoffz == 0u) *(LAS v2u*)(lds + imgoff) = val;
    else {
#pragma unroll
      for (int ge = 0; ge < 4; ++ge) *(LAS v2u*)(lds + imgoffz + ge * 64) = val;
    }
  }
  BAR();
  STAMP(18);
  {
    v4u ka[2][2];
#pragma unroll
    for (int hd = 0; hd < 2; ++hd)
#pragma unroll
      for (int bb = 0; bb < 2; ++bb) ka[hd][bb] = *(LAS v4u*)(lds + L_IMG + hd * 2048 + bb * 1024 + l16);
#pragma unroll
    for (int u = 0; u < 2; ++u) {
      v4u qa, qb;
      qa.x = qp[u][0][0]; qa.y = qp[u][0][1]; qa.z = g == 0 ? qp[u][1][0] : 0u; qa.w = g == 0 ? qp[u][1][1] : 0u;
      qb.x = g == 0 ? qp[u][2][0] : qp[u][1][0]; qb.y = g == 0 ? qp[u][2][1] : qp[u][1][1];
      qb.z = g == 1 ? qp[u][2][0] : 0u; qb.w = g == 1 ? qp[u][2][1] : 0u;
      const f32x4 z4 = {0.f, 0.f, 0.f, 0.f};
      const f32x4 a0 = mfma(ka[0][0], qa, z4), a1 = mfma(ka[0][1], qa, z4);
      const f32x4 b0 = mfma(ka[1][0], qb, z4), b1 = mfma(ka[1][1], qb, z4);
      const float ra = __builtin_amdgcn_rcpf(a1[1] + 1e-6f), rb = __builtin_amdgcn_rcpf(b1[1] + 1e-6f);
      const unsigned d0 = pk2(a0[0] * ra, a0[1] * ra), d1 = pk2(a0[2] * ra, a0[3] * ra), d2 = pk2(a1[0] * ra, b0[0] * rb),
                     d3 = pk2(b0[1] * rb, b0[2] * rb), d4 = pk2(b0[3] * rb, b1[0] * rb);
      const unsigned oi = (8 * P + 2 * w + u) * 16 + m;
      st128(v4u{d0, d1, d2, d3}, ws, l16, OFF_OM + oi * 1024);
      __builtin_amdgcn_raw_buffer_store_b32(d4, ws, lane * 4, OFF_OR + oi * 256, AUX_SC1);
    }
  }
  DRAIN();
  BAR();
  if (tid == 0) __hip_atomic_store(flg + (2 * 256 + cuid) * 32, tag, RLX);
  STAMP(8);
  for (int q = w; q < 12; q += 4) dma4(ws, l16, OFF_W1 + m * W1_STR + q * 4096, ldsb + L_B + q * 4096);
  if (w == 0) dma1(ws, l16, OFF_W1 + m * W1_STR + 49152, ldsb + L_B + 49152);
  v2u xr[5];
  {
    const GAS char* xp = (const GAS char*)x + (size_t)(64 * (2 * P + jl) + 16 * w + t16) * 1280 + (80 * ct + 4 * g) * 2;
#pragma unroll
    for (int n = 0; n < 5; ++n) xr[n] = *(const GAS v2u*)(xp + n * 32);
  }
  v4u w1r[23];
  const unsigned w1rb = OFF_W1 + m * W1_STR + 50176 + w * 1024;
#pragma unroll
  for (int i = 0; i < 12; ++i) w1r[i] = ld128(ws, l16, w1rb + i * 4096);
  SCHED();

  STAMP(29);
  if (w == 0) wait_flag(flg + (2 * 256 + P * 16 + (lane & 15)) * 32, tag, lane < 16);
  STAMP(30);
  BAR();
  STAMP(9);
  unsigned hp[5][2];
  {
    constexpr int D2 = 8;
    v4u of[20];
    const unsigned ob = OFF_OM + (8 * P + 4 * jl + w) * 16 * 1024, orb = OFF_OR + (8 * P + 4 * jl + w) * 16 * 256;
#define LDO(ks) ((ks) < 16 ? LDF(ob, ks) : v4u{ld32(ws, lane * 4 + (4 * ((ks) - 16)) * 256, orb), ld32(ws, lane * 4 + (4 * ((ks) - 16) + 1) * 256, orb), \
                                              ld32(ws, lane * 4 + (4 * ((ks) - 16) + 2) * 256, orb), ld32(ws, lane * 4 + (4 * ((ks) - 16) + 3) * 256, orb)})
#pragma unroll
    for (int ks = 0; ks < D2; ++ks) { of[ks] = LDO(ks); SCHED(); }
    f32x4 ah[5];
#pragma unroll
    for (int n = 0; n < 5; ++n) ah[n] = f32x4{0.f, 0.f, 0.f, 0.f};
    v4u wf[3][5];
#pragma unroll
    for (int n = 0; n < 5; ++n) { wf[0][n] = LDSF(L_A + n * 1024); wf[1][n] = LDSF(L_A + (5 + n) * 1024); }
    SCHED();
#pragma unroll
    for (int ks = 0; ks < 20; ++ks) {
      if (ks + D2 < 20) of[ks + D2] = LDO(ks + D2);
      else {
        const int i0 = ks < 15 ? 12 + 2 * (ks - 12) : 15 + (ks - 12);
        w1r[i0] = ld128(ws, l16, w1rb + (i0 < 22 || w < 3 ? i0 : 21) * 4096);
        if (ks < 15) w1r[i0 + 1] = ld128(ws, l16, w1rb + (i0 + 1) * 4096);
      }
      if (ks + 2 < 20) {
#pragma unroll
        for (int n = 0; n < 5; ++n) wf[(ks + 2) % 3][n] = LDSF(L_A + ((ks + 2) * 5 + n) * 1024);
      }
#pragma unroll
      for (int n = 0; n < 5; ++n) ah[n] = mfma(wf[ks % 3][n], of[ks], ah[n]);
#pragma unroll
      for (int n = 0; n < 5; ++n) { SGB(0x100, 1); SGB(0x8, 1); if (n < 4) SGB(0x20, 1); }
      SCHED();
    }
    STAMP(19);
    float hv[20];
#pragma unroll
    for (int n = 0; n < 5; ++n) {
      hp[n][0] = pk2(blo(xr[n].x) + ah[n][0], bhi(xr[n].x) + ah[n][1]);
      hp[n][1] = pk2(blo(xr[n].y) + ah[n][2], bhi(xr[n].y) + ah[n][3]);
    }
    const unsigned hi = (8 * P + 4 * jl + w) * 8 + ct;
    st128(v4u{hp[0][0], hp[0][1], hp[1][0], hp[1][1]}, ws, l16, OFF_HF + hi * 2048);
    st128(v4u{hp[2][0], hp[2][1], hp[3][0], hp[3][1]}, ws, l16 + 1024, OFF_HF + hi * 2048);
    st64(v2u{hp[4][0], hp[4][1]}, ws, lane * 8, OFF_HH + hi * 512);
    float s4[5];
#pragma unroll
    for (int n = 0; n < 5; ++n) {
      hv[4 * n] = blo(hp[n][0]); hv[4 * n + 1] = bhi(hp[n][0]); hv[4 * n + 2] = blo(hp[n][1]); hv[4 * n + 3] = bhi(hp[n][1]);
      s4[n] = (hv[4 * n] + hv[4 * n + 1]) + (hv[4 * n + 2] + hv[4 * n + 3]);
    }
    const float mi = sum_g((s4[0] + s4[1]) + (s4[2] + s4[3]) + s4[4]) * (1.f / 80.f);
#pragma unroll
    for (int n = 0; n < 5; ++n) {
      const float a = hv[4 * n] - mi, bq = hv[4 * n + 1] - mi, c = hv[4 * n + 2] - mi, d = hv[4 * n + 3] - mi;
      s4[n] = (a * a + bq * bq) + (c * c + d * d);
    }
    const float q = sum_g((s4[0] + s4[1]) + (s4[2] + s4[3]) + s4[4]);
    if (g == 0) st64(v2u{asu(mi), asu(q)}, ws, t16 * 8, OFF_ST + ((8 * P + 4 * jl + w) * 8 + ct) * 128);
  }
  DRAIN();
  BAR();
  if (tid == 0) __hip_atomic_store(flg + (3 * 256 + cuid) * 32, tag, RLX);
  STAMP(10);
#pragma unroll
  for (int i = 0; i < 23; ++i)
    if (i < 22 || w < 3) *(LAS v4u*)(lds + L_A + (w + 4 * i) * 1024 + l16) = w1r[i];
  W2LD(34, 46);
  BAR();
  {
    f32x4 cs[7];
#pragma unroll
    for (int n = 0; n < 7; ++n) cs[n] = f32x4{0.f, 0.f, 0.f, 0.f};
    const v4u ones = {0x3F803F80u, 0x3F803F80u, 0x3F803F80u, 0x3F803F80u};
    v4u wf[2][7];
#pragma unroll
    for (int n = 0; n < 7; ++n) wf[0][n] = *(LAS v4u*)(lb0 + L_B + w * 7168 + n * 1024);
#pragma unroll
    for (int i = 0; i < 5; ++i) {
      if (i + 1 < 5) {
        const int ks = w + 4 * (i + 1);
        const unsigned a = ks < 7 ? L_B + ks * 7168 : L_A + (ks - 7) * 7168;
#pragma unroll
        for (int n = 0; n < 7; ++n) wf[(i + 1) & 1][n] = *(LAS v4u*)(lb0 + a + n * 1024);
      }
#pragma unroll
      for (int n = 0; n < 7; ++n) cs[n] = mfma(wf[i & 1][n], ones, cs[n]);
    }
    if (t16 == 0) {
#pragma unroll
      for (int n = 0; n < 7; ++n) *(LAS f32x4*)(lds + L_CS + (w * 7 + n) * 64 + g * 16) = cs[n];
    }
    BAR();
    if (lane < 8 && w + 4 * (lane >> 2) < 7) {
      const int n = w + 4 * (lane >> 2), gq = lane & 3;
      f32x4 cw[4];
#pragma unroll
      for (int ww = 0; ww < 4; ++ww) cw[ww] = *(LAS f32x4*)(lds + L_CS + (ww * 7 + n) * 64 + gq * 16);
      *(LAS f32x4*)(lds + L_IMG + n * 64 + gq * 16) = (cw[0] + cw[1]) + (cw[2] + cw[3]);
    }
  }
  STAMP(20);

  if (w == 0) wait_flag(flg + (3 * 256 + P * 16 + (lane & 15)) * 32, tag, lane < 16);
  BAR();
  STAMP(11);
  {
    constexpr int D3 = 5;
    v4u hf[2][20];
    const unsigned hb = OFF_HF + (8 * P + 2 * w) * 16 * 1024, hhb = OFF_HH + (8 * P + 2 * w) * 8 * 512;
    auto ldh = [&](int u, int ks) -> v4u {
      if (ks < 16) return LDF(hb, u * 16 + ks);
      const v2u a = ld64(ws, lane * 8 + (2 * (ks - 16)) * 512, hhb + u * 4096), c = ld64(ws, lane * 8 + (2 * (ks - 16) + 1) * 512, hhb + u * 4096);
      return v4u{a.x, a.y, c.x, c.y};
    };
    v2u stv[2][2];
#pragma unroll
    for (int ks = 0; ks < D3; ++ks) {
#pragma unroll
      for (int u = 0; u < 2; ++u) hf[u][ks] = ldh(u, ks);
      SCHED();
    }
#pragma unroll
    for (int u = 0; u < 2; ++u)
#pragma unroll
      for (int i = 0; i < 2; ++i) stv[u][i] = ld64(ws, t16 * 8 + (2 * g + i) * 128, OFF_ST + (8 * P + 2 * w + u) * 1024);
    f32x4 a3[2][7];
#pragma unroll
    for (int u = 0; u < 2; ++u)
#pragma unroll
      for (int n = 0; n < 7; ++n) a3[u][n] = f32x4{0.f, 0.f, 0.f, 0.f};
    v4u wf[2][7];
#pragma unroll
    for (int n = 0; n < 7; ++n) wf[0][n] = LDSF(W1A(0, n));
    SCHED();
#pragma unroll
    for (int ks = 0; ks < 20; ++ks) {
      if (ks + D3 < 20) {
#pragma unroll
        for (int u = 0; u < 2; ++u) hf[u][ks + D3] = ldh(u, ks + D3);
      }
      w2f[(46 + ks) / 5][(46 + ks) % 5] = LDF(w2b, 46 + ks);
      if (ks + 1 < 20) {
#pragma unroll
        for (int n = 0; n < 7; ++n) wf[(ks + 1) & 1][n] = LDSF(W1A(ks + 1, n));
      }
#pragma unroll
      for (int n = 0; n < 7; ++n)
#pragma unroll
        for (int u = 0; u < 2; ++u) a3[u][n] = mfma(wf[ks & 1][n], hf[u][ks], a3[u][n]);
#pragma unroll
      for (int n = 0; n < 7; ++n) { SGB(0x100, 1); SGB(0x8, 1); if (n < 5) SGB(0x20, 1); SGB(0x8, 1); }
      SCHED();
    }
    STAMP(12);
    W2LD(66, 70);
    float mean[2], rstd[2];
#pragma unroll
    for (int u = 0; u < 2; ++u) {
      const float m0 = asf(stv[u][0].x), q0 = asf(stv[u][0].y), m1 = asf(stv[u][1].x), q1 = asf(stv[u][1].y);
      const float mu = sum_g(m0 + m1) * 0.125f;
      const float M2 = sum_g(q0 + q1 + 80.f * ((m0 - mu) * (m0 - mu) + (m1 - mu) * (m1 - mu)));
      rstd[u] = __builtin_amdgcn_rsqf(M2 * (1.f / 640.f) + 1e-5f);
      mean[u] = mu * rstd[u];
    }
    unsigned hq[2][7][2];
#pragma unroll
    for (int n = 0; n < 7; ++n) {
      const f32x4 cs = *(LAS f32x4*)(lds + L_IMG + n * 64 + g * 16);
#pragma unroll
      for (int u = 0; u < 2; ++u) {
        const float v0 = fmaxf(a3[u][n][0] * rstd[u] - mean[u] * cs[0], 0.f), v1 = fmaxf(a3[u][n][1] * rstd[u] - mean[u] * cs[1], 0.f);
        const float v2 = fmaxf(a3[u][n][2] * rstd[u] - mean[u] * cs[2], 0.f), v3 = fmaxf(a3[u][n][3] * rstd[u] - mean[u] * cs[3], 0.f);
        hq[u][n][0] = pk2(v0, v1); hq[u][n][1] = pk2(v2, v3);
      }
    }
#pragma unroll
    for (int u = 0; u < 2; ++u) {
      const unsigned di = (8 * P + 2 * w + u) * 16 + m;
      st128(v4u{hq[u][0][0], hq[u][0][1], hq[u][1][0], hq[u][1][1]}, ws, l16, OFF_DF + di * 3072);
      st128(v4u{hq[u][2][0], hq[u][2][1], hq[u][3][0], hq[u][3][1]}, ws, l16 + 1024, OFF_DF + di * 3072);
      st128(v4u{hq[u][4][0], hq[u][4][1], hq[u][5][0], hq[u][5][1]}, ws, l16 + 2048, OFF_DF + di * 3072);
      if ((m & 7) < 6) st64(v2u{hq[u][6][0], hq[u][6][1]}, ws, lane * 8, OFF_DH + di * 512);
    }
  }
  STAMP(21);
  DRAIN();
  BAR();
  if (tid == 0) {
    __hip_atomic_store(flg + (4 * 256 + cuid) * 32, tag, RLX);
    if (cuid == 0) __hip_atomic_store((gu32*)(wsp + OFF_EPOCH), epoch + 1u, RLX);
  }
  STAMP(13);
  if (w == 0) wait_flag(flg + (4 * 256 + P * 16 + (lane & 15)) * 32, tag, lane < 16);
  BAR();
  STAMP(14);
  {
    f32x4 a4[4][5];
#pragma unroll
    for (int u = 0; u < 4; ++u)
#pragma unroll
      for (int n = 0; n < 5; ++n) a4[u][n] = f32x4{0.f, 0.f, 0.f, 0.f};
    const unsigned hb = OFF_DF + ((8 * P + 4 * jl) * 48 + 12 * w) * 1024, dhb = OFF_DH + (8 * P + 4 * jl) * 16 * 512;
    const int ci0 = w < 2 ? 2 * w : w + 2;
    auto lda = [&](int u, int k) -> v4u {
      if (k < 12) return ld128(ws, l16 + (k & 3) * 1024, hb + u * 48 * 1024 + (k >> 2) * 4096);
      const int ci = ci0 + (k - 12), h2 = ci >= 3 ? 1 : 0, tA = 8 * h2 + 2 * (ci - 3 * h2);
      const v2u a = ld64(ws, lane * 8, dhb + (u * 16 + tA) * 512), c = ld64(ws, lane * 8 + 512, dhb + (u * 16 + tA) * 512);
      return v4u{a.x, a.y, c.x, c.y};
    };
    constexpr int DP = 3;
    v4u af[DP + 1][4];
#pragma unroll
    for (int k = 0; k < DP; ++k)
#pragma unroll
      for (int u = 0; u < 4; ++u) { af[k][u] = lda(u, k); SCHED(); }
#pragma unroll
    for (int k = 0; k < 14; ++k) {
      if (k + DP < 14) {
        if (k + DP < 13 || w < 2) {
#pragma unroll
          for (int u = 0; u < 4; ++u) af[(k + DP) % (DP + 1)][u] = lda(u, k + DP);
        }
      }
      if (k < 13 || w < 2) {
#pragma unroll
        for (int u = 0; u < 4; ++u)
#pragma unroll
          for (int n = 0; n < 5; ++n) a4[u][n] = mfma(w2f[k][n], af[k % (DP + 1)][u], a4[u][n]);
      }
#pragma unroll
      for (int u = 0; u < 4; ++u) { SGB(0x20, 2); SGB(0x8, 5); }
      SCHED();
    }
    STAMP(15);
#pragma unroll
    for (int u = 0; u < 4; ++u) {
      if (u != w) {
        const int si = w < u ? w : w - 1;
#pragma unroll
        for (int n = 0; n < 5; ++n) *(LAS f32x4*)(lds + L_A + ((u * 3 + si) * 5 + n) * 1024 + l16) = a4[u][n];
      }
    }
    BAR();
    GAS char* op = (GAS char*)out + (size_t)(64 * (2 * P + jl) + 16 * w + t16) * 1280 + (80 * ct + 4 * g) * 2;
#pragma unroll
    for (int n = 0; n < 5; ++n) {
      f32x4 v = w == 0 ? a4[0][n] : (w == 1 ? a4[1][n] : (w == 2 ? a4[2][n] : a4[3][n]));
      f32x4 p[3];
#pragma unroll
      for (int si = 0; si < 3; ++si) p[si] = *(LAS f32x4*)(lds + L_A + ((w * 3 + si) * 5 + n) * 1024 + l16);
      v = (v + p[0]) + (p[1] + p[2]);
      const v2u o = {pk2(blo(hp[n][0]) + v[0], bhi(hp[n][0]) + v[1]), pk2(blo(hp[n][1]) + v[2], bhi(hp[n][1]) + v[3])};
      *(GAS v2u*)(op + n * 32) = o;
    }
  }
  STAMP(16);
}

extern "C" void kernel_launch(void* const* d_in, const int* in_sizes, int n_in, void* d_out, int out_size, void* d_ws, size_t ws_size,
                              hipStream_t stream) {
  (void)in_sizes; (void)n_in; (void)out_size; (void)ws_size;
  hipFuncSetAttribute((const void*)fused_block, hipFuncAttributeMaxDynamicSharedMemorySize, LDS_BYTES);
  fused_block<<<256, 256, LDS_BYTES, stream>>>((const unsigned short*)d_in[0], (const unsigned short*)d_in[1], (const unsigned short*)d_in[2],
                                               (const unsigned short*)d_in[4], (const unsigned short*)d_in[5], (unsigned short*)d_out,
                                               (char*)d_ws);
}
```

```cpp
#include <hip/hip_runtime.h>
#include <hip/hip_bf16.h>
#include <stdint.h>

typedef unsigned v4u __attribute__((ext_vector_type(4)));
typedef unsigned v3u __attribute__((ext_vector_type(3)));
typedef unsigned v2u __attribute__((ext_vector_type(2)));
typedef float f32x4 __attribute__((ext_vector_type(4)));
typedef short bf16x8 __attribute__((ext_vector_type(8)));
typedef short s16x4 __attribute__((ext_vector_type(4)));
typedef __bf16 bf2 __attribute__((ext_vector_type(2)));
typedef __amdgpu_buffer_rsrc_t rsrc_t;
#define GAS __attribute__((address_space(1)))
#define LAS __attribute__((address_space(3)))
#define DI __device__ __forceinline__
#define RLX __ATOMIC_RELAXED, __HIP_MEMORY_SCOPE_AGENT
typedef GAS unsigned gu32;

#define TS 0
#define CODEPF 1
#define PRETOUCH 1
constexpr int CODE_LINES = 290;

constexpr unsigned OFF_EPOCH = 0;
constexpr unsigned OFF_FLAG = 4096;
constexpr unsigned OFF_FLAGB = OFF_FLAG + 5 * 256 * 128;
constexpr unsigned OFF_TS = 65 * 1024 * 1024;
constexpr unsigned OFF_TS2 = 64 * 1024 * 1024;
constexpr unsigned OFF_XN = 256 * 1024;
constexpr unsigned WQKV_STR = 153600;
constexpr unsigned OFF_WQKV = OFF_XN + 128 * 20 * 1024;
constexpr unsigned OFF_WO = OFF_WQKV + 16 * WQKV_STR;
constexpr unsigned W1_STR = 20 * 7 * 1024;
constexpr unsigned OFF_W1 = OFF_WO + 8 * 102400;
constexpr unsigned W2_STR = 54 * 5 * 1024;
constexpr unsigned OFF_W2 = OFF_W1 + 16 * W1_STR;
constexpr unsigned PART_STR = 3456;
constexpr unsigned OFF_PART = OFF_W2 + 8 * W2_STR;
constexpr unsigned OFF_OM = OFF_PART + 256 * PART_STR;
constexpr unsigned OFF_OR = OFF_OM + 128 * 16 * 1024;
constexpr unsigned OFF_HF = OFF_OR + 128 * 16 * 256;
constexpr unsigned OFF_HH = OFF_HF + 128 * 16 * 1024;
constexpr unsigned OFF_ST = OFF_HH + 128 * 8 * 512;
constexpr unsigned OFF_DF = OFF_ST + 128 * 8 * 128;
constexpr unsigned OFF_DH = OFF_DF + 128 * 48 * 1024;
constexpr unsigned WS_BYTES = OFF_DH + 128 * 16 * 512;

constexpr unsigned L_A = 0;
constexpr unsigned L_B = 102400;
constexpr unsigned L_M = 153600;
constexpr unsigned L_IMG = L_M;
constexpr unsigned L_CS = L_M + 4096;
constexpr unsigned L_PT = L_M + 6144;
constexpr unsigned LDS_BYTES = 163840;
constexpr unsigned EP = 272;

constexpr int AUX_SC1 = 16;
constexpr unsigned SPIN_CAP = 1u << 20;

DI f32x4 mfma(v4u a, v4u b, f32x4 c) {
  return __builtin_amdgcn_mfma_f32_16x16x32_bf16(__builtin_bit_cast(bf16x8, a), __builtin_bit_cast(bf16x8, b), c, 0, 0, 0);
}
DI unsigned pk2(float a, float b) { bf2 r = {(__bf16)a, (__bf16)b}; return __builtin_bit_cast(unsigned, r); }
DI float asf(unsigned u) { return __builtin_bit_cast(float, u); }
DI unsigned asu(float f) { return __builtin_bit_cast(unsigned, f); }
DI float blo(unsigned u) { return asf(u << 16); }
DI float bhi(unsigned u) { return asf(u & 0xffff0000u); }
DI float phi(float t) { return (t > 0.f ? t + 1.f : __expf(t)) + 1e-6f; }
template <int C> DI float dpp_add(float v) { return v + asf((unsigned)__builtin_amdgcn_update_dpp(0, (int)asu(v), C, 0xF, 0xF, true)); }
DI float sum16(float v) { v = dpp_add<0xB1>(v); v = dpp_add<0x4E>(v); v = dpp_add<0x141>(v); v = dpp_add<0x140>(v); return v; }
DI float sum_x16(float v) { const unsigned u = asu(v); auto r = __builtin_amdgcn_permlane16_swap(u, u, false, false); return asf(r[0]) + asf(r[1]); }
DI float sum_x32(float v) { const unsigned u = asu(v); auto r = __builtin_amdgcn_permlane32_swap(u, u, false, false); return asf(r[0]) + asf(r[1]); }
DI float sum_g(float v) { return sum_x32(sum_x16(v)); }
#define SCHED() __builtin_amdgcn_sched_barrier(0)
#define SGB(mask, n) __builtin_amdgcn_sched_group_barrier(mask, n, 0)
#define DRAIN() asm volatile("s_waitcnt vmcnt(0)" ::: "memory")
#define BAR() do { asm volatile("s_waitcnt lgkmcnt(0)" ::: "memory"); __builtin_amdgcn_s_barrier(); asm volatile("" ::: "memory"); } while (0)

DI v4u ld128(rsrc_t rs, unsigned voff, unsigned soff) { return __builtin_amdgcn_raw_buffer_load_b128(rs, voff, soff, AUX_SC1); }
DI void st128(v4u v, rsrc_t rs, unsigned voff, unsigned soff) { __builtin_amdgcn_raw_buffer_store_b128(v, rs, voff, soff, AUX_SC1); }
DI v2u ld64(rsrc_t rs, unsigned voff, unsigned soff) { return __builtin_amdgcn_raw_buffer_load_b64(rs, voff, soff, AUX_SC1); }
DI unsigned ld32(rsrc_t rs, unsigned voff, unsigned soff) { return __builtin_amdgcn_raw_buffer_load_b32(rs, voff, soff, AUX_SC1); }
DI void st64(v2u v, rsrc_t rs, unsigned voff, unsigned soff) { __builtin_amdgcn_raw_buffer_store_b64(v, rs, voff, soff, AUX_SC1); }

DI void dma4(rsrc_t rs, unsigned voff, unsigned soff, unsigned m0v) {
  asm volatile("s_nop 4\n\ts_mov_b32 m0, %0\n\ts_nop 0\n\t"
               "buffer_load_dwordx4 %1, %2, %3 offen sc1 lds\n\t"
               "buffer_load_dwordx4 %1, %2, %3 offen offset:1024 sc1 lds\n\t"
               "buffer_load_dwordx4 %1, %2, %3 offen offset:2048 sc1 lds\n\t"
               "buffer_load_dwordx4 %1, %2, %3 offen offset:3072 sc1 lds"
               :: "s"(m0v), "v"(voff), "s"(rs), "s"(soff) : "m0", "memory");
}
DI void dma1(rsrc_t rs, unsigned voff, unsigned soff, unsigned m0v) {
  asm volatile("s_nop 4\n\ts_mov_b32 m0, %0\n\ts_nop 0\n\t"
               "buffer_load_dwordx4 %1, %2, %3 offen sc1 lds"
               :: "s"(m0v), "v"(voff), "s"(rs), "s"(soff) : "m0", "memory");
}
DI void dma_item(rsrc_t rs, unsigned voff, unsigned m0v) {
  asm volatile("s_nop 4\n\ts_mov_b32 m0, %0\n\ts_nop 0\n\t"
               "buffer_load_dwordx4 %1, %2, 0 offen lds"
               :: "s"(m0v), "v"(voff), "s"(rs) : "m0", "memory");
}
DI v4u item_read(LAS char* img, int lane) {
  LAS char* p = img + ((lane >> 2) & 3) * 128 + (lane >> 4) * 32 + (lane & 3) * 8;
  const s16x4 a = __builtin_amdgcn_ds_read_tr16_b64_v4i16((LAS s16x4*)p);
  const s16x4 b = __builtin_amdgcn_ds_read_tr16_b64_v4i16((LAS s16x4*)(p + 512));
  const v2u x = __builtin_bit_cast(v2u, a), y = __builtin_bit_cast(v2u, b);
  return v4u{x.x, x.y, y.x, y.y};
}
#define LDF(sbase, i) ld128(ws, l16 + ((i) & 3) * 1024, (sbase) + ((i) >> 2) * 4096)
#define LDSF(off) (*(LAS v4u*)(((off) < 65536 ? lb0 : ((off) < 131072 ? lb1 : lb2)) + ((off) & 65535)))
#define W1A(ks, n) (((ks) < 7 ? L_B + (ks) * 7168 : L_A + ((ks) - 7) * 7168) + (n) * 1024)

DI void wait_flag(gu32* f, unsigned tag, bool active) {
  unsigned sp = 0;
  for (;;) {
    unsigned v = active ? __hip_atomic_load(f, RLX) : tag;
    if (__all(v == tag)) break;
    if (++sp > SPIN_CAP) break;
  }
  __builtin_amdgcn_fence(__ATOMIC_ACQUIRE, "wavefront");
}

#define STAMP(k) do { } while (0)

__global__ __launch_bounds__(256, 1) void fused_block(const unsigned short* __restrict__ x, const unsigned short* __restrict__ wqkv,
                                                      const unsigned short* __restrict__ wo, const unsigned short* __restrict__ w1,
                                                      const unsigned short* __restrict__ w2, unsigned short* __restrict__ out,
                                                      char* __restrict__ wsp) {
  extern __shared__ __attribute__((aligned(16))) char smem[];
  LAS char* lds = (LAS char*)smem;
  const unsigned ldsb = (unsigned)(size_t)lds;
  const int tid = threadIdx.x, lane = tid & 63;
  const int w = __builtin_amdgcn_readfirstlane(tid >> 6);
  const int g = lane >> 4, t16 = lane & 15;
  const int b = blockIdx.x, xcd = b & 7, sl = b >> 3;
  const int c2 = xcd & 1, P = 4 * (xcd >> 1) + (sl >> 3), m = 8 * c2 + (sl & 7);
  const int cuid = P * 16 + m;
  const int jl = (m & 7) >> 2, ct = 4 * c2 + (m & 3);
  const unsigned l16 = lane * 16;
  const rsrc_t ws = __builtin_amdgcn_make_buffer_rsrc(wsp, 0, WS_BYTES, 0x00020000);
  gu32* flg = (gu32*)(wsp + OFF_FLAG);
  STAMP(0);
  unsigned warm0 = 0, warm1 = 0;
  if (w == 3) {
    unsigned long long pc;
    asm volatile("s_getpc_b64 %0" : "=s"(pc));
    const GAS char* cp = (const GAS char*)((pc & ~127ull) + (unsigned long long)(sl * 10 + (lane < 10 ? lane : 0)) * 128ull);
    if (sl * 10 + lane < CODE_LINES && lane < 10) asm volatile("global_load_dword %0, %1, off" : "=v"(warm0) : "v"(cp) : "memory");
  }
  if (w == 2) {
    const GAS char* tp = lane == 0 ? (const GAS char*)wo : lane == 1 ? (const GAS char*)w1 : lane == 2 ? (const GAS char*)w2 : lane == 3 ? (const GAS char*)out
                       : lane == 4 ? (const GAS char*)w1 + 2097152 : lane == 5 ? (const GAS char*)w2 + 2097152 : lane == 6 ? (const GAS char*)out + 2097152
                       : (const GAS char*)wsp + (size_t)(lane - 7) * 2097152;
    if (lane < 19) asm volatile("global_load_dword %0, %1, off" : "=v"(warm1) : "v"(tp) : "memory");
  }
  const unsigned epoch = __hip_atomic_load((gu32*)(wsp + OFF_EPOCH), RLX);
  LAS char* lb0 = lds + l16; LAS char* lb1 = lb0 + 65536; LAS char* lb2 = lb0 + 131072;
  asm volatile("" : "+v"(lb1), "+v"(lb2));

  v2u xv[5];
  {
    const GAS char* xp = (const GAS char*)(x + (size_t)(128 * P + 8 * m + 2 * w + (lane >> 5)) * 640) + (lane & 31) * 8;
    asm volatile("global_load_dwordx2 %0, %5, off\n\tglobal_load_dwordx2 %1, %5, off offset:256\n\tglobal_load_dwordx2 %2, %5, off offset:512\n\t"
                 "global_load_dwordx2 %3, %5, off offset:768\n\tglobal_load_dwordx2 %4, %5, off offset:1024"
                 : "=&v"(xv[0]), "=&v"(xv[1]), "=&v"(xv[2]), "=&v"(xv[3]), "=&v"(xv[4]) : "v"(xp) : "memory");
  }
  const int r8 = lane >> 3, ch8 = lane & 7;
  {
    const rsrc_t rq = __builtin_amdgcn_make_buffer_rsrc((void*)wqkv, 0, 640 * 1920 * 2, 0x00020000);
    const int col0 = ch8 < 5 ? 640 + 40 * m + 8 * ch8 : 1280 + 40 * m + 8 * (ch8 - 5);
    const int col1 = ch8 < 2 ? 1280 + 40 * m + 24 + 8 * ch8 : 40 * m + 8 * ((ch8 < 7 ? ch8 : 6) - 2);
#pragma unroll
    for (int k = 0; k < 3; ++k) {
      const int q = w + 4 * k;
      if (q < 10) dma_item(rq, (unsigned)((40 * P + 8 * (q >> 1) + r8) * 1920 + ((q & 1) ? col1 : col0)) * 2, ldsb + L_A + 16384 + q * 1024);
    }
  }
  STAMP(22);
  unsigned dxa[3], dwa[3];
#pragma unroll
  for (int i = 0; i < 3; ++i) {
    const int L = 160 * w + 64 * i + lane, c = L >> 3, tt = 8 * (m & 1) + (L & 7);
    dxa[i] = OFF_XN + ((8 * P + (m >> 1)) * 20 + (c >> 2)) * 1024 + ((c & 3) * 16 + tt) * 16;
    const int q = w + 4 * i, pos = 64 * (q & 1) + lane, nb = pos >> 4, n = pos & 15;
    const int rg = 5 * P + (q >> 1), ks = rg >> 2, gg = rg & 3;
    dwa[i] = OFF_WQKV + m * WQKV_STR + (nb < 7 ? (nb * 20 + ks) * 1024 + (gg * 16 + n) * 16 : 143360 + ks * 512 + (gg * 8 + n) * 16);
  }
  const int gw = cuid * 4 + w;
  const int it0k = gw < 800 ? 0 : (gw < 912 ? 1 : 2), it0 = gw < 800 ? gw : (gw < 912 ? 2048 + gw - 800 : 2048 + gw - 912);
  unsigned srcb[5], dstb[5];
  {
    auto src_w1 = [&](int u) {
      const int rg = u / 27, cg = u - 27 * rg, ks = rg >> 2, gg = rg & 3;
      const int nA = ks < 16 ? 5 * (ks >> 1) + 2 * (ks & 1) : 10 * (ks - 16) + 4, nB = ks < 16 ? nA + 1 : nA + 5;
      return (unsigned)((16 * (r8 < 4 ? nA : nB) + 4 * gg + (r8 & 3)) * 1728 + 64 * cg + 8 * ch8) * 2;
    };
    auto src_w2 = [&](int u) {
      const int rg = u / 10, cg = u - 10 * rg, sq = rg >> 2, gg = rg & 3;
      const int wq = sq < 28 ? sq / 14 : 2 + (sq - 28) / 13, k = sq - (wq < 2 ? 14 * wq : 28 + 13 * (wq - 2));
      int nA, nB;
      if (k < 12) {
        const int tt = 4 * (wq & 1) + k / 3, i = k % 3;
        nA = 54 * (wq >> 1) + (tt < 6 ? 7 * tt : 42 + 6 * (tt - 6)) + 2 * i; nB = nA + 1;
      } else {
        const int ci = (wq < 2 ? 2 * wq : wq + 2) + (k - 12), h2 = ci / 3, a3 = ci - 3 * h2;
        nA = 54 * h2 + 14 * a3 + 6; nB = nA + 7;
      }
      return (unsigned)((16 * (r8 < 4 ? nA : nB) + 4 * gg + (r8 & 3)) * 640 + 64 * cg + 8 * ch8) * 2;
    };
    auto src_wo = [&](int u) {
      const int rg = u / 10, cg = u - 10 * rg, ks = rg >> 2, gg = rg & 3;
      const int row = ks < 16 ? 40 * ks + 5 * gg + (r8 < 5 ? r8 : 15 + r8) : 40 * (4 * (ks - 16) + (r8 >> 1)) + 23 + 5 * gg + (r8 & 1);
      return (unsigned)(row * 640 + 64 * cg + 8 * ch8) * 2;
    };
    auto dst_w1 = [&](int u) {
      const int rg = u / 27, cg = u - 27 * rg, ks = rg >> 2, gg = rg & 3, n = 64 * cg + lane;
      const int nb = n >> 4, h2 = nb >= 54 ? 1 : 0, nbp = nb - 54 * h2;
      const int tt = nbp < 42 ? nbp / 7 : 6 + (nbp - 42) / 6, nbl = nbp - (tt < 6 ? 7 * tt : 42 + 6 * (tt - 6));
      return OFF_W1 + (8 * h2 + tt) * W1_STR + (ks * 7 + nbl) * 1024 + (gg * 16 + (n & 15)) * 16;
    };
    auto dst_5 = [&](int u, unsigned base, unsigned str) {
      const int rg = u / 10, cg = u - 10 * rg, ks = rg >> 2, gg = rg & 3, n = 64 * cg + lane;
      const int c = n / 80, nl = n - 80 * c;
      return base + c * str + (ks * 5 + (nl >> 4)) * 1024 + (gg * 16 + (nl & 15)) * 16;
    };
    srcb[0] = it0k == 0 ? src_wo(it0) : (it0k == 1 ? src_w1(it0) : src_w2(it0));
    dstb[0] = it0k == 1 ? dst_w1(it0) : dst_5(it0, it0k == 0 ? OFF_WO : OFF_W2, it0k == 0 ? 102400u : W2_STR);
    srcb[1] = src_w1(gw); srcb[2] = src_w1(gw + 1024); srcb[3] = src_w2(gw); srcb[4] = src_w2(gw + 1024);
    dstb[1] = dst_w1(gw); dstb[2] = dst_w1(gw + 1024); dstb[3] = dst_5(gw, OFF_W2, W2_STR); dstb[4] = dst_5(gw + 1024, OFF_W2, W2_STR);
  }
  STAMP(23);
  asm volatile("s_waitcnt vmcnt(2)" : "+v"(xv[0]), "+v"(xv[1]), "+v"(xv[2]), "+v"(xv[3]), "+v"(xv[4]) :: "memory");
  {
    const rsrc_t ro = __builtin_amdgcn_make_buffer_rsrc((void*)wo, 0, 640 * 640 * 2, 0x00020000);
    const rsrc_t r1 = __builtin_amdgcn_make_buffer_rsrc((void*)w1, 0, 640 * 1728 * 2, 0x00020000);
    const rsrc_t r2 = __builtin_amdgcn_make_buffer_rsrc((void*)w2, 0, 1728 * 640 * 2, 0x00020000);
    const unsigned sb = ldsb + L_B + w * 5120;
    if (it0k == 0) dma_item(ro, srcb[0], sb); else if (it0k == 1) dma_item(r1, srcb[0], sb); else dma_item(r2, srcb[0], sb);
    dma_item(r1, srcb[1], sb + 1024); dma_item(r1, srcb[2], sb + 2048); dma_item(r2, srcb[3], sb + 3072); dma_item(r2, srcb[4], sb + 4096);
  }
  {
    float f[20];
#pragma unroll
    for (int i = 0; i < 5; ++i) { f[4 * i] = blo(xv[i].x); f[4 * i + 1] = bhi(xv[i].x); f[4 * i + 2] = blo(xv[i].y); f[4 * i + 3] = bhi(xv[i].y); }
    float s4[4];
#pragma unroll
    for (int j = 0; j < 4; ++j) s4[j] = (f[j] + f[4 + j]) + (f[8 + j] + f[12 + j]) + f[16 + j];
    const float mean = sum_x16(sum16((s4[0] + s4[1]) + (s4[2] + s4[3]))) * (1.f / 640.f);
#pragma unroll
    for (int i = 0; i < 20; ++i) f[i] -= mean;
#pragma unroll
    for (int j = 0; j < 4; ++j) s4[j] = (f[j] * f[j] + f[4 + j] * f[4 + j]) + (f[8 + j] * f[8 + j] + f[12 + j] * f[12 + j]) + f[16 + j] * f[16 + j];
    const float rstd = __builtin_amdgcn_rsqf(sum_x16(sum16((s4[0] + s4[1]) + (s4[2] + s4[3]))) * (1.f / 640.f) + 1e-5f);
    LAS char* sc = lds + L_A + ((lane & 31) >> 1) * 128 + (2 * w + (lane >> 5)) * 16 + (lane & 1) * 8;
#pragma unroll
    for (int i = 0; i < 5; ++i)
      *(LAS v2u*)(sc + i * 2048) = v2u{pk2(f[4 * i] * rstd, f[4 * i + 1] * rstd), pk2(f[4 * i + 2] * rstd, f[4 * i + 3] * rstd)};
  }
  STAMP(24);
  asm volatile("s_waitcnt vmcnt(5)" ::: "memory");
  asm volatile("" :: "v"(warm0), "v"(warm1));
  BAR();
  STAMP(25);
#pragma unroll
  for (int i = 0; i < 3; ++i)
    if (i < 2 || lane < 32) st128(*(LAS v4u*)(lds + L_A + (160 * w + 64 * i + lane) * 16), ws, dxa[i], 0);
  STAMP(32);
#pragma unroll
  for (int k = 0; k < 3; ++k) {
    const int q = w + 4 * k;
    if (q < 10) {
      const v4u o = item_read(lds + L_A + 16384 + q * 1024, lane);
      if (64 * (q & 1) + lane < 120) st128(o, ws, dwa[k], 0);
    }
  }
  STAMP(33);
  const unsigned tag = __builtin_amdgcn_readfirstlane(epoch) * 2654435761u + 0x9E3779B9u;
  STAMP(26);
  DRAIN();
  BAR();
  if (tid == 0) __hip_atomic_store(flg + (0 * 256 + cuid) * 32, tag, RLX);
  STAMP(1);
  *(LAS v4u*)(lds + L_IMG + tid * 16) = v4u{0u, 0u, 0u, 0u};
#pragma unroll
  for (int k = 0; k < 5; ++k) st128(item_read(lds + L_B + w * 5120 + k * 1024, lane), ws, dstb[k], 0);
  STAMP(2);

  if (w == 0) {
    const int l = lane & 15;
    wait_flag(flg + (0 * 256 + (lane < 16 ? P * 16 + l : l * 16 + m)) * 32, tag, lane < 32);
  }
  BAR();
  STAMP(3);
  v4u fb;
  v4u xn[2][20];
  f32x4 akv[2][5];
  v4u wqs[13];
  {
    constexpr int D1 = 5;
    for (int q = w; q < 25; q += 4) dma4(ws, l16, OFF_WQKV + m * WQKV_STR + q * 4096, ldsb + L_A + q * 4096);
    const unsigned xb = OFF_XN + (8 * P + 2 * w) * 20 * 1024;
#pragma unroll
    for (int ks = 0; ks < D1; ++ks) {
#pragma unroll
      for (int u = 0; u < 2; ++u) xn[u][ks] = LDF(xb, u * 20 + ks);
      SCHED();
    }
    asm volatile("s_waitcnt vmcnt(10)" ::: "memory");
    BAR();
    if (tid == 0) __hip_atomic_store((gu32*)(wsp + OFF_FLAGB) + cuid, tag, RLX);
    STAMP(17);
#pragma unroll
    for (int u = 0; u < 2; ++u)
#pragma unroll
      for (int n = 0; n < 5; ++n) akv[u][n] = f32x4{0.f, 0.f, 0.f, 0.f};
    v4u wf[2][5];
#pragma unroll
    for (int n = 0; n < 5; ++n) wf[0][n] = LDSF(L_A + (n * 20) * 1024);
    SCHED();
    const unsigned wqb = OFF_WQKV + m * WQKV_STR + 102400 + w * 1024;
#pragma unroll
    for (int ks = 0; ks < 20; ++ks) {
      if (ks + D1 < 20) {
#pragma unroll
        for (int u = 0; u < 2; ++u) xn[u][ks + D1] = LDF(xb, u * 20 + ks + D1);
      } else {
#pragma unroll
        for (int i = (ks + D1 - 20) * 3; i < (ks + D1 - 20) * 3 + 3 && i < 13; ++i) wqs[i] = ld128(ws, l16, wqb + (i < 12 || w < 2 ? i : 11) * 4096);
        if (ks == 19) fb = __builtin_amdgcn_raw_buffer_load_b128(ws, OFF_FLAGB + l16, 0, AUX_SC1);
      }
      if (ks + 1 < 20) {
#pragma unroll
        for (int n = 0; n < 5; ++n) wf[(ks + 1) & 1][n] = LDSF(L_A + (n * 20 + ks + 1) * 1024);
      }
#pragma unroll
      for (int n = 0; n < 5; ++n)
#pragma unroll
        for (int u = 0; u < 2; ++u) akv[u][n] = mfma(xn[u][ks], wf[ks & 1][n], akv[u][n]);
#pragma unroll
      for (int n = 0; n < 5; ++n) { SGB(0x100, 1); SGB(0x8, 1); if (n < 3) SGB(0x20, 1); SGB(0x8, 1); }
      SCHED();
    }
  }
  STAMP(4);
  if (!__all(fb.x == tag && fb.y == tag && fb.z == tag && fb.w == tag)) {
    unsigned sp = 0;
    for (;;) {
      asm volatile("" ::: "memory");
      fb = __builtin_amdgcn_raw_buffer_load_b128(ws, OFF_FLAGB + l16, 0, AUX_SC1);
      if (__all(fb.x == tag && fb.y == tag && fb.z == tag && fb.w == tag)) break;
      if (++sp > SPIN_CAP) break;
    }
  }
  v4u w2f[14][5];
  const int k4s = w < 2 ? 14 * w : 28 + 13 * (w - 2);
  const unsigned w2b = OFF_W2 + ct * W2_STR + k4s * 5 * 1024;
#define W2LD(f0, f1) _Pragma("unroll") for (int f_ = (f0); f_ < (f1); ++f_) w2f[f_ / 5][f_ % 5] = LDF(w2b, f_); SCHED()
  W2LD(0, 12);
  BAR();
#pragma unroll
  for (int u = 0; u < 2; ++u)
#pragma unroll
    for (int n = 0; n < 5; ++n) {
      f32x4 a = akv[u][n];
      if (n < 2) { a[0] = phi(a[0]); a[1] = phi(a[1]); a[2] = phi(a[2]); a[3] = phi(a[3]); }
      else if (n == 2) { if (t16 < 8) { a[0] = phi(a[0]); a[1] = phi(a[1]); a[2] = phi(a[2]); a[3] = phi(a[3]); } }
      *(LAS v2u*)(lds + L_A + (16 * n + t16) * EP + (32 * w + 16 * u + 4 * g) * 2) = v2u{pk2(a[0], a[1]), pk2(a[2], a[3])};
    }
  if (tid < 16) *(LAS v4u*)(lds + L_A + 80 * EP + tid * 16) = v4u{0x3F803F80u, 0x3F803F80u, 0x3F803F80u, 0x3F803F80u};
  BAR();
  W2LD(12, 24);
  STAMP(27);
  {
    const int hd = w >> 1, dt = w & 1;
    f32x4 t0 = {0.f, 0.f, 0.f, 0.f}, t1 = t0;
    const unsigned ra = L_A + (20 * hd + 16 * dt + t16) * EP + g * 16;
    const unsigned rb0 = L_A + (40 + 20 * hd + t16) * EP + g * 16;
    const unsigned rb1 = L_A + (t16 < 4 ? 56 + 20 * hd + t16 : 80) * EP + g * 16;
    v4u fa[4], f0[4], f1[4];
#pragma unroll
    for (int s = 0; s < 4; ++s) { fa[s] = *(LAS v4u*)(lds + ra + s * 64); f0[s] = *(LAS v4u*)(lds + rb0 + s * 64); f1[s] = *(LAS v4u*)(lds + rb1 + s * 64); }
#pragma unroll
    for (int s = 0; s < 4; ++s) { t0 = mfma(fa[s], f0[s], t0); t1 = mfma(fa[s], f1[s], t1); }
    LAS char* pt = lds + L_PT + hd * 1680;
    if (dt == 0) {
      *(LAS f32x4*)(pt + l16) = t0;
      if (t16 < 5) *(LAS f32x4*)(pt + 1024 + (g * 5 + t16) * 16) = t1;
    } else if (g == 0) {
      *(LAS f32x4*)(pt + 1344 + t16 * 16) = t0;
      if (t16 < 5) *(LAS f32x4*)(pt + 1600 + t16 * 16) = t1;
    }
    if (tid >= 250) *(LAS v4u*)(lds + L_PT + 3360 + (tid - 250) * 16) = v4u{0u, 0u, 0u, 0u};
  }
  BAR();
  if (tid < 216) st128(*(LAS v4u*)(lds + L_PT + tid * 16), ws, tid * 16, OFF_PART + (m * 16 + P) * PART_STR);
#pragma unroll
  for (int i = 0; i < 13; ++i)
    if (i < 12 || w < 2) *(LAS v4u*)(lds + L_B + (w + 4 * i) * 1024 + l16) = wqs[i];
  STAMP(28);
  DRAIN();
  BAR();
  if (tid == 0) __hip_atomic_store(flg + (1 * 256 + cuid) * 32, tag, RLX);
  STAMP(5);

  unsigned qp[2][3][2];
  {
    f32x4 aq[2][3];
#pragma unroll
    for (int u = 0; u < 2; ++u)
#pragma unroll
      for (int n = 0; n < 3; ++n) aq[u][n] = f32x4{0.f, 0.f, 0.f, 0.f};
    LAS char* lq7 = lds + L_B + 40960 + g * 128 + (t16 & 7) * 16;
    v4u wf[3][3];
#pragma unroll
    for (int k = 0; k < 2; ++k) { wf[k][0] = LDSF(L_B + k * 1024); wf[k][1] = LDSF(L_B + (20 + k) * 1024); wf[k][2] = *(LAS v4u*)(lq7 + k * 512); }
    SCHED();
#pragma unroll
    for (int ks = 0; ks < 20; ++ks) {
      if (ks % 3 == 0) {
        const int q = w + 4 * (ks / 3);
        if (q < 25) dma4(ws, l16, OFF_WO + ct * 102400 + q * 4096, ldsb + L_A + q * 4096);
      }
      if (ks + 2 < 20) {
        wf[(ks + 2) % 3][0] = LDSF(L_B + (ks + 2) * 1024);
        wf[(ks + 2) % 3][1] = LDSF(L_B + (20 + ks + 2) * 1024);
        wf[(ks + 2) % 3][2] = *(LAS v4u*)(lq7 + (ks + 2) * 512);
      }
#pragma unroll
      for (int n = 0; n < 3; ++n)
#pragma unroll
        for (int u = 0; u < 2; ++u) aq[u][n] = mfma(wf[ks % 3][n], xn[u][ks], aq[u][n]);
#pragma unroll
      for (int n = 0; n < 3; ++n) { SGB(0x100, 1); SGB(0x8, 2); }
      SCHED();
    }
#pragma unroll
    for (int u = 0; u < 2; ++u)
#pragma unroll
      for (int n = 0; n < 3; ++n) {
        qp[u][n][0] = pk2(phi(aq[u][n][0]), phi(aq[u][n][1]));
        qp[u][n][1] = pk2(phi(aq[u][n][2]), phi(aq[u][n][3]));
      }
  }
  W2LD(24, 34);
  unsigned imgoff, imgoffz;
  {
    const int hd = tid >= 105 ? 1 : 0, u = tid - 105 * hd;
    int dq, e;
    if (u < 64) { dq = u >> 4; e = u & 15; }
    else if (u < 84) { const int v = u - 64; dq = v / 5; e = 16 + v - 5 * dq; }
    else if (u < 100) { dq = 4; e = u - 84; }
    else { dq = 4; e = 16 + u - 100; }
    int gq;
    if (hd == 0) gq = dq < 4 ? dq : 0; else gq = dq < 3 ? dq + 1 : (dq == 3 ? 0 : 1);
    const int j0 = dq < 4 ? 0 : 4, ge = e / 5, re = e - 5 * ge;
    const unsigned ib = L_IMG + hd * 2048 + gq * 256 + j0 * 2;
    imgoff = ib + (re < 4 ? (4 * ge + re) * 16 : 1024 + (4 * ge) * 16);
    imgoffz = e == 20 ? ib + 1024 + 16 : 0u;
  }
  STAMP(6);

  if (w == 0) wait_flag(flg + (1 * 256 + (lane & 15) * 16 + m) * 32, tag, lane < 16);
  BAR();
  STAMP(7);
  if (tid < 210) {
    v4u pv[16];
#pragma unroll
    for (int i = 0; i < 16; ++i) pv[i] = ld128(ws, tid * 16, OFF_PART + (m * 16 + i) * PART_STR);
    f32x4 s[4];
#pragma unroll
    for (int i = 0; i < 4; ++i)
      s[i] = (__builtin_bit_cast(f32x4, pv[4 * i]) + __builtin_bit_cast(f32x4, pv[4 * i + 1])) +
             (__builtin_bit_cast(f32x4, pv[4 * i + 2]) + __builtin_bit_cast(f32x4, pv[4 * i + 3]));
    const f32x4 st = (s[0] + s[1]) + (s[2] + s[3]);
    const v2u val = v2u{pk2(st[0], st[1]), pk2(st[2], st[3])};
    if (imgoffz == 0u) *(LAS v2u*)(lds + imgoff) = val;
    else {
#pragma unroll
      for (int ge = 0; ge < 4; ++ge) *(LAS v2u*)(lds + imgoffz + ge * 64) = val;
    }
  }
  BAR();
  STAMP(18);
  {
    v4u ka[2][2];
#pragma unroll
    for (int hd = 0; hd < 2; ++hd)
#pragma unroll
      for (int bb = 0; bb < 2; ++bb) ka[hd][bb] = *(LAS v4u*)(lds + L_IMG + hd * 2048 + bb * 1024 + l16);
#pragma unroll
    for (int u = 0; u < 2; ++u) {
      v4u qa, qb;
      qa.x = qp[u][0][0]; qa.y = qp[u][0][1]; qa.z = g == 0 ? qp[u][1][0] : 0u; qa.w = g == 0 ? qp[u][1][1] : 0u;
      qb.x = g == 0 ? qp[u][2][0] : qp[u][1][0]; qb.y = g == 0 ? qp[u][2][1] : qp[u][1][1];
      qb.z = g == 1 ? qp[u][2][0] : 0u; qb.w = g == 1 ? qp[u][2][1] : 0u;
      const f32x4 z4 = {0.f, 0.f, 0.f, 0.f};
      const f32x4 a0 = mfma(ka[0][0], qa, z4), a1 = mfma(ka[0][1], qa, z4);
      const f32x4 b0 = mfma(ka[1][0], qb, z4), b1 = mfma(ka[1][1], qb, z4);
      const float ra = __builtin_amdgcn_rcpf(a1[1] + 1e-6f), rb = __builtin_amdgcn_rcpf(b1[1] + 1e-6f);
      const unsigned d0 = pk2(a0[0] * ra, a0[1] * ra), d1 = pk2(a0[2] * ra, a0[3] * ra), d2 = pk2(a1[0] * ra, b0[0] * rb),
                     d3 = pk2(b0[1] * rb, b0[2] * rb), d4 = pk2(b0[3] * rb, b1[0] * rb);
      const unsigned oi = (8 * P + 2 * w + u) * 16 + m;
      st128(v4u{d0, d1, d2, d3}, ws, l16, OFF_OM + oi * 1024);
      __builtin_amdgcn_raw_buffer_store_b32(d4, ws, lane * 4, OFF_OR + oi * 256, AUX_SC1);
    }
  }
  DRAIN();
  BAR();
  if (tid == 0) __hip_atomic_store(flg + (2 * 256 + cuid) * 32, tag, RLX);
  STAMP(8);
  for (int q = w; q < 12; q += 4) dma4(ws, l16, OFF_W1 + m * W1_STR + q * 4096, ldsb + L_B + q * 4096);
  if (w == 0) dma1(ws, l16, OFF_W1 + m * W1_STR + 49152, ldsb + L_B + 49152);
  v2u xr[5];
  {
    const GAS char* xp = (const GAS char*)x + (size_t)(64 * (2 * P + jl) + 16 * w + t16) * 1280 + (80 * ct + 4 * g) * 2;
#pragma unroll
    for (int n = 0; n < 5; ++n) xr[n] = *(const GAS v2u*)(xp + n * 32);
  }
  v4u w1r[23];
  const unsigned w1rb = OFF_W1 + m * W1_STR + 50176 + w * 1024;
#pragma unroll
  for (int i = 0; i < 12; ++i) w1r[i] = ld128(ws, l16, w1rb + i * 4096);
  SCHED();

  STAMP(29);
  if (w == 0) wait_flag(flg + (2 * 256 + P * 16 + (lane & 15)) * 32, tag, lane < 16);
  STAMP(30);
  BAR();
  STAMP(9);
  unsigned hp[5][2];
  {
    constexpr int D2 = 8;
    v4u of[20];
    const unsigned ob = OFF_OM + (8 * P + 4 * jl + w) * 16 * 1024, orb = OFF_OR + (8 * P + 4 * jl + w) * 16 * 256;
#define LDO(ks) ((ks) < 16 ? LDF(ob, ks) : v4u{ld32(ws, lane * 4 + (4 * ((ks) - 16)) * 256, orb), ld32(ws, lane * 4 + (4 * ((ks) - 16) + 1) * 256, orb), \
                                              ld32(ws, lane * 4 + (4 * ((ks) - 16) + 2) * 256, orb), ld32(ws, lane * 4 + (4 * ((ks) - 16) + 3) * 256, orb)})
#pragma unroll
    for (int ks = 0; ks < D2; ++ks) { of[ks] = LDO(ks); SCHED(); }
    f32x4 ah[5];
#pragma unroll
    for (int n = 0; n < 5; ++n) ah[n] = f32x4{0.f, 0.f, 0.f, 0.f};
    v4u wf[3][5];
#pragma unroll
    for (int n = 0; n < 5; ++n) { wf[0][n] = LDSF(L_A + n * 1024); wf[1][n] = LDSF(L_A + (5 + n) * 1024); }
    SCHED();
#pragma unroll
    for (int ks = 0; ks < 20; ++ks) {
      if (ks + D2 < 20) of[ks + D2] = LDO(ks + D2);
      else {
        const int i0 = ks < 15 ? 12 + 2 * (ks - 12) : 15 + (ks - 12);
        w1r[i0] = ld128(ws, l16, w1rb + (i0 < 22 || w < 3 ? i0 : 21) * 4096);
        if (ks < 15) w1r[i0 + 1] = ld128(ws, l16, w1rb + (i0 + 1) * 4096);
      }
      if (ks + 2 < 20) {
#pragma unroll
        for (int n = 0; n < 5; ++n) wf[(ks + 2) % 3][n] = LDSF(L_A + ((ks + 2) * 5 + n) * 1024);
      }
#pragma unroll
      for (int n = 0; n < 5; ++n) ah[n] = mfma(wf[ks % 3][n], of[ks], ah[n]);
#pragma unroll
      for (int n = 0; n < 5; ++n) { SGB(0x100, 1); SGB(0x8, 1); if (n < 4) SGB(0x20, 1); }
      SCHED();
    }
    STAMP(19);
    float hv[20];
#pragma unroll
    for (int n = 0; n < 5; ++n) {
      hp[n][0] = pk2(blo(xr[n].x) + ah[n][0], bhi(xr[n].x) + ah[n][1]);
      hp[n][1] = pk2(blo(xr[n].y) + ah[n][2], bhi(xr[n].y) + ah[n][3]);
    }
    const unsigned hi = (8 * P + 4 * jl + w) * 8 + ct;
    st128(v4u{hp[0][0], hp[0][1], hp[1][0], hp[1][1]}, ws, l16, OFF_HF + hi * 2048);
    st128(v4u{hp[2][0], hp[2][1], hp[3][0], hp[3][1]}, ws, l16 + 1024, OFF_HF + hi * 2048);
    st64(v2u{hp[4][0], hp[4][1]}, ws, lane * 8, OFF_HH + hi * 512);
    float s4[5];
#pragma unroll
    for (int n = 0; n < 5; ++n) {
      hv[4 * n] = blo(hp[n][0]); hv[4 * n + 1] = bhi(hp[n][0]); hv[4 * n + 2] = blo(hp[n][1]); hv[4 * n + 3] = bhi(hp[n][1]);
      s4[n] = (hv[4 * n] + hv[4 * n + 1]) + (hv[4 * n + 2] + hv[4 * n + 3]);
    }
    const float mi = sum_g((s4[0] + s4[1]) + (s4[2] + s4[3]) + s4[4]) * (1.f / 80.f);
#pragma unroll
    for (int n = 0; n < 5; ++n) {
      const float a = hv[4 * n] - mi, bq = hv[4 * n + 1] - mi, c = hv[4 * n + 2] - mi, d = hv[4 * n + 3] - mi;
      s4[n] = (a * a + bq * bq) + (c * c + d * d);
    }
    const float q = sum_g((s4[0] + s4[1]) + (s4[2] + s4[3]) + s4[4]);
    if (g == 0) st64(v2u{asu(mi), asu(q)}, ws, t16 * 8, OFF_ST + ((8 * P + 4 * jl + w) * 8 + ct) * 128);
  }
  DRAIN();
  BAR();
  if (tid == 0) __hip_atomic_store(flg + (3 * 256 + cuid) * 32, tag, RLX);
  STAMP(10);
#pragma unroll
  for (int i = 0; i < 23; ++i)
    if (i < 22 || w < 3) *(LAS v4u*)(lds + L_A + (w + 4 * i) * 1024 + l16) = w1r[i];
  W2LD(34, 46);
  BAR();
  {
    f32x4 cs[7];
#pragma unroll
    for (int n = 0; n < 7; ++n) cs[n] = f32x4{0.f, 0.f, 0.f, 0.f};
    const v4u ones = {0x3F803F80u, 0x3F803F80u, 0x3F803F80u, 0x3F803F80u};
    v4u wf[2][7];
#pragma unroll
    for (int n = 0; n < 7; ++n) wf[0][n] = *(LAS v4u*)(lb0 + L_B + w * 7168 + n * 1024);
#pragma unroll
    for (int i = 0; i < 5; ++i) {
      if (i + 1 < 5) {
        const int ks = w + 4 * (i + 1);
        const unsigned a = ks < 7 ? L_B + ks * 7168 : L_A + (ks - 7) * 7168;
#pragma unroll
        for (int n = 0; n < 7; ++n) wf[(i + 1) & 1][n] = *(LAS v4u*)(lb0 + a + n * 1024);
      }
#pragma unroll
      for (int n = 0; n < 7; ++n) cs[n] = mfma(wf[i & 1][n], ones, cs[n]);
    }
    if (t16 == 0) {
#pragma unroll
      for (int n = 0; n < 7; ++n) *(LAS f32x4*)(lds + L_CS + (w * 7 + n) * 64 + g * 16) = cs[n];
    }
    BAR();
    if (lane < 8 && w + 4 * (lane >> 2) < 7) {
      const int n = w + 4 * (lane >> 2), gq = lane & 3;
      f32x4 cw[4];
#pragma unroll
      for (int ww = 0; ww < 4; ++ww) cw[ww] = *(LAS f32x4*)(lds + L_CS + (ww * 7 + n) * 64 + gq * 16);
      *(LAS f32x4*)(lds + L_IMG + n * 64 + gq * 16) = (cw[0] + cw[1]) + (cw[2] + cw[3]);
    }
  }
  STAMP(20);

  if (w == 0) wait_flag(flg + (3 * 256 + P * 16 + (lane & 15)) * 32, tag, lane < 16);
  BAR();
  STAMP(11);
  {
    constexpr int D3 = 5;
    v4u hf[2][20];
    const unsigned hb = OFF_HF + (8 * P + 2 * w) * 16 * 1024, hhb = OFF_HH + (8 * P + 2 * w) * 8 * 512;
    auto ldh = [&](int u, int ks) -> v4u {
      if (ks < 16) return LDF(hb, u * 16 + ks);
      const v2u a = ld64(ws, lane * 8 + (2 * (ks - 16)) * 512, hhb + u * 4096), c = ld64(ws, lane * 8 + (2 * (ks - 16) + 1) * 512, hhb + u * 4096);
      return v4u{a.x, a.y, c.x, c.y};
    };
    v2u stv[2][2];
#pragma unroll
    for (int ks = 0; ks < D3; ++ks) {
#pragma unroll
      for (int u = 0; u < 2; ++u) hf[u][ks] = ldh(u, ks);
      SCHED();
    }
#pragma unroll
    for (int u = 0; u < 2; ++u)
#pragma unroll
      for (int i = 0; i < 2; ++i) stv[u][i] = ld64(ws, t16 * 8 + (2 * g + i) * 128, OFF_ST + (8 * P + 2 * w + u) * 1024);
    f32x4 a3[2][7];
#pragma unroll
    for (int u = 0; u < 2; ++u)
#pragma unroll
      for (int n = 0; n < 7; ++n) a3[u][n] = f32x4{0.f, 0.f, 0.f, 0.f};
    v4u wf[2][7];
#pragma unroll
    for (int n = 0; n < 7; ++n) wf[0][n] = LDSF(W1A(0, n));
    SCHED();
#pragma unroll
    for (int ks = 0; ks < 20; ++ks) {
      if (ks + D3 < 20) {
#pragma unroll
        for (int u = 0; u < 2; ++u) hf[u][ks + D3] = ldh(u, ks + D3);
      }
      w2f[(46 + ks) / 5][(46 + ks) % 5] = LDF(w2b, 46 + ks);
      if (ks + 1 < 20) {
#pragma unroll
        for (int n = 0; n < 7; ++n) wf[(ks + 1) & 1][n] = LDSF(W1A(ks + 1, n));
      }
#pragma unroll
      for (int n = 0; n < 7; ++n)
#pragma unroll
        for (int u = 0; u < 2; ++u) a3[u][n] = mfma(wf[ks & 1][n], hf[u][ks], a3[u][n]);
#pragma unroll
      for (int n = 0; n < 7; ++n) { SGB(0x100, 1); SGB(0x8, 1); if (n < 5) SGB(0x20, 1); SGB(0x8, 1); }
      SCHED();
    }
    STAMP(12);
    W2LD(66, 70);
    float mean[2], rstd[2];
#pragma unroll
    for (int u = 0; u < 2; ++u) {
      const float m0 = asf(stv[u][0].x), q0 = asf(stv[u][0].y), m1 = asf(stv[u][1].x), q1 = asf(stv[u][1].y);
      const float mu = sum_g(m0 + m1) * 0.125f;
      const float M2 = sum_g(q0 + q1 + 80.f * ((m0 - mu) * (m0 - mu) + (m1 - mu) * (m1 - mu)));
      rstd[u] = __builtin_amdgcn_rsqf(M2 * (1.f / 640.f) + 1e-5f);
      mean[u] = mu * rstd[u];
    }
    unsigned hq[2][7][2];
#pragma unroll
    for (int n = 0; n < 7; ++n) {
      const f32x4 cs = *(LAS f32x4*)(lds + L_IMG + n * 64 + g * 16);
#pragma unroll
      for (int u = 0; u < 2; ++u) {
        const float v0 = fmaxf(a3[u][n][0] * rstd[u] - mean[u] * cs[0], 0.f), v1 = fmaxf(a3[u][n][1] * rstd[u] - mean[u] * cs[1], 0.f);
        const float v2 = fmaxf(a3[u][n][2] * rstd[u] - mean[u] * cs[2], 0.f), v3 = fmaxf(a3[u][n][3] * rstd[u] - mean[u] * cs[3], 0.f);
        hq[u][n][0] = pk2(v0, v1); hq[u][n][1] = pk2(v2, v3);
      }
    }
#pragma unroll
    for (int u = 0; u < 2; ++u) {
      const unsigned di = (8 * P + 2 * w + u) * 16 + m;
      st128(v4u{hq[u][0][0], hq[u][0][1], hq[u][1][0], hq[u][1][1]}, ws, l16, OFF_DF + di * 3072);
      st128(v4u{hq[u][2][0], hq[u][2][1], hq[u][3][0], hq[u][3][1]}, ws, l16 + 1024, OFF_DF + di * 3072);
      st128(v4u{hq[u][4][0], hq[u][4][1], hq[u][5][0], hq[u][5][1]}, ws, l16 + 2048, OFF_DF + di * 3072);
      if ((m & 7) < 6) st64(v2u{hq[u][6][0], hq[u][6][1]}, ws, lane * 8, OFF_DH + di * 512);
    }
  }
  STAMP(21);
  DRAIN();
  BAR();
  if (tid == 0) {
    __hip_atomic_store(flg + (4 * 256 + cuid) * 32, tag, RLX);
    if (cuid == 0) __hip_atomic_store((gu32*)(wsp + OFF_EPOCH), epoch + 1u, RLX);
  }
  STAMP(13);
  if (w == 0) wait_flag(flg + (4 * 256 + P * 16 + (lane & 15)) * 32, tag, lane < 16);
  BAR();
  STAMP(14);
  {
    f32x4 a4[4][5];
#pragma unroll
    for (int u = 0; u < 4; ++u)
#pragma unroll
      for (int n = 0; n < 5; ++n) a4[u][n] = f32x4{0.f, 0.f, 0.f, 0.f};
    const unsigned hb = OFF_DF + ((8 * P + 4 * jl) * 48 + 12 * w) * 1024, dhb = OFF_DH + (8 * P + 4 * jl) * 16 * 512;
    const int ci0 = w < 2 ? 2 * w : w + 2;
    auto lda = [&](int u, int k) -> v4u {
      if (k < 12) return ld128(ws, l16 + (k & 3) * 1024, hb + u * 48 * 1024 + (k >> 2) * 4096);
      const int ci = ci0 + (k - 12), h2 = ci >= 3 ? 1 : 0, tA = 8 * h2 + 2 * (ci - 3 * h2);
      const v2u a = ld64(ws, lane * 8, dhb + (u * 16 + tA) * 512), c = ld64(ws, lane * 8 + 512, dhb + (u * 16 + tA) * 512);
      return v4u{a.x, a.y, c.x, c.y};
    };
    constexpr int DP = 3;
    v4u af[DP + 1][4];
#pragma unroll
    for (int k = 0; k < DP; ++k)
#pragma unroll
      for (int u = 0; u < 4; ++u) { af[k][u] = lda(u, k); SCHED(); }
#pragma unroll
    for (int k = 0; k < 14; ++k) {
      if (k + DP < 14) {
        if (k + DP < 13 || w < 2) {
#pragma unroll
          for (int u = 0; u < 4; ++u) af[(k + DP) % (DP + 1)][u] = lda(u, k + DP);
        }
      }
      if (k < 13 || w < 2) {
#pragma unroll
        for (int u = 0; u < 4; ++u)
#pragma unroll
          for (int n = 0; n < 5; ++n) a4[u][n] = mfma(w2f[k][n], af[k % (DP + 1)][u], a4[u][n]);
      }
#pragma unroll
      for (int u = 0; u < 4; ++u) { SGB(0x20, 2); SGB(0x8, 5); }
      SCHED();
    }
    STAMP(15);
#pragma unroll
    for (int u = 0; u < 4; ++u) {
      if (u != w) {
        const int si = w < u ? w : w - 1;
#pragma unroll
        for (int n = 0; n < 5; ++n) *(LAS f32x4*)(lds + L_A + ((u * 3 + si) * 5 + n) * 1024 + l16) = a4[u][n];
      }
    }
    BAR();
    GAS char* op = (GAS char*)out + (size_t)(64 * (2 * P + jl) + 16 * w + t16) * 1280 + (80 * ct + 4 * g) * 2;
#pragma unroll
    for (int n = 0; n < 5; ++n) {
      f32x4 v = w == 0 ? a4[0][n] : (w == 1 ? a4[1][n] : (w == 2 ? a4[2][n] : a4[3][n]));
      f32x4 p[3];
#pragma unroll
      for (int si = 0; si < 3; ++si) p[si] = *(LAS f32x4*)(lds + L_A + ((w * 3 + si) * 5 + n) * 1024 + l16);
      v = (v + p[0]) + (p[1] + p[2]);
      const v2u o = {pk2(blo(hp[n][0]) + v[0], bhi(hp[n][0]) + v[1]), pk2(blo(hp[n][1]) + v[2], bhi(hp[n][1]) + v[3])};
      *(GAS v2u*)(op + n * 32) = o;
    }
  }
  STAMP(16);
}

__global__ void empty_k(unsigned* p) { if (p == nullptr) p[0] = 1; }

extern "C" void kernel_launch(void* const* d_in, const int* in_sizes, int n_in, void* d_out, int out_size, void* d_ws, size_t ws_size,
                              hipStream_t stream) {
  (void)in_sizes; (void)n_in; (void)out_size; (void)ws_size;
  hipFuncSetAttribute((const void*)fused_block, hipFuncAttributeMaxDynamicSharedMemorySize, LDS_BYTES);
  fused_block<<<256, 256, LDS_BYTES, stream>>>((const unsigned short*)d_in[0], (const unsigned short*)d_in[1], (const unsigned short*)d_in[2],
                                               (const unsigned short*)d_in[4], (const unsigned short*)d_in[5], (unsigned short*)d_out,
                                               (char*)d_ws);
  for (int i = 0; i < 1; ++i) empty_k<<<256, 256, 0, stream>>>((unsigned*)d_ws);
}
```

```cpp
#include <hip/hip_runtime.h>
#include <hip/hip_bf16.h>
#include <stdint.h>

typedef unsigned v4u __attribute__((ext_vector_type(4)));
typedef unsigned v3u __attribute__((ext_vector_type(3)));
typedef unsigned v2u __attribute__((ext_vector_type(2)));
typedef float f32x4 __attribute__((ext_vector_type(4)));
typedef float f32x2 __attribute__((ext_vector_type(2)));
typedef short bf16x8 __attribute__((ext_vector_type(8)));
typedef short s16x4 __attribute__((ext_vector_type(4)));
typedef __bf16 bf2 __attribute__((ext_vector_type(2)));
typedef __amdgpu_buffer_rsrc_t rsrc_t;
#define GAS __attribute__((address_space(1)))
#define LAS __attribute__((address_space(3)))
#define DI __device__ __forceinline__
#define RLX __ATOMIC_RELAXED, __HIP_MEMORY_SCOPE_AGENT
typedef GAS unsigned gu32;

#define TS 0
constexpr int CODE_LINES = 320;

constexpr unsigned OFF_EPOCH = 0;
constexpr unsigned OFF_FLAG = 4096;
constexpr unsigned OFF_FLAGB = OFF_FLAG + 5 * 256 * 128;
constexpr unsigned OFF_TS = 65 * 1024 * 1024;
constexpr unsigned OFF_TS2 = 64 * 1024 * 1024;
constexpr unsigned OFF_XN = 256 * 1024;
constexpr unsigned WQKV_STR = 153600;
constexpr unsigned OFF_WQKV = OFF_XN + 128 * 20 * 1024;
constexpr unsigned OFF_WO = OFF_WQKV + 16 * WQKV_STR;
constexpr unsigned W1_STR = 20 * 7 * 1024;
constexpr unsigned OFF_W1 = OFF_WO + 8 * 102400;
constexpr unsigned W2_STR = 54 * 5 * 1024;
constexpr unsigned OFF_W2 = OFF_W1 + 16 * W1_STR;
constexpr unsigned PART_STR = 3456;
constexpr unsigned OFF_PART = OFF_W2 + 8 * W2_STR;
constexpr unsigned OFF_OM = OFF_PART + 256 * PART_STR;
constexpr unsigned OFF_OR = OFF_OM + 128 * 16 * 1024;
constexpr unsigned OFF_HF = OFF_OR + 128 * 16 * 256;
constexpr unsigned OFF_HH = OFF_HF + 128 * 16 * 1024;
constexpr unsigned OFF_ST = OFF_HH + 128 * 8 * 512;
constexpr unsigned OFF_DF = OFF_ST + 128 * 8 * 128;
constexpr unsigned OFF_DH = OFF_DF + 128 * 48 * 1024;
constexpr unsigned WS_BYTES = OFF_DH + 128 * 16 * 512;

constexpr unsigned L_A = 0;
constexpr unsigned L_B = 102400;
constexpr unsigned L_M = 153600;
constexpr unsigned L_IMG = L_M;
constexpr unsigned L_CS = L_M + 4096;
constexpr unsigned L_PT = L_M + 6144;
constexpr unsigned LDS_BYTES = 163840;
constexpr unsigned EP = 272;

constexpr int AUX_SC1 = 16;
constexpr unsigned SPIN_CAP = 1u << 20;

DI f32x4 mfma(v4u a, v4u b, f32x4 c) {
  return __builtin_amdgcn_mfma_f32_16x16x32_bf16(__builtin_bit_cast(bf16x8, a), __builtin_bit_cast(bf16x8, b), c, 0, 0, 0);
}
DI unsigned pk2(float a, float b) { bf2 r = {(__bf16)a, (__bf16)b}; return __builtin_bit_cast(unsigned, r); }
DI float asf(unsigned u) { return __builtin_bit_cast(float, u); }
DI unsigned asu(float f) { return __builtin_bit_cast(unsigned, f); }
DI float blo(unsigned u) { return asf(u << 16); }
DI float bhi(unsigned u) { return asf(u & 0xffff0000u); }
DI float phi(float t) { return (t > 0.f ? t + 1.f : __expf(t)) + 1e-6f; }
template <int C> DI float dpp_add(float v) { return v + asf((unsigned)__builtin_amdgcn_update_dpp(0, (int)asu(v), C, 0xF, 0xF, true)); }
DI float sum16(float v) { v = dpp_add<0xB1>(v); v = dpp_add<0x4E>(v); v = dpp_add<0x141>(v); v = dpp_add<0x140>(v); return v; }
DI float sum_x16(float v) { const unsigned u = asu(v); auto r = __builtin_amdgcn_permlane16_swap(u, u, false, false); return asf(r[0]) + asf(r[1]); }
DI float sum_x32(float v) { const unsigned u = asu(v); auto r = __builtin_amdgcn_permlane32_swap(u, u, false, false); return asf(r[0]) + asf(r[1]); }
DI float sum_g(float v) { return sum_x32(sum_x16(v)); }
#define SCHED() __builtin_amdgcn_sched_barrier(0)
#define SGB(mask, n) __builtin_amdgcn_sched_group_barrier(mask, n, 0)
#define DRAIN() asm volatile("s_waitcnt vmcnt(0)" ::: "memory")
#define BAR() do { asm volatile("s_waitcnt lgkmcnt(0)" ::: "memory"); __builtin_amdgcn_s_barrier(); asm volatile("" ::: "memory"); } while (0)

DI v4u ld128(rsrc_t rs, unsigned voff, unsigned soff) { return __builtin_amdgcn_raw_buffer_load_b128(rs, voff, soff, AUX_SC1); }
DI void st128(v4u v, rsrc_t rs, unsigned voff, unsigned soff) { __builtin_amdgcn_raw_buffer_store_b128(v, rs, voff, soff, AUX_SC1); }
DI v2u ld64(rsrc_t rs, unsigned voff, unsigned soff) { return __builtin_amdgcn_raw_buffer_load_b64(rs, voff, soff, AUX_SC1); }
DI unsigned ld32(rsrc_t rs, unsigned voff, unsigned soff) { return __builtin_amdgcn_raw_buffer_load_b32(rs, voff, soff, AUX_SC1); }
DI void st64(v2u v, rsrc_t rs, unsigned voff, unsigned soff) { __builtin_amdgcn_raw_buffer_store_b64(v, rs, voff, soff, AUX_SC1); }

DI void dma4(rsrc_t rs, unsigned voff, unsigned soff, unsigned m0v) {
  asm volatile("s_nop 4\n\ts_mov_b32 m0, %0\n\ts_nop 0\n\t"
               "buffer_load_dwordx4 %1, %2, %3 offen sc1 lds\n\t"
               "buffer_load_dwordx4 %1, %2, %3 offen offset:1024 sc1 lds\n\t"
               "buffer_load_dwordx4 %1, %2, %3 offen offset:2048 sc1 lds\n\t"
               "buffer_load_dwordx4 %1, %2, %3 offen offset:3072 sc1 lds"
               :: "s"(m0v), "v"(voff), "s"(rs), "s"(soff) : "m0", "memory");
}
DI void dma1(rsrc_t rs, unsigned voff, unsigned soff, unsigned m0v) {
  asm volatile("s_nop 4\n\ts_mov_b32 m0, %0\n\ts_nop 0\n\t"
               "buffer_load_dwordx4 %1, %2, %3 offen sc1 lds"
               :: "s"(m0v), "v"(voff), "s"(rs), "s"(soff) : "m0", "memory");
}
DI void dma_item(rsrc_t rs, unsigned voff, unsigned m0v) {
  asm volatile("s_nop 4\n\ts_mov_b32 m0, %0\n\ts_nop 0\n\t"
               "buffer_load_dwordx4 %1, %2, 0 offen lds"
               :: "s"(m0v), "v"(voff), "s"(rs) : "m0", "memory");
}
DI v4u item_read(LAS char* img, int lane) {
  LAS char* p = img + ((lane >> 2) & 3) * 128 + (lane >> 4) * 32 + (lane & 3) * 8;
  const s16x4 a = __builtin_amdgcn_ds_read_tr16_b64_v4i16((LAS s16x4*)p);
  const s16x4 b = __builtin_amdgcn_ds_read_tr16_b64_v4i16((LAS s16x4*)(p + 512));
  const v2u x = __builtin_bit_cast(v2u, a), y = __builtin_bit_cast(v2u, b);
  return v4u{x.x, x.y, y.x, y.y};
}
#define LDF(sbase, i) ld128(ws, l16 + ((i) & 3) * 1024, (sbase) + ((i) >> 2) * 4096)
#define LDSF(off) (*(LAS v4u*)(((off) < 65536 ? lb0 : ((off) < 131072 ? lb1 : lb2)) + ((off) & 65535)))
#define W1A(ks, n) (((ks) < 7 ? L_B + (ks) * 7168 : L_A + ((ks) - 7) * 7168) + (n) * 1024)

DI void wait_flag(gu32* f, unsigned tag, bool active) {
  unsigned sp = 0;
  for (;;) {
    unsigned v = active ? __hip_atomic_load(f, RLX) : tag;
    if (__all(v == tag)) break;
    if (++sp > SPIN_CAP) break;
  }
  __builtin_amdgcn_fence(__ATOMIC_ACQUIRE, "wavefront");
}

#define PUBLISH(ph) do { DRAIN(); if (lane == 0) __hip_atomic_store(flg + ((ph) * 256 + cuid) * 32 + w, tag, RLX); } while (0)

#define STAMP(k) do { } while (0)

__global__ __launch_bounds__(256, 1) void fused_block(const unsigned short* __restrict__ x, const unsigned short* __restrict__ wqkv,
                                                      const unsigned short* __restrict__ wo, const unsigned short* __restrict__ w1,
                                                      const unsigned short* __restrict__ w2, unsigned short* __restrict__ out,
                                                      char* __restrict__ wsp) {
  extern __shared__ __attribute__((aligned(16))) char smem[];
  LAS char* lds = (LAS char*)smem;
  const unsigned ldsb = (unsigned)(size_t)lds;
  const int tid = threadIdx.x, lane = tid & 63;
  const int w = __builtin_amdgcn_readfirstlane(tid >> 6);
  const int g = lane >> 4, t16 = lane & 15;
  const int b = blockIdx.x, xcd = b & 7, sl = b >> 3;
  const int c2 = xcd & 1, P = 4 * (xcd >> 1) + (sl >> 3), m = 8 * c2 + (sl & 7);
  const int cuid = P * 16 + m;
  const int jl = (m & 7) >> 2, ct = 4 * c2 + (m & 3);
  const unsigned l16 = lane * 16;
  const rsrc_t ws = __builtin_amdgcn_make_buffer_rsrc(wsp, 0, WS_BYTES, 0x00020000);
  gu32* flg = (gu32*)(wsp + OFF_FLAG);
  STAMP(0);
  v2u xv[5];
  {
    const GAS char* xp = (const GAS char*)(x + (size_t)(128 * P + 8 * m + 2 * w + (lane >> 5)) * 640) + (lane & 31) * 8;
    asm volatile("global_load_dwordx2 %0, %5, off\n\tglobal_load_dwordx2 %1, %5, off offset:256\n\tglobal_load_dwordx2 %2, %5, off offset:512\n\t"
                 "global_load_dwordx2 %3, %5, off offset:768\n\tglobal_load_dwordx2 %4, %5, off offset:1024"
                 : "=&v"(xv[0]), "=&v"(xv[1]), "=&v"(xv[2]), "=&v"(xv[3]), "=&v"(xv[4]) : "v"(xp) : "memory");
  }
  unsigned warm0;
  {
    unsigned long long pc;
    asm volatile("s_getpc_b64 %0" : "=s"(pc));
    const int ln = min(sl * 11 + min(w * 3 + min(lane, 2), 10), CODE_LINES - 1);
    asm volatile("global_load_dword %0, %1, off" : "=v"(warm0) : "v"((const GAS char*)((pc & ~127ull) + (unsigned long long)ln * 128ull)) : "memory");
  }
  unsigned epoch;
  asm volatile("global_load_dword %0, %1, off sc1" : "=v"(epoch) : "v"((const GAS char*)wsp + OFF_EPOCH) : "memory");
  LAS char* lb0 = lds + l16; LAS char* lb1 = lb0 + 65536; LAS char* lb2 = lb0 + 131072;
  asm volatile("" : "+v"(lb1), "+v"(lb2));

  const int r8 = lane >> 3, ch8 = lane & 7;
#define P0B_INDEX() \
  int gv = cuid * 2 + (w & 1); asm volatile("" : "+s"(gv)); \
  const int rg1 = gv / 6, g6 = gv - 6 * rg1, cg1 = g6 < 3 ? 5 * g6 : 15 + 4 * (g6 - 3), n1 = gv < 480 ? (g6 < 3 ? 5 : 4) : 0; \
  const int rgA = gv < 432 ? gv >> 1 : gv - 432, cgA = gv < 432 ? 5 * (gv & 1) : 0, nAi = gv < 432 ? 5 : 10
  unsigned voA = 0, vo1 = 0;
  if (__builtin_expect(w < 2, 1)) {
    const rsrc_t rq = __builtin_amdgcn_make_buffer_rsrc((void*)wqkv, 0, 640 * 1920 * 2, 0x00020000);
    const int col = w == 0 ? (ch8 < 5 ? 640 + 40 * m + 8 * ch8 : 1280 + 40 * m + 8 * (ch8 - 5))
                           : (ch8 < 2 ? 1280 + 40 * m + 24 + 8 * ch8 : 40 * m + 8 * ((ch8 < 7 ? ch8 : 6) - 2));
    const unsigned vo = (unsigned)((40 * P + r8) * 1920 + col) * 2;
#pragma unroll
    for (int i = 0; i < 5; ++i) dma_item(rq, vo + i * 8 * 3840, ldsb + L_A + 16384 + (w * 5 + i) * 1024);
  } else {
    P0B_INDEX();
    (void)n1; (void)nAi;
    {
      const int sq = rgA >> 2, gg = rgA & 3;
      int row;
      if (gv < 432) {
        const int wq = sq < 28 ? sq / 14 : 2 + (sq - 28) / 13, k = sq - (wq < 2 ? 14 * wq : 28 + 13 * (wq - 2));
        int nA, nB;
        if (k < 12) {
          const int tt = 4 * (wq & 1) + k / 3, i = k % 3;
          nA = 54 * (wq >> 1) + (tt < 6 ? 7 * tt : 42 + 6 * (tt - 6)) + 2 * i; nB = nA + 1;
        } else {
          const int ci = (wq < 2 ? 2 * wq : wq + 2) + (k - 12), h2 = ci / 3, a3 = ci - 3 * h2;
          nA = 54 * h2 + 14 * a3 + 6; nB = nA + 7;
        }
        row = 16 * (r8 < 4 ? nA : nB) + 4 * gg + (r8 & 3);
      } else {
        row = sq < 16 ? 40 * sq + 5 * gg + (r8 < 5 ? r8 : 15 + r8) : 40 * (4 * (sq - 16) + (r8 >> 1)) + 23 + 5 * gg + (r8 & 1);
      }
      voA = (unsigned)(row * 640 + 64 * cgA + 8 * ch8) * 2;
    }
    {
      const int ks = rg1 >> 2, gg = rg1 & 3;
      const int nA = ks < 16 ? 5 * (ks >> 1) + 2 * (ks & 1) : 10 * (ks - 16) + 4, nB = ks < 16 ? nA + 1 : nA + 5;
      vo1 = (unsigned)((16 * (r8 < 4 ? nA : nB) + 4 * gg + (r8 & 3)) * 1728 + 64 * cg1 + 8 * ch8) * 2;
    }
  }
  STAMP(22);
  asm volatile("s_cmp_lt_u32 %7, 2\n\ts_cbranch_scc1 1f\n\ts_waitcnt vmcnt(0)\n\ts_branch 2f\n1:\n\ts_waitcnt vmcnt(5)\n2:"
               : "+v"(xv[0]), "+v"(xv[1]), "+v"(xv[2]), "+v"(xv[3]), "+v"(xv[4]), "+v"(epoch), "+v"(warm0) : "s"(w) : "memory", "scc");
  if (__builtin_expect(w >= 2, 0)) {
    P0B_INDEX();
    (void)rg1; (void)cg1; (void)rgA; (void)cgA;
    const unsigned sb = ldsb + L_B + (w & 1) * 15360;
    const rsrc_t rA = __builtin_amdgcn_make_buffer_rsrc((void*)(gv < 432 ? w2 : wo), 0, gv < 432 ? 1728 * 640 * 2 : 640 * 640 * 2, 0x00020000);
    for (int i = 0; i < nAi; ++i) dma_item(rA, voA + i * 128, sb + i * 1024);
    const rsrc_t r1 = __builtin_amdgcn_make_buffer_rsrc((void*)w1, 0, 640 * 1728 * 2, 0x00020000);
    for (int i = 0; i < n1; ++i) dma_item(r1, vo1 + i * 128, sb + (10 + i) * 1024);
  }
  STAMP(23);
  {
    float f[20];
#pragma unroll
    for (int i = 0; i < 5; ++i) { f[4 * i] = blo(xv[i].x); f[4 * i + 1] = bhi(xv[i].x); f[4 * i + 2] = blo(xv[i].y); f[4 * i + 3] = bhi(xv[i].y); }
    float s4[4];
#pragma unroll
    for (int j = 0; j < 4; ++j) s4[j] = (f[j] + f[4 + j]) + (f[8 + j] + f[12 + j]) + f[16 + j];
    const float mean = sum_x16(sum16((s4[0] + s4[1]) + (s4[2] + s4[3]))) * (1.f / 640.f);
#pragma unroll
    for (int i = 0; i < 20; ++i) f[i] -= mean;
#pragma unroll
    for (int j = 0; j < 4; ++j) s4[j] = (f[j] * f[j] + f[4 + j] * f[4 + j]) + (f[8 + j] * f[8 + j] + f[12 + j] * f[12 + j]) + f[16 + j] * f[16 + j];
    const float rstd = __builtin_amdgcn_rsqf(sum_x16(sum16((s4[0] + s4[1]) + (s4[2] + s4[3]))) * (1.f / 640.f) + 1e-5f);
    LAS char* sc = lds + L_A + ((lane & 31) >> 1) * 128 + (2 * w + (lane >> 5)) * 16 + (lane & 1) * 8;
#pragma unroll
    for (int i = 0; i < 5; ++i)
      *(LAS v2u*)(sc + i * 2048) = v2u{pk2(f[4 * i] * rstd, f[4 * i + 1] * rstd), pk2(f[4 * i + 2] * rstd, f[4 * i + 3] * rstd)};
  }
  STAMP(24);
  BAR();
  STAMP(25);
  unsigned tag = 0;
  if (__builtin_expect(w < 2, 1)) {
#pragma unroll
    for (int i = 0; i < 5; ++i) {
      const int L = 320 * w + 64 * i + lane, c = L >> 3, tt = 8 * (m & 1) + (L & 7);
      st128(*(LAS v4u*)(lds + L_A + L * 16), ws, OFF_XN + ((8 * P + (m >> 1)) * 20 + (c >> 2)) * 1024 + ((c & 3) * 16 + tt) * 16, 0);
    }
    asm volatile("s_waitcnt vmcnt(5)" ::: "memory");
    const int pos = 64 * w + lane, nb = pos >> 4, n = pos & 15;
#pragma unroll
    for (int i = 0; i < 5; ++i) {
      const v4u o = item_read(lds + L_A + 16384 + (w * 5 + i) * 1024, lane);
      const int rg = 5 * P + i, ks = rg >> 2, gg = rg & 3;
      if (pos < 120) st128(o, ws, OFF_WQKV + m * WQKV_STR + (nb < 7 ? (nb * 20 + ks) * 1024 + (gg * 16 + n) * 16 : 143360 + ks * 512 + (gg * 8 + n) * 16), 0);
    }
    STAMP(33);
    DRAIN();
  }
  tag = __builtin_amdgcn_readfirstlane(epoch) * 2654435761u + 0x9E3779B9u;
  STAMP(26);
  BAR();
  if (tid == 0) __hip_atomic_store(flg + (0 * 256 + cuid) * 32, tag, RLX);
  STAMP(1);
  if (w < 2) *(LAS v4u*)(lds + L_IMG + tid * 32) = v4u{0u, 0u, 0u, 0u}, *(LAS v4u*)(lds + L_IMG + tid * 32 + 16) = v4u{0u, 0u, 0u, 0u};
  else {
    P0B_INDEX();
    LAS char* im = lds + L_B + (w & 1) * 15360;
    DRAIN();
    const unsigned baseA = (gv < 432 ? OFF_W2 : OFF_WO) + ((rgA >> 2) * 5 * 64 + (rgA & 3) * 16) * 16, strA = gv < 432 ? W2_STR : 102400u;
    for (int i = 0; i < nAi; ++i) {
      const int n = 64 * (cgA + i) + lane, c = n / 80, nl = n - 80 * c;
      st128(item_read(im + i * 1024, lane), ws, baseA + c * strA + (nl >> 4) * 1024 + (nl & 15) * 16, 0);
    }
    const unsigned base1 = OFF_W1 + ((rg1 >> 2) * 7 * 64 + (rg1 & 3) * 16) * 16;
    for (int i = 0; i < n1; ++i) {
      const int n = 64 * (cg1 + i) + lane;
      const int nb = n >> 4, h2 = nb >= 54 ? 1 : 0, nbp = nb - 54 * h2;
      const int tt = nbp < 42 ? nbp / 7 : 6 + (nbp - 42) / 6, nbl = nbp - (tt < 6 ? 7 * tt : 42 + 6 * (tt - 6));
      st128(item_read(im + (10 + i) * 1024, lane), ws, base1 + (8 * h2 + tt) * W1_STR + nbl * 1024 + (n & 15) * 16, 0);
    }
  }
  STAMP(2);

  if (w == 0) {
    const int l = lane & 15;
    wait_flag(flg + (0 * 256 + (lane < 16 ? P * 16 + l : l * 16 + m)) * 32, tag, lane < 32);
  }
  BAR();
  STAMP(3);
  v4u fb;
  v4u xn[2][20];
  f32x4 akv[2][5];
  v4u wqs[13];
  {
    constexpr int D1 = 5;
    for (int q = w; q < 25; q += 4) dma4(ws, l16, OFF_WQKV + m * WQKV_STR + q * 4096, ldsb + L_A + q * 4096);
    const unsigned xb = OFF_XN + (8 * P + 2 * w) * 20 * 1024;
#pragma unroll
    for (int ks = 0; ks < D1; ++ks) {
#pragma unroll
      for (int u = 0; u < 2; ++u) xn[u][ks] = LDF(xb, u * 20 + ks);
      SCHED();
    }
    asm volatile("s_waitcnt vmcnt(10)" ::: "memory");
    BAR();
    if (tid == 0) __hip_atomic_store((gu32*)(wsp + OFF_FLAGB) + cuid, tag, RLX);
    STAMP(17);
#pragma unroll
    for (int u = 0; u < 2; ++u)
#pragma unroll
      for (int n = 0; n < 5; ++n) akv[u][n] = f32x4{0.f, 0.f, 0.f, 0.f};
    v4u wf[2][5];
#pragma unroll
    for (int n = 0; n < 5; ++n) wf[0][n] = LDSF(L_A + (n * 20) * 1024);
    SCHED();
    const unsigned wqb = OFF_WQKV + m * WQKV_STR + 102400 + w * 1024;
#pragma unroll
    for (int ks = 0; ks < 20; ++ks) {
      if (ks + D1 < 20) {
#pragma unroll
        for (int u = 0; u < 2; ++u) xn[u][ks + D1] = LDF(xb, u * 20 + ks + D1);
      }
      if (ks >= 4 && ks < 17) wqs[ks - 4] = ld128(ws, l16, wqb + (ks - 4 < 12 || w < 2 ? ks - 4 : 11) * 4096);
      if (ks == 17) fb = __builtin_amdgcn_raw_buffer_load_b128(ws, OFF_FLAGB + l16, 0, AUX_SC1);
      if (ks + 1 < 20) {
#pragma unroll
        for (int n = 0; n < 5; ++n) wf[(ks + 1) & 1][n] = LDSF(L_A + (n * 20 + ks + 1) * 1024);
      }
#pragma unroll
      for (int n = 0; n < 5; ++n)
#pragma unroll
        for (int u = 0; u < 2; ++u) akv[u][n] = mfma(xn[u][ks], wf[ks & 1][n], akv[u][n]);
#pragma unroll
      for (int n = 0; n < 5; ++n) { SGB(0x100, 1); SGB(0x8, 1); if (n < 3) SGB(0x20, 1); SGB(0x8, 1); }
      SCHED();
    }
  }
  STAMP(4);
  if (!__all(fb.x == tag && fb.y == tag && fb.z == tag && fb.w == tag)) {
    unsigned sp = 0;
    for (;;) {
      asm volatile("" ::: "memory");
      fb = __builtin_amdgcn_raw_buffer_load_b128(ws, OFF_FLAGB + l16, 0, AUX_SC1);
      if (__all(fb.x == tag && fb.y == tag && fb.z == tag && fb.w == tag)) break;
      if (++sp > SPIN_CAP) break;
    }
  }
  v4u w2f[14][5];
  const int k4s = w < 2 ? 14 * w : 28 + 13 * (w - 2);
  const unsigned w2b = OFF_W2 + ct * W2_STR + k4s * 5 * 1024;
#define W2LD(f0, f1) _Pragma("unroll") for (int f_ = (f0); f_ < (f1); ++f_) w2f[f_ / 5][f_ % 5] = LDF(w2b, f_); SCHED()
  W2LD(0, 12);
  BAR();
#pragma unroll
  for (int u = 0; u < 2; ++u)
#pragma unroll
    for (int n = 0; n < 5; ++n) {
      f32x4 a = akv[u][n];
      if (n < 2) { a[0] = phi(a[0]); a[1] = phi(a[1]); a[2] = phi(a[2]); a[3] = phi(a[3]); }
      else if (n == 2) { if (t16 < 8) { a[0] = phi(a[0]); a[1] = phi(a[1]); a[2] = phi(a[2]); a[3] = phi(a[3]); } }
      *(LAS v2u*)(lds + L_A + (16 * n + t16) * EP + (32 * w + 16 * u + 4 * g) * 2) = v2u{pk2(a[0], a[1]), pk2(a[2], a[3])};
    }
  if (tid < 16) *(LAS v4u*)(lds + L_A + 80 * EP + tid * 16) = v4u{0x3F803F80u, 0x3F803F80u, 0x3F803F80u, 0x3F803F80u};
  BAR();
  W2LD(12, 24);
  STAMP(27);
  {
    const int hd = w >> 1, dt = w & 1;
    f32x4 t0 = {0.f, 0.f, 0.f, 0.f}, t1 = t0;
    const unsigned ra = L_A + (20 * hd + 16 * dt + t16) * EP + g * 16;
    const unsigned rb0 = L_A + (40 + 20 * hd + t16) * EP + g * 16;
    const unsigned rb1 = L_A + (t16 < 4 ? 56 + 20 * hd + t16 : 80) * EP + g * 16;
    v4u fa[4], f0[4], f1[4];
#pragma unroll
    for (int s = 0; s < 4; ++s) { fa[s] = *(LAS v4u*)(lds + ra + s * 64); f0[s] = *(LAS v4u*)(lds + rb0 + s * 64); f1[s] = *(LAS v4u*)(lds + rb1 + s * 64); }
#pragma unroll
    for (int s = 0; s < 4; ++s) { t0 = mfma(fa[s], f0[s], t0); t1 = mfma(fa[s], f1[s], t1); }
    LAS char* pt = lds + L_PT + hd * 1680;
    if (dt == 0) {
      *(LAS f32x4*)(pt + l16) = t0;
      if (t16 < 5) *(LAS f32x4*)(pt + 1024 + (g * 5 + t16) * 16) = t1;
    } else if (g == 0) {
      *(LAS f32x4*)(pt + 1344 + t16 * 16) = t0;
      if (t16 < 5) *(LAS f32x4*)(pt + 1600 + t16 * 16) = t1;
    }
    if (tid >= 250) *(LAS v4u*)(lds + L_PT + 3360 + (tid - 250) * 16) = v4u{0u, 0u, 0u, 0u};
  }
  BAR();
  if (tid < 216) st128(*(LAS v4u*)(lds + L_PT + tid * 16), ws, tid * 16, OFF_PART + (m * 16 + P) * PART_STR);
#pragma unroll
  for (int i = 0; i < 13; ++i)
    if (i < 12 || w < 2) *(LAS v4u*)(lds + L_B + (w + 4 * i) * 1024 + l16) = wqs[i];
  STAMP(28);
  PUBLISH(1);
  BAR();
  STAMP(5);

  unsigned qp[2][3][2];
  {
    f32x4 aq[2][3];
#pragma unroll
    for (int u = 0; u < 2; ++u)
#pragma unroll
      for (int n = 0; n < 3; ++n) aq[u][n] = f32x4{0.f, 0.f, 0.f, 0.f};
    LAS char* lq7 = lds + L_B + 40960 + g * 128 + (t16 & 7) * 16;
    v4u wf[3][3];
#pragma unroll
    for (int k = 0; k < 2; ++k) { wf[k][0] = LDSF(L_B + k * 1024); wf[k][1] = LDSF(L_B + (20 + k) * 1024); wf[k][2] = *(LAS v4u*)(lq7 + k * 512); }
    SCHED();
#pragma unroll
    for (int ks = 0; ks < 20; ++ks) {
      if (ks % 3 == 0) {
        const int q = w + 4 * (ks / 3);
        if (q < 25) dma4(ws, l16, OFF_WO + ct * 102400 + q * 4096, ldsb + L_A + q * 4096);
      }
      if (ks + 2 < 20) {
        wf[(ks + 2) % 3][0] = LDSF(L_B + (ks + 2) * 1024);
        wf[(ks + 2) % 3][1] = LDSF(L_B + (20 + ks + 2) * 1024);
        wf[(ks + 2) % 3][2] = *(LAS v4u*)(lq7 + (ks + 2) * 512);
      }
#pragma unroll
      for (int n = 0; n < 3; ++n)
#pragma unroll
        for (int u = 0; u < 2; ++u) aq[u][n] = mfma(wf[ks % 3][n], xn[u][ks], aq[u][n]);
#pragma unroll
      for (int n = 0; n < 3; ++n) { SGB(0x100, 1); SGB(0x8, 2); }
      SCHED();
    }
#pragma unroll
    for (int u = 0; u < 2; ++u)
#pragma unroll
      for (int n = 0; n < 3; ++n) {
        qp[u][n][0] = pk2(phi(aq[u][n][0]), phi(aq[u][n][1]));
        qp[u][n][1] = pk2(phi(aq[u][n][2]), phi(aq[u][n][3]));
      }
  }
  BAR();
  for (int q = w; q < 12; q += 4) dma4(ws, l16, OFF_W1 + m * W1_STR + q * 4096, ldsb + L_B + q * 4096);
  if (w == 0) dma1(ws, l16, OFF_W1 + m * W1_STR + 49152, ldsb + L_B + 49152);
  unsigned imgoff, imgoffz;
  {
    const int hd = tid >= 105 ? 1 : 0, u = tid - 105 * hd;
    int dq, e;
    if (u < 64) { dq = u >> 4; e = u & 15; }
    else if (u < 84) { const int v = u - 64; dq = v / 5; e = 16 + v - 5 * dq; }
    else if (u < 100) { dq = 4; e = u - 84; }
    else { dq = 4; e = 16 + u - 100; }
    int gq;
    if (hd == 0) gq = dq < 4 ? dq : 0; else gq = dq < 3 ? dq + 1 : (dq == 3 ? 0 : 1);
    const int j0 = dq < 4 ? 0 : 4, ge = e / 5, re = e - 5 * ge;
    const unsigned ib = L_IMG + hd * 2048 + gq * 256 + j0 * 2;
    imgoff = ib + (re < 4 ? (4 * ge + re) * 16 : 1024 + (4 * ge) * 16);
    imgoffz = e == 20 ? ib + 1024 + 16 : 0u;
  }
  STAMP(6);

  wait_flag(flg + (1 * 256 + (lane >> 2) * 16 + m) * 32 + (lane & 3), tag, true);
  STAMP(7);
  if (tid < 210) {
    v4u pv[16];
#pragma unroll
    for (int i = 0; i < 16; ++i) pv[i] = ld128(ws, tid * 16, OFF_PART + (m * 16 + i) * PART_STR);
    f32x4 s[4];
#pragma unroll
    for (int i = 0; i < 4; ++i)
      s[i] = (__builtin_bit_cast(f32x4, pv[4 * i]) + __builtin_bit_cast(f32x4, pv[4 * i + 1])) +
             (__builtin_bit_cast(f32x4, pv[4 * i + 2]) + __builtin_bit_cast(f32x4, pv[4 * i + 3]));
    const f32x4 st = (s[0] + s[1]) + (s[2] + s[3]);
    const v2u val = v2u{pk2(st[0], st[1]), pk2(st[2], st[3])};
    if (imgoffz == 0u) *(LAS v2u*)(lds + imgoff) = val;
    else {
#pragma unroll
      for (int ge = 0; ge < 4; ++ge) *(LAS v2u*)(lds + imgoffz + ge * 64) = val;
    }
  }
  BAR();
  STAMP(18);
  {
    v4u ka[2][2];
#pragma unroll
    for (int hd = 0; hd < 2; ++hd)
#pragma unroll
      for (int bb = 0; bb < 2; ++bb) ka[hd][bb] = *(LAS v4u*)(lds + L_IMG + hd * 2048 + bb * 1024 + l16);
#pragma unroll
    for (int u = 0; u < 2; ++u) {
      v4u qa, qb;
      qa.x = qp[u][0][0]; qa.y = qp[u][0][1]; qa.z = g == 0 ? qp[u][1][0] : 0u; qa.w = g == 0 ? qp[u][1][1] : 0u;
      qb.x = g == 0 ? qp[u][2][0] : qp[u][1][0]; qb.y = g == 0 ? qp[u][2][1] : qp[u][1][1];
      qb.z = g == 1 ? qp[u][2][0] : 0u; qb.w = g == 1 ? qp[u][2][1] : 0u;
      const f32x4 z4 = {0.f, 0.f, 0.f, 0.f};
      const f32x4 a0 = mfma(ka[0][0], qa, z4), a1 = mfma(ka[0][1], qa, z4);
      const f32x4 b0 = mfma(ka[1][0], qb, z4), b1 = mfma(ka[1][1], qb, z4);
      const float ra = __builtin_amdgcn_rcpf(a1[1] + 1e-6f), rb = __builtin_amdgcn_rcpf(b1[1] + 1e-6f);
      const unsigned d0 = pk2(a0[0] * ra, a0[1] * ra), d1 = pk2(a0[2] * ra, a0[3] * ra), d2 = pk2(a1[0] * ra, b0[0] * rb),
                     d3 = pk2(b0[1] * rb, b0[2] * rb), d4 = pk2(b0[3] * rb, b1[0] * rb);
      const unsigned oi = (8 * P + 2 * w + u) * 16 + m;
      st128(v4u{d0, d1, d2, d3}, ws, l16, OFF_OM + oi * 1024);
      __builtin_amdgcn_raw_buffer_store_b32(d4, ws, lane * 4, OFF_OR + oi * 256, AUX_SC1);
    }
  }
  PUBLISH(2);
  STAMP(8);
  v2u xr[5];
  {
    const GAS char* xp = (const GAS char*)x + (size_t)(64 * (2 * P + jl) + 16 * w + t16) * 1280 + (80 * ct + 4 * g) * 2;
#pragma unroll
    for (int n = 0; n < 5; ++n) xr[n] = *(const GAS v2u*)(xp + n * 32);
  }
  v4u w1r[23];
  const unsigned w1rb = OFF_W1 + m * W1_STR + 50176 + w * 1024;
#pragma unroll
  for (int i = 0; i < 12; ++i) w1r[i] = ld128(ws, l16, w1rb + i * 4096);
  SCHED();

  f32x4 cs7[7];
  const v4u ones = {0x3F803F80u, 0x3F803F80u, 0x3F803F80u, 0x3F803F80u};
  {
    v4u wv[13];
#pragma unroll
    for (int i = 0; i < 13; ++i) wv[i] = *(LAS v4u*)(lb0 + L_B + (w + 4 * (i < 12 || w == 0 ? i : 11)) * 1024);
#pragma unroll
    for (int j = 0; j < 7; ++j) cs7[j] = mfma(wv[j], ones, f32x4{0.f, 0.f, 0.f, 0.f});
#pragma unroll
    for (int i = 7; i < 12; ++i) cs7[i - 7] = mfma(wv[i], ones, cs7[i - 7]);
    if (w == 0) cs7[5] = mfma(wv[12], ones, cs7[5]);
  }
  STAMP(29);
  wait_flag(flg + (2 * 256 + P * 16 + (lane & 15)) * 32 + ((4 * jl + w) >> 1), tag, lane < 16);
  STAMP(30);
  STAMP(9);
  unsigned hp[5][2];
  {
    constexpr int D2 = 8;
    v4u of[20];
    const unsigned ob = OFF_OM + (8 * P + 4 * jl + w) * 16 * 1024, orb = OFF_OR + (8 * P + 4 * jl + w) * 16 * 256;
#define LDO(ks) ((ks) < 16 ? LDF(ob, ks) : v4u{ld32(ws, lane * 4 + (4 * ((ks) - 16)) * 256, orb), ld32(ws, lane * 4 + (4 * ((ks) - 16) + 1) * 256, orb), \
                                              ld32(ws, lane * 4 + (4 * ((ks) - 16) + 2) * 256, orb), ld32(ws, lane * 4 + (4 * ((ks) - 16) + 3) * 256, orb)})
#pragma unroll
    for (int ks = 0; ks < D2; ++ks) { of[ks] = LDO(ks); SCHED(); }
    f32x4 ah[5];
#pragma unroll
    for (int n = 0; n < 5; ++n) ah[n] = f32x4{0.f, 0.f, 0.f, 0.f};
    v4u wf[3][5];
#pragma unroll
    for (int n = 0; n < 5; ++n) { wf[0][n] = LDSF(L_A + n * 1024); wf[1][n] = LDSF(L_A + (5 + n) * 1024); }
    SCHED();
#pragma unroll
    for (int ks = 0; ks < 20; ++ks) {
      if (ks + D2 < 20) of[ks + D2] = LDO(ks + D2);
      else {
        const int i0 = ks < 15 ? 12 + 2 * (ks - 12) : 15 + (ks - 12);
        w1r[i0] = ld128(ws, l16, w1rb + (i0 < 22 || w < 3 ? i0 : 21) * 4096);
        if (ks < 15) w1r[i0 + 1] = ld128(ws, l16, w1rb + (i0 + 1) * 4096);
      }
      if (ks + 2 < 20) {
#pragma unroll
        for (int n = 0; n < 5; ++n) wf[(ks + 2) % 3][n] = LDSF(L_A + ((ks + 2) * 5 + n) * 1024);
      }
#pragma unroll
      for (int n = 0; n < 5; ++n) ah[n] = mfma(wf[ks % 3][n], of[ks], ah[n]);
#pragma unroll
      for (int n = 0; n < 5; ++n) { SGB(0x100, 1); SGB(0x8, 1); if (n < 4) SGB(0x20, 1); }
      SCHED();
    }
    STAMP(19);
    float hv[20];
#pragma unroll
    for (int n = 0; n < 5; ++n) {
      hp[n][0] = pk2(blo(xr[n].x) + ah[n][0], bhi(xr[n].x) + ah[n][1]);
      hp[n][1] = pk2(blo(xr[n].y) + ah[n][2], bhi(xr[n].y) + ah[n][3]);
    }
    const unsigned hi = (8 * P + 4 * jl + w) * 8 + ct;
    st128(v4u{hp[0][0], hp[0][1], hp[1][0], hp[1][1]}, ws, l16, OFF_HF + hi * 2048);
    st128(v4u{hp[2][0], hp[2][1], hp[3][0], hp[3][1]}, ws, l16 + 1024, OFF_HF + hi * 2048);
    st64(v2u{hp[4][0], hp[4][1]}, ws, lane * 8, OFF_HH + hi * 512);
    float s4[5];
#pragma unroll
    for (int n = 0; n < 5; ++n) {
      hv[4 * n] = blo(hp[n][0]); hv[4 * n + 1] = bhi(hp[n][0]); hv[4 * n + 2] = blo(hp[n][1]); hv[4 * n + 3] = bhi(hp[n][1]);
      s4[n] = (hv[4 * n] + hv[4 * n + 1]) + (hv[4 * n + 2] + hv[4 * n + 3]);
    }
    const float mi = sum_g((s4[0] + s4[1]) + (s4[2] + s4[3]) + s4[4]) * (1.f / 80.f);
#pragma unroll
    for (int n = 0; n < 5; ++n) {
      const float a = hv[4 * n] - mi, bq = hv[4 * n + 1] - mi, c = hv[4 * n + 2] - mi, d = hv[4 * n + 3] - mi;
      s4[n] = (a * a + bq * bq) + (c * c + d * d);
    }
    const float q = sum_g((s4[0] + s4[1]) + (s4[2] + s4[3]) + s4[4]);
    if (g == 0) st64(v2u{asu(mi), asu(q)}, ws, t16 * 8, OFF_ST + ((8 * P + 4 * jl + w) * 8 + ct) * 128);
  }
  PUBLISH(3);
  BAR();
  STAMP(10);
#pragma unroll
  for (int i = 0; i < 23; ++i)
    if (i < 22 || w < 3) {
      *(LAS v4u*)(lds + L_A + (w + 4 * i) * 1024 + l16) = w1r[i];
      cs7[i % 7] = mfma(w1r[i], ones, cs7[i % 7]);
    }
  if (t16 == 0) {
#pragma unroll
    for (int j = 0; j < 7; ++j) *(LAS f32x4*)(lds + L_CS + (w * 7 + (w + 4 * j) % 7) * 64 + g * 16) = cs7[j];
  }
  W2LD(24, 36);
  STAMP(20);

  BAR();
  {
    const int i = (lane & 15) >> 1;
    wait_flag(flg + (3 * 256 + P * 16 + 8 * (i >> 2) + 4 * (w >> 1) + (i & 3)) * 32 + ((2 * w + (lane & 1)) & 3), tag, lane < 16);
  }
  STAMP(11);
  {
    constexpr int D3 = 5;
    v4u hf[2][20];
    const unsigned hb = OFF_HF + (8 * P + 2 * w) * 16 * 1024, hhb = OFF_HH + (8 * P + 2 * w) * 8 * 512;
    auto ldh = [&](int u, int ks) -> v4u {
      if (ks < 16) return LDF(hb, u * 16 + ks);
      const v2u a = ld64(ws, lane * 8 + (2 * (ks - 16)) * 512, hhb + u * 4096), c = ld64(ws, lane * 8 + (2 * (ks - 16) + 1) * 512, hhb + u * 4096);
      return v4u{a.x, a.y, c.x, c.y};
    };
    v2u stv[2][2];
#pragma unroll
    for (int ks = 0; ks < D3; ++ks) {
#pragma unroll
      for (int u = 0; u < 2; ++u) hf[u][ks] = ldh(u, ks);
      SCHED();
    }
#pragma unroll
    for (int u = 0; u < 2; ++u)
#pragma unroll
      for (int i = 0; i < 2; ++i) stv[u][i] = ld64(ws, t16 * 8 + (2 * g + i) * 128, OFF_ST + (8 * P + 2 * w + u) * 1024);
    f32x4 cs[7];
#pragma unroll
    for (int n = 0; n < 7; ++n) {
      f32x4 cw[4];
#pragma unroll
      for (int ww = 0; ww < 4; ++ww) cw[ww] = *(LAS f32x4*)(lds + L_CS + (ww * 7 + n) * 64 + g * 16);
      cs[n] = (cw[0] + cw[1]) + (cw[2] + cw[3]);
    }
    f32x4 a3[2][7];
#pragma unroll
    for (int u = 0; u < 2; ++u)
#pragma unroll
      for (int n = 0; n < 7; ++n) a3[u][n] = f32x4{0.f, 0.f, 0.f, 0.f};
    v4u wf[2][7];
#pragma unroll
    for (int n = 0; n < 7; ++n) wf[0][n] = LDSF(W1A(0, n));
    SCHED();
#pragma unroll
    for (int ks = 0; ks < 20; ++ks) {
      if (ks + D3 < 20) {
#pragma unroll
        for (int u = 0; u < 2; ++u) hf[u][ks + D3] = ldh(u, ks + D3);
      }
      w2f[(36 + ks) / 5][(36 + ks) % 5] = LDF(w2b, 36 + ks);
      if (ks + 1 < 20) {
#pragma unroll
        for (int n = 0; n < 7; ++n) wf[(ks + 1) & 1][n] = LDSF(W1A(ks + 1, n));
      }
#pragma unroll
      for (int n = 0; n < 7; ++n)
#pragma unroll
        for (int u = 0; u < 2; ++u) a3[u][n] = mfma(wf[ks & 1][n], hf[u][ks], a3[u][n]);
#pragma unroll
      for (int n = 0; n < 7; ++n) { SGB(0x100, 1); SGB(0x8, 1); if (n < 5) SGB(0x20, 1); SGB(0x8, 1); }
      SCHED();
    }
    STAMP(12);
    float mean[2], rstd[2];
#pragma unroll
    for (int u = 0; u < 2; ++u) {
      const float m0 = asf(stv[u][0].x), q0 = asf(stv[u][0].y), m1 = asf(stv[u][1].x), q1 = asf(stv[u][1].y);
      const float mu = sum_g(m0 + m1) * 0.125f;
      const float M2 = sum_g(q0 + q1 + 80.f * ((m0 - mu) * (m0 - mu) + (m1 - mu) * (m1 - mu)));
      rstd[u] = __builtin_amdgcn_rsqf(M2 * (1.f / 640.f) + 1e-5f);
      mean[u] = mu * rstd[u];
    }
    unsigned hq[2][7][2];
#pragma unroll
    for (int n = 0; n < 7; ++n) {
#pragma unroll
      for (int u = 0; u < 2; ++u) {
        const f32x2 r2 = {rstd[u], rstd[u]}, m2 = {-mean[u], -mean[u]};
        const f32x2 lo = __builtin_elementwise_fma(f32x2{a3[u][n][0], a3[u][n][1]}, r2, f32x2{cs[n][0], cs[n][1]} * m2);
        const f32x2 hi2 = __builtin_elementwise_fma(f32x2{a3[u][n][2], a3[u][n][3]}, r2, f32x2{cs[n][2], cs[n][3]} * m2);
        const float v0 = fmaxf(lo[0], 0.f), v1 = fmaxf(lo[1], 0.f), v2 = fmaxf(hi2[0], 0.f), v3 = fmaxf(hi2[1], 0.f);
        hq[u][n][0] = pk2(v0, v1); hq[u][n][1] = pk2(v2, v3);
      }
    }
#pragma unroll
    for (int u = 0; u < 2; ++u) {
      const unsigned di = (8 * P + 2 * w + u) * 16 + m;
      st128(v4u{hq[u][0][0], hq[u][0][1], hq[u][1][0], hq[u][1][1]}, ws, l16, OFF_DF + di * 3072);
      st128(v4u{hq[u][2][0], hq[u][2][1], hq[u][3][0], hq[u][3][1]}, ws, l16 + 1024, OFF_DF + di * 3072);
      st128(v4u{hq[u][4][0], hq[u][4][1], hq[u][5][0], hq[u][5][1]}, ws, l16 + 2048, OFF_DF + di * 3072);
      if ((m & 7) < 6) st64(v2u{hq[u][6][0], hq[u][6][1]}, ws, lane * 8, OFF_DH + di * 512);
    }
  }
  STAMP(21);
  PUBLISH(4);
  if (tid == 0 && cuid == 0) __hip_atomic_store((gu32*)(wsp + OFF_EPOCH), epoch + 1u, RLX);
  STAMP(13);
  W2LD(56, 70);
  {
    const int l = lane & 15, j = (l - 8) >> 2, ci = (w < 2 ? 2 * w : w + 2) + (j > 0 && w < 2 ? 1 : 0);
    const int tile = l < 8 ? 4 * w + (l >> 1) : 8 * (ci >= 3 ? 1 : 0) + 2 * (ci >= 3 ? ci - 3 : ci) + ((l >> 1) & 1);
    wait_flag(flg + (4 * 256 + P * 16 + tile) * 32 + 2 * jl + (l & 1), tag, lane < 16);
  }
  STAMP(14);
  {
    f32x4 a4[4][5];
#pragma unroll
    for (int u = 0; u < 4; ++u)
#pragma unroll
      for (int n = 0; n < 5; ++n) a4[u][n] = f32x4{0.f, 0.f, 0.f, 0.f};
    const unsigned hb = OFF_DF + ((8 * P + 4 * jl) * 48 + 12 * w) * 1024, dhb = OFF_DH + (8 * P + 4 * jl) * 16 * 512;
    const int ci0 = w < 2 ? 2 * w : w + 2;
    auto lda = [&](int u, int k) -> v4u {
      if (k < 12) return ld128(ws, l16 + (k & 3) * 1024, hb + u * 48 * 1024 + (k >> 2) * 4096);
      const int ci = ci0 + (k - 12), h2 = ci >= 3 ? 1 : 0, tA = 8 * h2 + 2 * (ci - 3 * h2);
      const v2u a = ld64(ws, lane * 8, dhb + (u * 16 + tA) * 512), c = ld64(ws, lane * 8 + 512, dhb + (u * 16 + tA) * 512);
      return v4u{a.x, a.y, c.x, c.y};
    };
    constexpr int DP = 3;
    v4u af[DP + 1][4];
#pragma unroll
    for (int k = 0; k < DP; ++k)
#pragma unroll
      for (int u = 0; u < 4; ++u) { af[k][u] = lda(u, k); SCHED(); }
#pragma unroll
    for (int k = 0; k < 14; ++k) {
      if (k + DP < 14) {
        if (k + DP < 13 || w < 2) {
#pragma unroll
          for (int u = 0; u < 4; ++u) af[(k + DP) % (DP + 1)][u] = lda(u, k + DP);
        }
      }
      if (k < 13 || w < 2) {
#pragma unroll
        for (int u = 0; u < 4; ++u)
#pragma unroll
          for (int n = 0; n < 5; ++n) a4[u][n] = mfma(w2f[k][n], af[k % (DP + 1)][u], a4[u][n]);
      }
#pragma unroll
      for (int u = 0; u < 4; ++u) { SGB(0x20, 2); SGB(0x8, 5); }
      SCHED();
    }
    STAMP(15);
    BAR();
#pragma unroll
    for (int u = 0; u < 4; ++u) {
      if (u != w) {
        const int si = w < u ? w : w - 1;
#pragma unroll
        for (int n = 0; n < 5; ++n) *(LAS f32x4*)(lds + L_A + ((u * 3 + si) * 5 + n) * 1024 + l16) = a4[u][n];
      }
    }
    BAR();
    GAS char* op = (GAS char*)out + (size_t)(64 * (2 * P + jl) + 16 * w + t16) * 1280 + (80 * ct + 4 * g) * 2;
#pragma unroll
    for (int n = 0; n < 5; ++n) {
      f32x4 v = w == 0 ? a4[0][n] : (w == 1 ? a4[1][n] : (w == 2 ? a4[2][n] : a4[3][n]));
      f32x4 p[3];
#pragma unroll
      for (int si = 0; si < 3; ++si) p[si] = *(LAS f32x4*)(lds + L_A + ((w * 3 + si) * 5 + n) * 1024 + l16);
      v = (v + p[0]) + (p[1] + p[2]);
      const v2u o = {pk2(blo(hp[n][0]) + v[0], bhi(hp[n][0]) + v[1]), pk2(blo(hp[n][1]) + v[2], bhi(hp[n][1]) + v[3])};
      *(GAS v2u*)(op + n * 32) = o;
    }
  }
  STAMP(16);
}

extern "C" void kernel_launch(void* const* d_in, const int* in_sizes, int n_in, void* d_out, int out_size, void* d_ws, size_t ws_size,
                              hipStream_t stream) {
  (void)in_sizes; (void)n_in; (void)out_size; (void)ws_size;
  hipFuncSetAttribute((const void*)fused_block, hipFuncAttributeMaxDynamicSharedMemorySize, LDS_BYTES);
  fused_block<<<256, 256, LDS_BYTES, stream>>>((const unsigned short*)d_in[0], (const unsigned short*)d_in[1], (const unsigned short*)d_in[2],
                                               (const unsigned short*)d_in[4], (const unsigned short*)d_in[5], (unsigned short*)d_out,
                                               (char*)d_ws);
}
```

```cpp
#include <hip/hip_runtime.h>
#include <hip/hip_bf16.h>
#include <stdint.h>

typedef unsigned v4u __attribute__((ext_vector_type(4)));
typedef unsigned v3u __attribute__((ext_vector_type(3)));
typedef unsigned v2u __attribute__((ext_vector_type(2)));
typedef float f32x4 __attribute__((ext_vector_type(4)));
typedef float f32x2 __attribute__((ext_vector_type(2)));
typedef short bf16x8 __attribute__((ext_vector_type(8)));
typedef short s16x4 __attribute__((ext_vector_type(4)));
typedef __bf16 bf2 __attribute__((ext_vector_type(2)));
typedef __amdgpu_buffer_rsrc_t rsrc_t;
#define GAS __attribute__((address_space(1)))
#define LAS __attribute__((address_space(3)))
#define DI __device__ __forceinline__
#define RLX __ATOMIC_RELAXED, __HIP_MEMORY_SCOPE_AGENT
typedef GAS unsigned gu32;

#define TS 0
constexpr int CODE_LINES = 285;

constexpr unsigned OFF_EPOCH = 0;
constexpr unsigned OFF_FLAG = 4096;
constexpr unsigned OFF_FLAGB = OFF_FLAG + 5 * 256 * 128;
constexpr unsigned OFF_TS = 65 * 1024 * 1024;
constexpr unsigned OFF_TS2 = 64 * 1024 * 1024;
constexpr unsigned OFF_XN = 256 * 1024;
constexpr unsigned WQKV_STR = 153600;
constexpr unsigned OFF_WQKV = OFF_XN + 128 * 20 * 1024;
constexpr unsigned OFF_WO = OFF_WQKV + 16 * WQKV_STR;
constexpr unsigned W1_STR = 20 * 7 * 1024;
constexpr unsigned OFF_W1 = OFF_WO + 8 * 102400;
constexpr unsigned W2_STR = 54 * 5 * 1024;
constexpr unsigned OFF_W2 = OFF_W1 + 16 * W1_STR;
constexpr unsigned PART_STR = 3456;
constexpr unsigned OFF_PART = OFF_W2 + 8 * W2_STR;
constexpr unsigned OFF_OM = OFF_PART + 256 * PART_STR;
constexpr unsigned OFF_OR = OFF_OM + 128 * 16 * 1024;
constexpr unsigned OFF_HF = OFF_OR + 128 * 16 * 256;
constexpr unsigned OFF_HH = OFF_HF + 128 * 16 * 1024;
constexpr unsigned OFF_ST = OFF_HH + 128 * 8 * 512;
constexpr unsigned OFF_DF = OFF_ST + 128 * 8 * 128;
constexpr unsigned OFF_DH = OFF_DF + 128 * 48 * 1024;
constexpr unsigned WS_BYTES = OFF_DH + 128 * 16 * 512;

constexpr unsigned L_A = 0;
constexpr unsigned L_B = 102400;
constexpr unsigned L_M = 153600;
constexpr unsigned L_IMG = L_M;
constexpr unsigned L_CS = L_M + 4096;
constexpr unsigned L_PT = L_M + 6144;
constexpr unsigned LDS_BYTES = 163840;
constexpr unsigned EP = 272;

constexpr int AUX_SC1 = 16;
constexpr unsigned SPIN_CAP = 1u << 20;

DI f32x4 mfma(v4u a, v4u b, f32x4 c) {
  return __builtin_amdgcn_mfma_f32_16x16x32_bf16(__builtin_bit_cast(bf16x8, a), __builtin_bit_cast(bf16x8, b), c, 0, 0, 0);
}
DI unsigned pk2(float a, float b) { bf2 r = {(__bf16)a, (__bf16)b}; return __builtin_bit_cast(unsigned, r); }
DI float asf(unsigned u) { return __builtin_bit_cast(float, u); }
DI unsigned asu(float f) { return __builtin_bit_cast(unsigned, f); }
DI float blo(unsigned u) { return asf(u << 16); }
DI float bhi(unsigned u) { return asf(u & 0xffff0000u); }
DI float phi(float t) { return (t > 0.f ? t + 1.f : __expf(t)) + 1e-6f; }
template <int C> DI float dpp_add(float v) { return v + asf((unsigned)__builtin_amdgcn_update_dpp(0, (int)asu(v), C, 0xF, 0xF, true)); }
DI float sum16(float v) { v = dpp_add<0xB1>(v); v = dpp_add<0x4E>(v); v = dpp_add<0x141>(v); v = dpp_add<0x140>(v); return v; }
DI float sum_x16(float v) { const unsigned u = asu(v); auto r = __builtin_amdgcn_permlane16_swap(u, u, false, false); return asf(r[0]) + asf(r[1]); }
DI float sum_x32(float v) { const unsigned u = asu(v); auto r = __builtin_amdgcn_permlane32_swap(u, u, false, false); return asf(r[0]) + asf(r[1]); }
DI float sum_g(float v) { return sum_x32(sum_x16(v)); }
#define SCHED() __builtin_amdgcn_sched_barrier(0)
#define SGB(mask, n) __builtin_amdgcn_sched_group_barrier(mask, n, 0)
#define DRAIN() asm volatile("s_waitcnt vmcnt(0)" ::: "memory")
#define BAR() do { asm volatile("s_waitcnt lgkmcnt(0)" ::: "memory"); __builtin_amdgcn_s_barrier(); asm volatile("" ::: "memory"); } while (0)

DI v4u ld128(rsrc_t rs, unsigned voff, unsigned soff) { return __builtin_amdgcn_raw_buffer_load_b128(rs, voff, soff, AUX_SC1); }
DI void st128(v4u v, rsrc_t rs, unsigned voff, unsigned soff) { __builtin_amdgcn_raw_buffer_store_b128(v, rs, voff, soff, AUX_SC1); }
DI v2u ld64(rsrc_t rs, unsigned voff, unsigned soff) { return __builtin_amdgcn_raw_buffer_load_b64(rs, voff, soff, AUX_SC1); }
DI unsigned ld32(rsrc_t rs, unsigned voff, unsigned soff) { return __builtin_amdgcn_raw_buffer_load_b32(rs, voff, soff, AUX_SC1); }
DI void st64(v2u v, rsrc_t rs, unsigned voff, unsigned soff) { __builtin_amdgcn_raw_buffer_store_b64(v, rs, voff, soff, AUX_SC1); }

DI void dma4(rsrc_t rs, unsigned voff, unsigned soff, unsigned m0v) {
  asm volatile("s_nop 4\n\ts_mov_b32 m0, %0\n\ts_nop 0\n\t"
               "buffer_load_dwordx4 %1, %2, %3 offen sc1 lds\n\t"
               "buffer_load_dwordx4 %1, %2, %3 offen offset:1024 sc1 lds\n\t"
               "buffer_load_dwordx4 %1, %2, %3 offen offset:2048 sc1 lds\n\t"
               "buffer_load_dwordx4 %1, %2, %3 offen offset:3072 sc1 lds"
               :: "s"(m0v), "v"(voff), "s"(rs), "s"(soff) : "m0", "memory");
}
DI void dma1(rsrc_t rs, unsigned voff, unsigned soff, unsigned m0v) {
  asm volatile("s_nop 4\n\ts_mov_b32 m0, %0\n\ts_nop 0\n\t"
               "buffer_load_dwordx4 %1, %2, %3 offen sc1 lds"
               :: "s"(m0v), "v"(voff), "s"(rs), "s"(soff) : "m0", "memory");
}
DI void dma1q(rsrc_t rs, unsigned voff, unsigned soff, unsigned m0v) {
  asm volatile("s_mov_b32 m0, %0\n\ts_nop 0\n\tbuffer_load_dwordx4 %1, %2, %3 offen sc1 lds"
               :: "s"(m0v), "v"(voff), "s"(rs), "s"(soff) : "m0", "memory");
}
DI void dma_item(rsrc_t rs, unsigned voff, unsigned m0v) {
  asm volatile("s_nop 4\n\ts_mov_b32 m0, %0\n\ts_nop 0\n\t"
               "buffer_load_dwordx4 %1, %2, 0 offen lds"
               :: "s"(m0v), "v"(voff), "s"(rs) : "m0", "memory");
}
DI v4u item_read(LAS char* img, int lane) {
  LAS char* p = img + ((lane >> 2) & 3) * 128 + (lane >> 4) * 32 + (lane & 3) * 8;
  const s16x4 a = __builtin_amdgcn_ds_read_tr16_b64_v4i16((LAS s16x4*)p);
  const s16x4 b = __builtin_amdgcn_ds_read_tr16_b64_v4i16((LAS s16x4*)(p + 512));
  const v2u x = __builtin_bit_cast(v2u, a), y = __builtin_bit_cast(v2u, b);
  return v4u{x.x, x.y, y.x, y.y};
}
#define LDF(sbase, i) ld128(ws, l16 + ((i) & 3) * 1024, (sbase) + ((i) >> 2) * 4096)
#define LDSF(off) (*(LAS v4u*)(((off) < 65536 ? lb0 : ((off) < 131072 ? lb1 : lb2)) + ((off) & 65535)))
#define W1A(ks, n) (((ks) < 7 ? L_B + (ks) * 7168 : L_A + ((ks) - 7) * 7168) + (n) * 1024)

DI void wait_flag(gu32* f, unsigned tag, bool active) {
  unsigned sp = 0;
  for (;;) {
    unsigned v = active ? __hip_atomic_load(f, RLX) : tag;
    if (__all(v == tag)) break;
    if (++sp > SPIN_CAP) break;
  }
  __builtin_amdgcn_fence(__ATOMIC_ACQUIRE, "wavefront");
}

#define PUBLISH(ph) do { DRAIN(); if (lane == 0) __hip_atomic_store(flg + ((ph) * 256 + cuid) * 32 + w, tag, RLX); } while (0)

#define STAMP(k) do { } while (0)

__global__ __launch_bounds__(256, 1) void fused_block(const unsigned short* __restrict__ x, const unsigned short* __restrict__ wqkv,
                                                      const unsigned short* __restrict__ wo, const unsigned short* __restrict__ w1,
                                                      const unsigned short* __restrict__ w2, unsigned short* __restrict__ out,
                                                      char* __restrict__ wsp) {
  extern __shared__ __attribute__((aligned(16))) char smem[];
  LAS char* lds = (LAS char*)smem;
  const unsigned ldsb = (unsigned)(size_t)lds;
  const int tid = threadIdx.x, lane = tid & 63;
  const int w = __builtin_amdgcn_readfirstlane(tid >> 6);
  const int g = lane >> 4, t16 = lane & 15;
  const int b = blockIdx.x, xcd = b & 7, sl = b >> 3;
  const int c2 = xcd & 1, P = 4 * (xcd >> 1) + (sl >> 3), m = 8 * c2 + (sl & 7);
  const int cuid = P * 16 + m;
  const int jl = (m & 7) >> 2, ct = 4 * c2 + (m & 3);
  const unsigned l16 = lane * 16;
  const rsrc_t ws = __builtin_amdgcn_make_buffer_rsrc(wsp, 0, WS_BYTES, 0x00020000);
  gu32* flg = (gu32*)(wsp + OFF_FLAG);
  STAMP(0);
  v2u xv[5];
  {
    const GAS char* xp = (const GAS char*)(x + (size_t)(128 * P + 8 * m + 2 * w + (lane >> 5)) * 640) + (lane & 31) * 8;
    asm volatile("global_load_dwordx2 %0, %5, off\n\tglobal_load_dwordx2 %1, %5, off offset:256\n\tglobal_load_dwordx2 %2, %5, off offset:512\n\t"
                 "global_load_dwordx2 %3, %5, off offset:768\n\tglobal_load_dwordx2 %4, %5, off offset:1024"
                 : "=&v"(xv[0]), "=&v"(xv[1]), "=&v"(xv[2]), "=&v"(xv[3]), "=&v"(xv[4]) : "v"(xp) : "memory");
  }
  unsigned warm0;
  {
    unsigned long long pc;
    asm volatile("s_getpc_b64 %0" : "=s"(pc));
    const int ln = min(sl * 11 + min(w * 3 + min(lane, 2), 10), CODE_LINES - 1);
    asm volatile("global_load_dword %0, %1, off" : "=v"(warm0) : "v"((const GAS char*)((pc & ~127ull) + (unsigned long long)ln * 128ull)) : "memory");
  }
  unsigned epoch;
  asm volatile("global_load_dword %0, %1, off sc1" : "=v"(epoch) : "v"((const GAS char*)wsp + OFF_EPOCH) : "memory");
  LAS char* lb0 = lds + l16; LAS char* lb1 = lb0 + 65536; LAS char* lb2 = lb0 + 131072;
  asm volatile("" : "+v"(lb1), "+v"(lb2));

  const int r8 = lane >> 3, ch8 = lane & 7;
#define P0B_INDEX() \
  int gv = cuid * 2 + (w & 1); asm volatile("" : "+s"(gv)); \
  const int rg1 = gv / 6, g6 = gv - 6 * rg1, cg1 = g6 < 3 ? 5 * g6 : 15 + 4 * (g6 - 3), n1 = gv < 480 ? (g6 < 3 ? 5 : 4) : 0; \
  const int rgA = gv < 432 ? gv >> 1 : gv - 432, cgA = gv < 432 ? 5 * (gv & 1) : 0, nAi = gv < 432 ? 5 : 10
  unsigned voA = 0, vo1 = 0;
  if (__builtin_expect(w < 2, 1)) {
    const rsrc_t rq = __builtin_amdgcn_make_buffer_rsrc((void*)wqkv, 0, 640 * 1920 * 2, 0x00020000);
    const int col = w == 0 ? (ch8 < 5 ? 640 + 40 * m + 8 * ch8 : 1280 + 40 * m + 8 * (ch8 - 5))
                           : (ch8 < 2 ? 1280 + 40 * m + 24 + 8 * ch8 : 40 * m + 8 * ((ch8 < 7 ? ch8 : 6) - 2));
    const unsigned vo = (unsigned)((40 * P + r8) * 1920 + col) * 2;
#pragma unroll
    for (int i = 0; i < 5; ++i) dma_item(rq, vo + i * 8 * 3840, ldsb + L_A + 16384 + (w * 5 + i) * 1024);
  } else {
    P0B_INDEX();
    (void)n1; (void)nAi;
    {
      const int sq = rgA >> 2, gg = rgA & 3;
      int row;
      if (gv < 432) {
        const int wq = sq < 28 ? sq / 14 : 2 + (sq - 28) / 13, k = sq - (wq < 2 ? 14 * wq : 28 + 13 * (wq - 2));
        int nA, nB;
        if (k < 12) {
          const int tt = 4 * (wq & 1) + k / 3, i = k % 3;
          nA = 54 * (wq >> 1) + (tt < 6 ? 7 * tt : 42 + 6 * (tt - 6)) + 2 * i; nB = nA + 1;
        } else {
          const int ci = (wq < 2 ? 2 * wq : wq + 2) + (k - 12), h2 = ci / 3, a3 = ci - 3 * h2;
          nA = 54 * h2 + 14 * a3 + 6; nB = nA + 7;
        }
        row = 16 * (r8 < 4 ? nA : nB) + 4 * gg + (r8 & 3);
      } else {
        row = sq < 16 ? 40 * sq + 5 * gg + (r8 < 5 ? r8 : 15 + r8) : 40 * (4 * (sq - 16) + (r8 >> 1)) + 23 + 5 * gg + (r8 & 1);
      }
      voA = (unsigned)(row * 640 + 64 * cgA + 8 * ch8) * 2;
    }
    {
      const int ks = rg1 >> 2, gg = rg1 & 3;
      const int nA = ks < 16 ? 5 * (ks >> 1) + 2 * (ks & 1) : 10 * (ks - 16) + 4, nB = ks < 16 ? nA + 1 : nA + 5;
      vo1 = (unsigned)((16 * (r8 < 4 ? nA : nB) + 4 * gg + (r8 & 3)) * 1728 + 64 * cg1 + 8 * ch8) * 2;
    }
  }
  STAMP(22);
  asm volatile("s_cmp_lt_u32 %7, 2\n\ts_cbranch_scc1 1f\n\ts_waitcnt vmcnt(0)\n\ts_branch 2f\n1:\n\ts_waitcnt vmcnt(5)\n2:"
               : "+v"(xv[0]), "+v"(xv[1]), "+v"(xv[2]), "+v"(xv[3]), "+v"(xv[4]), "+v"(epoch), "+v"(warm0) : "s"(w) : "memory", "scc");
  if (__builtin_expect(w >= 2, 0)) {
    P0B_INDEX();
    (void)rg1; (void)cg1; (void)rgA; (void)cgA;
    const unsigned sb = ldsb + L_B + (w & 1) * 15360;
    const rsrc_t rA = __builtin_amdgcn_make_buffer_rsrc((void*)(gv < 432 ? w2 : wo), 0, gv < 432 ? 1728 * 640 * 2 : 640 * 640 * 2, 0x00020000);
    for (int i = 0; i < nAi; ++i) dma_item(rA, voA + i * 128, sb + i * 1024);
    const rsrc_t r1 = __builtin_amdgcn_make_buffer_rsrc((void*)w1, 0, 640 * 1728 * 2, 0x00020000);
    for (int i = 0; i < n1; ++i) dma_item(r1, vo1 + i * 128, sb + (10 + i) * 1024);
  }
  STAMP(23);
  {
    float f[20];
#pragma unroll
    for (int i = 0; i < 5; ++i) { f[4 * i] = blo(xv[i].x); f[4 * i + 1] = bhi(xv[i].x); f[4 * i + 2] = blo(xv[i].y); f[4 * i + 3] = bhi(xv[i].y); }
    float s4[4];
#pragma unroll
    for (int j = 0; j < 4; ++j) s4[j] = (f[j] + f[4 + j]) + (f[8 + j] + f[12 + j]) + f[16 + j];
    const float mean = sum_x16(sum16((s4[0] + s4[1]) + (s4[2] + s4[3]))) * (1.f / 640.f);
#pragma unroll
    for (int i = 0; i < 20; ++i) f[i] -= mean;
#pragma unroll
    for (int j = 0; j < 4; ++j) s4[j] = (f[j] * f[j] + f[4 + j] * f[4 + j]) + (f[8 + j] * f[8 + j] + f[12 + j] * f[12 + j]) + f[16 + j] * f[16 + j];
    const float rstd = __builtin_amdgcn_rsqf(sum_x16(sum16((s4[0] + s4[1]) + (s4[2] + s4[3]))) * (1.f / 640.f) + 1e-5f);
    LAS char* sc = lds + L_A + ((lane & 31) >> 1) * 128 + (2 * w + (lane >> 5)) * 16 + (lane & 1) * 8;
#pragma unroll
    for (int i = 0; i < 5; ++i)
      *(LAS v2u*)(sc + i * 2048) = v2u{pk2(f[4 * i] * rstd, f[4 * i + 1] * rstd), pk2(f[4 * i + 2] * rstd, f[4 * i + 3] * rstd)};
  }
  STAMP(24);
  BAR();
  STAMP(25);
  unsigned tag = 0;
  if (__builtin_expect(w < 2, 1)) {
#pragma unroll
    for (int i = 0; i < 5; ++i) {
      const int L = 320 * w + 64 * i + lane, c = L >> 3, tt = 8 * (m & 1) + (L & 7);
      st128(*(LAS v4u*)(lds + L_A + L * 16), ws, OFF_XN + ((8 * P + (m >> 1)) * 20 + (c >> 2)) * 1024 + ((c & 3) * 16 + tt) * 16, 0);
    }
    asm volatile("s_waitcnt vmcnt(5)" ::: "memory");
    const int pos = 64 * w + lane, nb = pos >> 4, n = pos & 15;
#pragma unroll
    for (int i = 0; i < 5; ++i) {
      const v4u o = item_read(lds + L_A + 16384 + (w * 5 + i) * 1024, lane);
      const int rg = 5 * P + i, ks = rg >> 2, gg = rg & 3;
      if (pos < 120) st128(o, ws, OFF_WQKV + m * WQKV_STR + (nb < 7 ? (nb * 20 + ks) * 1024 + (gg * 16 + n) * 16 : 143360 + ks * 512 + (gg * 8 + n) * 16), 0);
    }
    STAMP(33);
    DRAIN();
  }
  tag = __builtin_amdgcn_readfirstlane(epoch) * 2654435761u + 0x9E3779B9u;
  STAMP(26);
  BAR();
  if (tid == 0) __hip_atomic_store(flg + (0 * 256 + cuid) * 32, tag, RLX);
  STAMP(1);
  if (w < 2) *(LAS v4u*)(lds + L_IMG + tid * 32) = v4u{0u, 0u, 0u, 0u}, *(LAS v4u*)(lds + L_IMG + tid * 32 + 16) = v4u{0u, 0u, 0u, 0u};
  else {
    P0B_INDEX();
    LAS char* im = lds + L_B + (w & 1) * 15360;
    DRAIN();
    const unsigned baseA = (gv < 432 ? OFF_W2 : OFF_WO) + ((rgA >> 2) * 5 * 64 + (rgA & 3) * 16) * 16, strA = gv < 432 ? W2_STR : 102400u;
    for (int i = 0; i < nAi; ++i) {
      const int n = 64 * (cgA + i) + lane, c = n / 80, nl = n - 80 * c;
      st128(item_read(im + i * 1024, lane), ws, baseA + c * strA + (nl >> 4) * 1024 + (nl & 15) * 16, 0);
    }
    const unsigned base1 = OFF_W1 + ((rg1 >> 2) * 7 * 64 + (rg1 & 3) * 16) * 16;
    for (int i = 0; i < n1; ++i) {
      const int n = 64 * (cg1 + i) + lane;
      const int nb = n >> 4, h2 = nb >= 54 ? 1 : 0, nbp = nb - 54 * h2;
      const int tt = nbp < 42 ? nbp / 7 : 6 + (nbp - 42) / 6, nbl = nbp - (tt < 6 ? 7 * tt : 42 + 6 * (tt - 6));
      st128(item_read(im + (10 + i) * 1024, lane), ws, base1 + (8 * h2 + tt) * W1_STR + nbl * 1024 + (n & 15) * 16, 0);
    }
  }
  STAMP(2);

  if (w == 0) {
    const int l = lane & 15;
    wait_flag(flg + (0 * 256 + (lane < 16 ? P * 16 + l : l * 16 + m)) * 32, tag, lane < 32);
  }
  BAR();
  STAMP(3);
  v4u fb;
  v4u xn[2][20];
  f32x4 akv[2][5];
  v4u wqs[13];
  {
    constexpr int D1 = 5;
    for (int q = w; q < 25; q += 4) dma4(ws, l16, OFF_WQKV + m * WQKV_STR + q * 4096, ldsb + L_A + q * 4096);
    const unsigned xb = OFF_XN + (8 * P + 2 * w) * 20 * 1024;
#pragma unroll
    for (int ks = 0; ks < D1; ++ks) {
#pragma unroll
      for (int u = 0; u < 2; ++u) xn[u][ks] = LDF(xb, u * 20 + ks);
      SCHED();
    }
    asm volatile("s_waitcnt vmcnt(10)" ::: "memory");
    BAR();
    if (tid == 0) __hip_atomic_store((gu32*)(wsp + OFF_FLAGB) + cuid, tag, RLX);
    STAMP(17);
#pragma unroll
    for (int u = 0; u < 2; ++u)
#pragma unroll
      for (int n = 0; n < 5; ++n) akv[u][n] = f32x4{0.f, 0.f, 0.f, 0.f};
    v4u wf[2][5];
#pragma unroll
    for (int n = 0; n < 5; ++n) wf[0][n] = LDSF(L_A + (n * 20) * 1024);
    SCHED();
    const unsigned wqb = OFF_WQKV + m * WQKV_STR + 102400 + w * 1024;
#pragma unroll
    for (int ks = 0; ks < 20; ++ks) {
      if (ks + D1 < 20) {
#pragma unroll
        for (int u = 0; u < 2; ++u) xn[u][ks + D1] = LDF(xb, u * 20 + ks + D1);
      }
      if (ks >= 4 && ks < 17) wqs[ks - 4] = ld128(ws, l16, wqb + (ks - 4 < 12 || w < 2 ? ks - 4 : 11) * 4096);
      if (ks == 17) fb = __builtin_amdgcn_raw_buffer_load_b128(ws, OFF_FLAGB + l16, 0, AUX_SC1);
      if (ks + 1 < 20) {
#pragma unroll
        for (int n = 0; n < 5; ++n) wf[(ks + 1) & 1][n] = LDSF(L_A + (n * 20 + ks + 1) * 1024);
      }
#pragma unroll
      for (int n = 0; n < 5; ++n)
#pragma unroll
        for (int u = 0; u < 2; ++u) akv[u][n] = mfma(xn[u][ks], wf[ks & 1][n], akv[u][n]);
#pragma unroll
      for (int n = 0; n < 5; ++n) { SGB(0x100, 1); SGB(0x8, 1); if (n < 3) SGB(0x20, 1); SGB(0x8, 1); }
      SCHED();
    }
  }
  STAMP(4);
  if (!__all(fb.x == tag && fb.y == tag && fb.z == tag && fb.w == tag)) {
    unsigned sp = 0;
    for (;;) {
      asm volatile("" ::: "memory");
      fb = __builtin_amdgcn_raw_buffer_load_b128(ws, OFF_FLAGB + l16, 0, AUX_SC1);
      if (__all(fb.x == tag && fb.y == tag && fb.z == tag && fb.w == tag)) break;
      if (++sp > SPIN_CAP) break;
    }
  }
  v4u w2f[14][5];
  const int k4s = w < 2 ? 14 * w : 28 + 13 * (w - 2);
  const unsigned w2b = OFF_W2 + ct * W2_STR + k4s * 5 * 1024;
#define W2LD(f0, f1) _Pragma("unroll") for (int f_ = (f0); f_ < (f1); ++f_) w2f[f_ / 5][f_ % 5] = LDF(w2b, f_); SCHED()
  W2LD(0, 12);
  BAR();
#pragma unroll
  for (int u = 0; u < 2; ++u)
#pragma unroll
    for (int n = 0; n < 5; ++n) {
      f32x4 a = akv[u][n];
      if (n < 2) { a[0] = phi(a[0]); a[1] = phi(a[1]); a[2] = phi(a[2]); a[3] = phi(a[3]); }
      else if (n == 2) { if (t16 < 8) { a[0] = phi(a[0]); a[1] = phi(a[1]); a[2] = phi(a[2]); a[3] = phi(a[3]); } }
      *(LAS v2u*)(lds + L_A + (16 * n + t16) * EP + (32 * w + 16 * u + 4 * g) * 2) = v2u{pk2(a[0], a[1]), pk2(a[2], a[3])};
    }
  if (tid < 16) *(LAS v4u*)(lds + L_A + 80 * EP + tid * 16) = v4u{0x3F803F80u, 0x3F803F80u, 0x3F803F80u, 0x3F803F80u};
  BAR();
  W2LD(12, 24);
  STAMP(27);
  {
    const int hd = w >> 1, dt = w & 1;
    f32x4 t0 = {0.f, 0.f, 0.f, 0.f}, t1 = t0;
    const unsigned ra = L_A + (20 * hd + 16 * dt + t16) * EP + g * 16;
    const unsigned rb0 = L_A + (40 + 20 * hd + t16) * EP + g * 16;
    const unsigned rb1 = L_A + (t16 < 4 ? 56 + 20 * hd + t16 : 80) * EP + g * 16;
    v4u fa[4], f0[4], f1[4];
#pragma unroll
    for (int s = 0; s < 4; ++s) { fa[s] = *(LAS v4u*)(lds + ra + s * 64); f0[s] = *(LAS v4u*)(lds + rb0 + s * 64); f1[s] = *(LAS v4u*)(lds + rb1 + s * 64); }
#pragma unroll
    for (int s = 0; s < 4; ++s) { t0 = mfma(fa[s], f0[s], t0); t1 = mfma(fa[s], f1[s], t1); }
    LAS char* pt = lds + L_PT + hd * 1680;
    if (dt == 0) {
      *(LAS f32x4*)(pt + l16) = t0;
      if (t16 < 5) *(LAS f32x4*)(pt + 1024 + (g * 5 + t16) * 16) = t1;
    } else if (g == 0) {
      *(LAS f32x4*)(pt + 1344 + t16 * 16) = t0;
      if (t16 < 5) *(LAS f32x4*)(pt + 1600 + t16 * 16) = t1;
    }
    if (tid >= 250) *(LAS v4u*)(lds + L_PT + 3360 + (tid - 250) * 16) = v4u{0u, 0u, 0u, 0u};
  }
  BAR();
  if (tid < 216) st128(*(LAS v4u*)(lds + L_PT + tid * 16), ws, tid * 16, OFF_PART + (m * 16 + P) * PART_STR);
#pragma unroll
  for (int i = 0; i < 13; ++i)
    if (i < 12 || w < 2) *(LAS v4u*)(lds + L_B + (w + 4 * i) * 1024 + l16) = wqs[i];
  STAMP(28);
  PUBLISH(1);
  BAR();
  STAMP(5);

  unsigned qp[2][3][2];
  {
    f32x4 aq[2][3];
#pragma unroll
    for (int u = 0; u < 2; ++u)
#pragma unroll
      for (int n = 0; n < 3; ++n) aq[u][n] = f32x4{0.f, 0.f, 0.f, 0.f};
    LAS char* lq7 = lds + L_B + 40960 + g * 128 + (t16 & 7) * 16;
    const unsigned wob = OFF_WO + ct * 102400 + w * 1024, wod = ldsb + L_A + w * 1024;
    v4u wf[3][3];
#pragma unroll
    for (int k = 0; k < 2; ++k) { wf[k][0] = LDSF(L_B + k * 1024); wf[k][1] = LDSF(L_B + (20 + k) * 1024); wf[k][2] = *(LAS v4u*)(lq7 + k * 512); }
    SCHED();
#pragma unroll
    for (int ks = 0; ks < 20; ++ks) {
      if (ks >= 4) dma1q(ws, l16, wob + (ks - 4) * 4096, wod + (ks - 4) * 4096);
      if (ks >= 11) dma1q(ws, l16, wob + (ks + 5) * 4096, wod + (ks + 5) * 4096);
      if (ks + 2 < 20) {
        wf[(ks + 2) % 3][0] = LDSF(L_B + (ks + 2) * 1024);
        wf[(ks + 2) % 3][1] = LDSF(L_B + (20 + ks + 2) * 1024);
        wf[(ks + 2) % 3][2] = *(LAS v4u*)(lq7 + (ks + 2) * 512);
      }
#pragma unroll
      for (int n = 0; n < 3; ++n)
#pragma unroll
        for (int u = 0; u < 2; ++u) aq[u][n] = mfma(wf[ks % 3][n], xn[u][ks], aq[u][n]);
#pragma unroll
      for (int n = 0; n < 3; ++n) { SGB(0x100, 1); SGB(0x8, 2); }
      SCHED();
    }
#pragma unroll
    for (int u = 0; u < 2; ++u)
#pragma unroll
      for (int n = 0; n < 3; ++n) {
        qp[u][n][0] = pk2(phi(aq[u][n][0]), phi(aq[u][n][1]));
        qp[u][n][1] = pk2(phi(aq[u][n][2]), phi(aq[u][n][3]));
      }
  }
  BAR();
  unsigned imgoff, imgoffz;
  {
    const int hd = tid >= 105 ? 1 : 0, u = tid - 105 * hd;
    int dq, e;
    if (u < 64) { dq = u >> 4; e = u & 15; }
    else if (u < 84) { const int v = u - 64; dq = v / 5; e = 16 + v - 5 * dq; }
    else if (u < 100) { dq = 4; e = u - 84; }
    else { dq = 4; e = 16 + u - 100; }
    int gq;
    if (hd == 0) gq = dq < 4 ? dq : 0; else gq = dq < 3 ? dq + 1 : (dq == 3 ? 0 : 1);
    const int j0 = dq < 4 ? 0 : 4, ge = e / 5, re = e - 5 * ge;
    const unsigned ib = L_IMG + hd * 2048 + gq * 256 + j0 * 2;
    imgoff = ib + (re < 4 ? (4 * ge + re) * 16 : 1024 + (4 * ge) * 16);
    imgoffz = e == 20 ? ib + 1024 + 16 : 0u;
  }
  STAMP(6);

  wait_flag(flg + (1 * 256 + (lane >> 2) * 16 + m) * 32 + (lane & 3), tag, true);
  STAMP(7);
  v4u pv[16];
  {
    const unsigned vo = (tid < 216 ? tid : 215) * 16, pb = OFF_PART + m * 16 * PART_STR;
#define PV8(o) asm volatile("buffer_load_dwordx4 %0, %8, %9, %10 offen sc1\n\tbuffer_load_dwordx4 %1, %8, %9, %11 offen sc1\n\t" \
      "buffer_load_dwordx4 %2, %8, %9, %12 offen sc1\n\tbuffer_load_dwordx4 %3, %8, %9, %13 offen sc1\n\t" \
      "buffer_load_dwordx4 %4, %8, %9, %14 offen sc1\n\tbuffer_load_dwordx4 %5, %8, %9, %15 offen sc1\n\t" \
      "buffer_load_dwordx4 %6, %8, %9, %16 offen sc1\n\tbuffer_load_dwordx4 %7, %8, %9, %17 offen sc1" \
      : "=&v"(pv[o]), "=&v"(pv[o + 1]), "=&v"(pv[o + 2]), "=&v"(pv[o + 3]), "=&v"(pv[o + 4]), "=&v"(pv[o + 5]), "=&v"(pv[o + 6]), "=&v"(pv[o + 7]) \
      : "v"(vo), "s"(ws), "s"(pb + (o) * PART_STR), "s"(pb + (o + 1) * PART_STR), "s"(pb + (o + 2) * PART_STR), "s"(pb + (o + 3) * PART_STR), \
        "s"(pb + (o + 4) * PART_STR), "s"(pb + (o + 5) * PART_STR), "s"(pb + (o + 6) * PART_STR), "s"(pb + (o + 7) * PART_STR) : "memory")
    PV8(0); PV8(8);
    for (int q = w; q < 12; q += 4) dma4(ws, l16, OFF_W1 + m * W1_STR + q * 4096, ldsb + L_B + q * 4096);
    if (w == 0) dma1(ws, l16, OFF_W1 + m * W1_STR + 49152, ldsb + L_B + 49152);
    asm volatile("s_waitcnt vmcnt(12)"
                 : "+v"(pv[0]), "+v"(pv[1]), "+v"(pv[2]), "+v"(pv[3]), "+v"(pv[4]), "+v"(pv[5]), "+v"(pv[6]), "+v"(pv[7]),
                   "+v"(pv[8]), "+v"(pv[9]), "+v"(pv[10]), "+v"(pv[11]), "+v"(pv[12]), "+v"(pv[13]), "+v"(pv[14]), "+v"(pv[15]) :: "memory");
  }
  if (tid < 210) {
    f32x4 s[4];
#pragma unroll
    for (int i = 0; i < 4; ++i)
      s[i] = (__builtin_bit_cast(f32x4, pv[4 * i]) + __builtin_bit_cast(f32x4, pv[4 * i + 1])) +
             (__builtin_bit_cast(f32x4, pv[4 * i + 2]) + __builtin_bit_cast(f32x4, pv[4 * i + 3]));
    const f32x4 st = (s[0] + s[1]) + (s[2] + s[3]);
    const v2u val = v2u{pk2(st[0], st[1]), pk2(st[2], st[3])};
    if (imgoffz == 0u) *(LAS v2u*)(lds + imgoff) = val;
    else {
#pragma unroll
      for (int ge = 0; ge < 4; ++ge) *(LAS v2u*)(lds + imgoffz + ge * 64) = val;
    }
  }
  BAR();
  STAMP(18);
  {
    v4u ka[2][2];
#pragma unroll
    for (int hd = 0; hd < 2; ++hd)
#pragma unroll
      for (int bb = 0; bb < 2; ++bb) ka[hd][bb] = *(LAS v4u*)(lds + L_IMG + hd * 2048 + bb * 1024 + l16);
#pragma unroll
    for (int u = 0; u < 2; ++u) {
      v4u qa, qb;
      qa.x = qp[u][0][0]; qa.y = qp[u][0][1]; qa.z = g == 0 ? qp[u][1][0] : 0u; qa.w = g == 0 ? qp[u][1][1] : 0u;
      qb.x = g == 0 ? qp[u][2][0] : qp[u][1][0]; qb.y = g == 0 ? qp[u][2][1] : qp[u][1][1];
      qb.z = g == 1 ? qp[u][2][0] : 0u; qb.w = g == 1 ? qp[u][2][1] : 0u;
      const f32x4 z4 = {0.f, 0.f, 0.f, 0.f};
      const f32x4 a0 = mfma(ka[0][0], qa, z4), a1 = mfma(ka[0][1], qa, z4);
      const f32x4 b0 = mfma(ka[1][0], qb, z4), b1 = mfma(ka[1][1], qb, z4);
      const float ra = __builtin_amdgcn_rcpf(a1[1] + 1e-6f), rb = __builtin_amdgcn_rcpf(b1[1] + 1e-6f);
      const unsigned d0 = pk2(a0[0] * ra, a0[1] * ra), d1 = pk2(a0[2] * ra, a0[3] * ra), d2 = pk2(a1[0] * ra, b0[0] * rb),
                     d3 = pk2(b0[1] * rb, b0[2] * rb), d4 = pk2(b0[3] * rb, b1[0] * rb);
      const unsigned oi = (8 * P + 2 * w + u) * 16 + m;
      st128(v4u{d0, d1, d2, d3}, ws, l16, OFF_OM + oi * 1024);
      __builtin_amdgcn_raw_buffer_store_b32(d4, ws, lane * 4, OFF_OR + oi * 256, AUX_SC1);
    }
  }
  PUBLISH(2);
  BAR();
  STAMP(8);
  v2u xr[5];
  {
    const GAS char* xp = (const GAS char*)x + (size_t)(64 * (2 * P + jl) + 16 * w + t16) * 1280 + (80 * ct + 4 * g) * 2;
#pragma unroll
    for (int n = 0; n < 5; ++n) xr[n] = *(const GAS v2u*)(xp + n * 32);
  }
  v4u w1r[23];
  const unsigned w1rb = OFF_W1 + m * W1_STR + 50176 + w * 1024;
#pragma unroll
  for (int i = 0; i < 12; ++i) w1r[i] = ld128(ws, l16, w1rb + i * 4096);
  SCHED();

  f32x4 cs7[7];
  const v4u ones = {0x3F803F80u, 0x3F803F80u, 0x3F803F80u, 0x3F803F80u};
  {
    v4u wv[13];
#pragma unroll
    for (int i = 0; i < 13; ++i) wv[i] = *(LAS v4u*)(lb0 + L_B + (w + 4 * (i < 12 || w == 0 ? i : 11)) * 1024);
#pragma unroll
    for (int j = 0; j < 7; ++j) cs7[j] = mfma(wv[j], ones, f32x4{0.f, 0.f, 0.f, 0.f});
#pragma unroll
    for (int i = 7; i < 12; ++i) cs7[i - 7] = mfma(wv[i], ones, cs7[i - 7]);
    if (w == 0) cs7[5] = mfma(wv[12], ones, cs7[5]);
  }
  STAMP(29);
  wait_flag(flg + (2 * 256 + P * 16 + (lane & 15)) * 32 + ((4 * jl + w) >> 1), tag, lane < 16);
  STAMP(30);
  STAMP(9);
  unsigned hp[5][2];
  {
    constexpr int D2 = 8;
    v4u of[20];
    const unsigned ob = OFF_OM + (8 * P + 4 * jl + w) * 16 * 1024, orb = OFF_OR + (8 * P + 4 * jl + w) * 16 * 256;
#define LDO(ks) ((ks) < 16 ? LDF(ob, ks) : v4u{ld32(ws, lane * 4 + (4 * ((ks) - 16)) * 256, orb), ld32(ws, lane * 4 + (4 * ((ks) - 16) + 1) * 256, orb), \
                                              ld32(ws, lane * 4 + (4 * ((ks) - 16) + 2) * 256, orb), ld32(ws, lane * 4 + (4 * ((ks) - 16) + 3) * 256, orb)})
#pragma unroll
    for (int ks = 0; ks < D2; ++ks) { of[ks] = LDO(ks); SCHED(); }
    f32x4 ah[5];
#pragma unroll
    for (int n = 0; n < 5; ++n) ah[n] = f32x4{0.f, 0.f, 0.f, 0.f};
    v4u wf[3][5];
#pragma unroll
    for (int n = 0; n < 5; ++n) { wf[0][n] = LDSF(L_A + n * 1024); wf[1][n] = LDSF(L_A + (5 + n) * 1024); }
    SCHED();
#pragma unroll
    for (int ks = 0; ks < 20; ++ks) {
      if (ks + D2 < 20) of[ks + D2] = LDO(ks + D2);
      else {
        const int i0 = ks < 15 ? 12 + 2 * (ks - 12) : 15 + (ks - 12);
        w1r[i0] = ld128(ws, l16, w1rb + (i0 < 22 || w < 3 ? i0 : 21) * 4096);
        if (ks < 15) w1r[i0 + 1] = ld128(ws, l16, w1rb + (i0 + 1) * 4096);
      }
      if (ks + 2 < 20) {
#pragma unroll
        for (int n = 0; n < 5; ++n) wf[(ks + 2) % 3][n] = LDSF(L_A + ((ks + 2) * 5 + n) * 1024);
      }
#pragma unroll
      for (int n = 0; n < 5; ++n) ah[n] = mfma(wf[ks % 3][n], of[ks], ah[n]);
#pragma unroll
      for (int n = 0; n < 5; ++n) { SGB(0x100, 1); SGB(0x8, 1); if (n < 4) SGB(0x20, 1); }
      SCHED();
    }
    STAMP(19);
    float hv[20];
#pragma unroll
    for (int n = 0; n < 5; ++n) {
      hp[n][0] = pk2(blo(xr[n].x) + ah[n][0], bhi(xr[n].x) + ah[n][1]);
      hp[n][1] = pk2(blo(xr[n].y) + ah[n][2], bhi(xr[n].y) + ah[n][3]);
    }
    const unsigned hi = (8 * P + 4 * jl + w) * 8 + ct;
    st128(v4u{hp[0][0], hp[0][1], hp[1][0], hp[1][1]}, ws, l16, OFF_HF + hi * 2048);
    st128(v4u{hp[2][0], hp[2][1], hp[3][0], hp[3][1]}, ws, l16 + 1024, OFF_HF + hi * 2048);
    st64(v2u{hp[4][0], hp[4][1]}, ws, lane * 8, OFF_HH + hi * 512);
    float s4[5];
#pragma unroll
    for (int n = 0; n < 5; ++n) {
      hv[4 * n] = blo(hp[n][0]); hv[4 * n + 1] = bhi(hp[n][0]); hv[4 * n + 2] = blo(hp[n][1]); hv[4 * n + 3] = bhi(hp[n][1]);
      s4[n] = (hv[4 * n] + hv[4 * n + 1]) + (hv[4 * n + 2] + hv[4 * n + 3]);
    }
    const float mi = sum_g((s4[0] + s4[1]) + (s4[2] + s4[3]) + s4[4]) * (1.f / 80.f);
#pragma unroll
    for (int n = 0; n < 5; ++n) {
      const float a = hv[4 * n] - mi, bq = hv[4 * n + 1] - mi, c = hv[4 * n + 2] - mi, d = hv[4 * n + 3] - mi;
      s4[n] = (a * a + bq * bq) + (c * c + d * d);
    }
    const float q = sum_g((s4[0] + s4[1]) + (s4[2] + s4[3]) + s4[4]);
    if (g == 0) st64(v2u{asu(mi), asu(q)}, ws, t16 * 8, OFF_ST + ((8 * P + 4 * jl + w) * 8 + ct) * 128);
  }
  PUBLISH(3);
  BAR();
  STAMP(10);
#pragma unroll
  for (int i = 0; i < 23; ++i)
    if (i < 22 || w < 3) {
      *(LAS v4u*)(lds + L_A + (w + 4 * i) * 1024 + l16) = w1r[i];
      cs7[i % 7] = mfma(w1r[i], ones, cs7[i % 7]);
    }
  if (t16 == 0) {
#pragma unroll
    for (int j = 0; j < 7; ++j) *(LAS f32x4*)(lds + L_CS + (w * 7 + (w + 4 * j) % 7) * 64 + g * 16) = cs7[j];
  }
  W2LD(24, 36);
  STAMP(20);

  BAR();
  {
    const int i = (lane & 15) >> 1;
    wait_flag(flg + (3 * 256 + P * 16 + 8 * (i >> 2) + 4 * (w >> 1) + (i & 3)) * 32 + ((2 * w + (lane & 1)) & 3), tag, lane < 16);
  }
  STAMP(11);
  {
    constexpr int D3 = 5;
    v4u hf[2][20];
    const unsigned hb = OFF_HF + (8 * P + 2 * w) * 16 * 1024, hhb = OFF_HH + (8 * P + 2 * w) * 8 * 512;
    auto ldh = [&](int u, int ks) -> v4u {
      if (ks < 16) return LDF(hb, u * 16 + ks);
      const v2u a = ld64(ws, lane * 8 + (2 * (ks - 16)) * 512, hhb + u * 4096), c = ld64(ws, lane * 8 + (2 * (ks - 16) + 1) * 512, hhb + u * 4096);
      return v4u{a.x, a.y, c.x, c.y};
    };
    v2u stv[2][2];
#pragma unroll
    for (int ks = 0; ks < D3; ++ks) {
#pragma unroll
      for (int u = 0; u < 2; ++u) hf[u][ks] = ldh(u, ks);
      SCHED();
    }
#pragma unroll
    for (int u = 0; u < 2; ++u)
#pragma unroll
      for (int i = 0; i < 2; ++i) stv[u][i] = ld64(ws, t16 * 8 + (2 * g + i) * 128, OFF_ST + (8 * P + 2 * w + u) * 1024);
    f32x4 cs[7];
#pragma unroll
    for (int n = 0; n < 7; ++n) {
      f32x4 cw[4];
#pragma unroll
      for (int ww = 0; ww < 4; ++ww) cw[ww] = *(LAS f32x4*)(lds + L_CS + (ww * 7 + n) * 64 + g * 16);
      cs[n] = (cw[0] + cw[1]) + (cw[2] + cw[3]);
    }
    f32x4 a3[2][7];
#pragma unroll
    for (int u = 0; u < 2; ++u)
#pragma unroll
      for (int n = 0; n < 7; ++n) a3[u][n] = f32x4{0.f, 0.f, 0.f, 0.f};
    v4u wf[2][7];
#pragma unroll
    for (int n = 0; n < 7; ++n) wf[0][n] = LDSF(W1A(0, n));
    SCHED();
#pragma unroll
    for (int ks = 0; ks < 20; ++ks) {
      if (ks + D3 < 20) {
#pragma unroll
        for (int u = 0; u < 2; ++u) hf[u][ks + D3] = ldh(u, ks + D3);
      }
      w2f[(36 + ks) / 5][(36 + ks) % 5] = LDF(w2b, 36 + ks);
      if (ks + 1 < 20) {
#pragma unroll
        for (int n = 0; n < 7; ++n) wf[(ks + 1) & 1][n] = LDSF(W1A(ks + 1, n));
      }
#pragma unroll
      for (int n = 0; n < 7; ++n)
#pragma unroll
        for (int u = 0; u < 2; ++u) a3[u][n] = mfma(wf[ks & 1][n], hf[u][ks], a3[u][n]);
#pragma unroll
      for (int n = 0; n < 7; ++n) { SGB(0x100, 1); SGB(0x8, 1); if (n < 5) SGB(0x20, 1); SGB(0x8, 1); }
      SCHED();
    }
    STAMP(12);
    float mean[2], rstd[2];
#pragma unroll
    for (int u = 0; u < 2; ++u) {
      const float m0 = asf(stv[u][0].x), q0 = asf(stv[u][0].y), m1 = asf(stv[u][1].x), q1 = asf(stv[u][1].y);
      const float mu = sum_g(m0 + m1) * 0.125f;
      const float M2 = sum_g(q0 + q1 + 80.f * ((m0 - mu) * (m0 - mu) + (m1 - mu) * (m1 - mu)));
      rstd[u] = __builtin_amdgcn_rsqf(M2 * (1.f / 640.f) + 1e-5f);
      mean[u] = mu * rstd[u];
    }
    unsigned hq[2][7][2];
#pragma unroll
    for (int n = 0; n < 7; ++n) {
#pragma unroll
      for (int u = 0; u < 2; ++u) {
        const f32x2 r2 = {rstd[u], rstd[u]}, m2 = {-mean[u], -mean[u]};
        const f32x2 lo = __builtin_elementwise_fma(f32x2{a3[u][n][0], a3[u][n][1]}, r2, f32x2{cs[n][0], cs[n][1]} * m2);
        const f32x2 hi2 = __builtin_elementwise_fma(f32x2{a3[u][n][2], a3[u][n][3]}, r2, f32x2{cs[n][2], cs[n][3]} * m2);
        const float v0 = fmaxf(lo[0], 0.f), v1 = fmaxf(lo[1], 0.f), v2 = fmaxf(hi2[0], 0.f), v3 = fmaxf(hi2[1], 0.f);
        hq[u][n][0] = pk2(v0, v1); hq[u][n][1] = pk2(v2, v3);
      }
    }
#pragma unroll
    for (int u = 0; u < 2; ++u) {
      const unsigned di = (8 * P + 2 * w + u) * 16 + m;
      st128(v4u{hq[u][0][0], hq[u][0][1], hq[u][1][0], hq[u][1][1]}, ws, l16, OFF_DF + di * 3072);
      st128(v4u{hq[u][2][0], hq[u][2][1], hq[u][3][0], hq[u][3][1]}, ws, l16 + 1024, OFF_DF + di * 3072);
      st128(v4u{hq[u][4][0], hq[u][4][1], hq[u][5][0], hq[u][5][1]}, ws, l16 + 2048, OFF_DF + di * 3072);
      if ((m & 7) < 6) st64(v2u{hq[u][6][0], hq[u][6][1]}, ws, lane * 8, OFF_DH + di * 512);
    }
  }
  STAMP(21);
  PUBLISH(4);
  if (tid == 0 && cuid == 0) __hip_atomic_store((gu32*)(wsp + OFF_EPOCH), epoch + 1u, RLX);
  STAMP(13);
  W2LD(56, 70);
  BAR();
  {
    const int l = lane & 15, j = (l - 8) >> 2, ci = (w < 2 ? 2 * w : w + 2) + (j > 0 && w < 2 ? 1 : 0);
    const int tile = l < 8 ? 4 * w + (l >> 1) : 8 * (ci >= 3 ? 1 : 0) + 2 * (ci >= 3 ? ci - 3 : ci) + ((l >> 1) & 1);
    wait_flag(flg + (4 * 256 + P * 16 + tile) * 32 + 2 * jl + (l & 1), tag, lane < 16);
  }
  STAMP(14);
  {
    f32x4 a4[4][5];
#pragma unroll
    for (int u = 0; u < 4; ++u)
#pragma unroll
      for (int n = 0; n < 5; ++n) a4[u][n] = f32x4{0.f, 0.f, 0.f, 0.f};
    const unsigned hb = OFF_DF + ((8 * P + 4 * jl) * 48 + 12 * w) * 1024, dhb = OFF_DH + (8 * P + 4 * jl) * 16 * 512;
    const int ci0 = w < 2 ? 2 * w : w + 2;
    auto lda = [&](int u, int k) -> v4u {
      if (k < 12) return ld128(ws, l16 + (k & 3) * 1024, hb + u * 48 * 1024 + (k >> 2) * 4096);
      const int ci = ci0 + (k - 12), h2 = ci >= 3 ? 1 : 0, tA = 8 * h2 + 2 * (ci - 3 * h2);
      const v2u a = ld64(ws, lane * 8, dhb + (u * 16 + tA) * 512), c = ld64(ws, lane * 8 + 512, dhb + (u * 16 + tA) * 512);
      return v4u{a.x, a.y, c.x, c.y};
    };
    constexpr int DP = 3;
    v4u af[DP + 1][4];
#pragma unroll
    for (int k = 0; k < DP; ++k)
#pragma unroll
      for (int u = 0; u < 4; ++u) { af[k][u] = lda(u, k); SCHED(); }
#pragma unroll
    for (int k = 0; k < 14; ++k) {
      if (k + DP < 14) {
        if (k + DP < 13 || w < 2) {
#pragma unroll
          for (int u = 0; u < 4; ++u) af[(k + DP) % (DP + 1)][u] = lda(u, k + DP);
        }
      }
      if (k < 13 || w < 2) {
#pragma unroll
        for (int u = 0; u < 4; ++u)
#pragma unroll
          for (int n = 0; n < 5; ++n) a4[u][n] = mfma(w2f[k][n], af[k % (DP + 1)][u], a4[u][n]);
      }
#pragma unroll
      for (int u = 0; u < 4; ++u) { SGB(0x20, 2); SGB(0x8, 5); }
      SCHED();
    }
    STAMP(15);
#pragma unroll
    for (int u = 0; u < 4; ++u) {
      if (u != w) {
        const int si = w < u ? w : w - 1;
#pragma unroll
        for (int n = 0; n < 5; ++n) *(LAS f32x4*)(lds + L_A + ((u * 3 + si) * 5 + n) * 1024 + l16) = a4[u][n];
      }
    }
    BAR();
    GAS char* op = (GAS char*)out + (size_t)(64 * (2 * P + jl) + 16 * w + t16) * 1280 + (80 * ct + 4 * g) * 2;
#pragma unroll
    for (int n = 0; n < 5; ++n) {
      f32x4 v = w == 0 ? a4[0][n] : (w == 1 ? a4[1][n] : (w == 2 ? a4[2][n] : a4[3][n]));
      f32x4 p[3];
#pragma unroll
      for (int si = 0; si < 3; ++si) p[si] = *(LAS f32x4*)(lds + L_A + ((w * 3 + si) * 5 + n) * 1024 + l16);
      v = (v + p[0]) + (p[1] + p[2]);
      const v2u o = {pk2(blo(hp[n][0]) + v[0], bhi(hp[n][0]) + v[1]), pk2(blo(hp[n][1]) + v[2], bhi(hp[n][1]) + v[3])};
      *(GAS v2u*)(op + n * 32) = o;
    }
  }
  STAMP(16);
}

extern "C" void kernel_launch(void* const* d_in, const int* in_sizes, int n_in, void* d_out, int out_size, void* d_ws, size_t ws_size,
                              hipStream_t stream) {
  (void)in_sizes; (void)n_in; (void)out_size; (void)ws_size;
  hipFuncSetAttribute((const void*)fused_block, hipFuncAttributeMaxDynamicSharedMemorySize, LDS_BYTES);
  fused_block<<<256, 256, LDS_BYTES, stream>>>((const unsigned short*)d_in[0], (const unsigned short*)d_in[1], (const unsigned short*)d_in[2],
                                               (const unsigned short*)d_in[4], (const unsigned short*)d_in[5], (unsigned short*)d_out,
                                               (char*)d_ws);
}
```

```cpp
#include <hip/hip_runtime.h>
#include <hip/hip_bf16.h>
#include <stdint.h>

typedef unsigned v4u __attribute__((ext_vector_type(4)));
typedef unsigned v3u __attribute__((ext_vector_type(3)));
typedef unsigned v2u __attribute__((ext_vector_type(2)));
typedef float f32x4 __attribute__((ext_vector_type(4)));
typedef float f32x2 __attribute__((ext_vector_type(2)));
typedef short bf16x8 __attribute__((ext_vector_type(8)));
typedef short s16x4 __attribute__((ext_vector_type(4)));
typedef __bf16 bf2 __attribute__((ext_vector_type(2)));
typedef __amdgpu_buffer_rsrc_t rsrc_t;
#define GAS __attribute__((address_space(1)))
#define LAS __attribute__((address_space(3)))
#define DI __device__ __forceinline__
#define RLX __ATOMIC_RELAXED, __HIP_MEMORY_SCOPE_AGENT
typedef GAS unsigned gu32;

constexpr int CODE_LINES = 294;

constexpr unsigned OFF_EPOCH = 0;
constexpr unsigned OFF_FLAG = 4096;
constexpr unsigned OFF_FLAGB = OFF_FLAG + 5 * 256 * 128;
constexpr unsigned OFF_XN = 256 * 1024;
constexpr unsigned WQKV_STR = 153600;
constexpr unsigned OFF_WQKV = OFF_XN + 128 * 20 * 1024;
constexpr unsigned OFF_WO = OFF_WQKV + 16 * WQKV_STR;
constexpr unsigned W1_STR = 20 * 7 * 1024;
constexpr unsigned OFF_W1 = OFF_WO + 8 * 102400;
constexpr unsigned W2_STR = 54 * 5 * 1024;
constexpr unsigned OFF_W2 = OFF_W1 + 16 * W1_STR;
constexpr unsigned PART_STR = 3456;
constexpr unsigned OFF_PART = OFF_W2 + 8 * W2_STR;
constexpr unsigned OFF_OM = OFF_PART + 256 * PART_STR;
constexpr unsigned OFF_OR = OFF_OM + 128 * 16 * 1024;
constexpr unsigned OFF_HF = OFF_OR + 128 * 16 * 256;
constexpr unsigned OFF_HH = OFF_HF + 128 * 16 * 1024;
constexpr unsigned OFF_ST = OFF_HH + 128 * 8 * 512;
constexpr unsigned OFF_DF = OFF_ST + 128 * 8 * 128;
constexpr unsigned OFF_DH = OFF_DF + 128 * 48 * 1024;
constexpr unsigned WS_BYTES = OFF_DH + 128 * 16 * 512;

constexpr unsigned L_A = 0;
constexpr unsigned L_B = 102400;
constexpr unsigned L_M = 153600;
constexpr unsigned L_IMG = L_M;
constexpr unsigned L_CS = L_M + 4096;
constexpr unsigned L_PT = L_M + 6144;
constexpr unsigned LDS_BYTES = 163840;
constexpr unsigned EP = 272;

constexpr int AUX_SC1 = 16;
constexpr unsigned SPIN_CAP = 1u << 20;

DI f32x4 mfma(v4u a, v4u b, f32x4 c) {
  return __builtin_amdgcn_mfma_f32_16x16x32_bf16(__builtin_bit_cast(bf16x8, a), __builtin_bit_cast(bf16x8, b), c, 0, 0, 0);
}
DI unsigned pk2(float a, float b) { bf2 r = {(__bf16)a, (__bf16)b}; return __builtin_bit_cast(unsigned, r); }
DI float asf(unsigned u) { return __builtin_bit_cast(float, u); }
DI unsigned asu(float f) { return __builtin_bit_cast(unsigned, f); }
DI float blo(unsigned u) { return asf(u << 16); }
DI float bhi(unsigned u) { return asf(u & 0xffff0000u); }
DI float phi(float t) { return (t > 0.f ? t + 1.f : __expf(t)) + 1e-6f; }
template <int C> DI float dpp_add(float v) { return v + asf((unsigned)__builtin_amdgcn_update_dpp(0, (int)asu(v), C, 0xF, 0xF, true)); }
DI float sum16(float v) { v = dpp_add<0xB1>(v); v = dpp_add<0x4E>(v); v = dpp_add<0x141>(v); v = dpp_add<0x140>(v); return v; }
DI float sum_x16(float v) { const unsigned u = asu(v); auto r = __builtin_amdgcn_permlane16_swap(u, u, false, false); return asf(r[0]) + asf(r[1]); }
DI float sum_x32(float v) { const unsigned u = asu(v); auto r = __builtin_amdgcn_permlane32_swap(u, u, false, false); return asf(r[0]) + asf(r[1]); }
DI float sum_g(float v) { return sum_x32(sum_x16(v)); }
#define SCHED() __builtin_amdgcn_sched_barrier(0)
#define SGB(mask, n) __builtin_amdgcn_sched_group_barrier(mask, n, 0)
#define DRAIN() asm volatile("s_waitcnt vmcnt(0)" ::: "memory")
#define BAR() do { asm volatile("s_waitcnt lgkmcnt(0)" ::: "memory"); __builtin_amdgcn_s_barrier(); asm volatile("" ::: "memory"); } while (0)

DI v4u ld128(rsrc_t rs, unsigned voff, unsigned soff) { return __builtin_amdgcn_raw_buffer_load_b128(rs, voff, soff, AUX_SC1); }
DI void st128(v4u v, rsrc_t rs, unsigned voff, unsigned soff) { __builtin_amdgcn_raw_buffer_store_b128(v, rs, voff, soff, AUX_SC1); }
DI v2u ld64(rsrc_t rs, unsigned voff, unsigned soff) { return __builtin_amdgcn_raw_buffer_load_b64(rs, voff, soff, AUX_SC1); }
DI unsigned ld32(rsrc_t rs, unsigned voff, unsigned soff) { return __builtin_amdgcn_raw_buffer_load_b32(rs, voff, soff, AUX_SC1); }
DI void st64(v2u v, rsrc_t rs, unsigned voff, unsigned soff) { __builtin_amdgcn_raw_buffer_store_b64(v, rs, voff, soff, AUX_SC1); }

DI void dma4(rsrc_t rs, unsigned voff, unsigned soff, unsigned m0v) {
  asm volatile("s_nop 4\n\ts_mov_b32 m0, %0\n\ts_nop 0\n\t"
               "buffer_load_dwordx4 %1, %2, %3 offen sc1 lds\n\t"
               "buffer_load_dwordx4 %1, %2, %3 offen offset:1024 sc1 lds\n\t"
               "buffer_load_dwordx4 %1, %2, %3 offen offset:2048 sc1 lds\n\t"
               "buffer_load_dwordx4 %1, %2, %3 offen offset:3072 sc1 lds"
               :: "s"(m0v), "v"(voff), "s"(rs), "s"(soff) : "m0", "memory");
}
DI void dma1(rsrc_t rs, unsigned voff, unsigned soff, unsigned m0v) {
  asm volatile("s_nop 4\n\ts_mov_b32 m0, %0\n\ts_nop 0\n\t"
               "buffer_load_dwordx4 %1, %2, %3 offen sc1 lds"
               :: "s"(m0v), "v"(voff), "s"(rs), "s"(soff) : "m0", "memory");
}
DI void dma1q(rsrc_t rs, unsigned voff, unsigned soff, unsigned m0v) {
  asm volatile("s_mov_b32 m0, %0\n\ts_nop 0\n\tbuffer_load_dwordx4 %1, %2, %3 offen sc1 lds"
               :: "s"(m0v), "v"(voff), "s"(rs), "s"(soff) : "m0", "memory");
}
DI void dma_item(rsrc_t rs, unsigned voff, unsigned m0v) {
  asm volatile("s_nop 4\n\ts_mov_b32 m0, %0\n\ts_nop 0\n\t"
               "buffer_load_dwordx4 %1, %2, 0 offen lds"
               :: "s"(m0v), "v"(voff), "s"(rs) : "m0", "memory");
}
DI v4u item_read(LAS char* img, int lane) {
  LAS char* p = img + ((lane >> 2) & 3) * 128 + (lane >> 4) * 32 + (lane & 3) * 8;
  const s16x4 a = __builtin_amdgcn_ds_read_tr16_b64_v4i16((LAS s16x4*)p);
  const s16x4 b = __builtin_amdgcn_ds_read_tr16_b64_v4i16((LAS s16x4*)(p + 512));
  const v2u x = __builtin_bit_cast(v2u, a), y = __builtin_bit_cast(v2u, b);
  return v4u{x.x, x.y, y.x, y.y};
}
#define LDF(sbase, i) ld128(ws, l16 + ((i) & 3) * 1024, (sbase) + ((i) >> 2) * 4096)
#define LDSF(off) (*(LAS v4u*)(((off) < 65536 ? lb0 : ((off) < 131072 ? lb1 : lb2)) + ((off) & 65535)))
#define W1A(ks, n) (((ks) < 7 ? L_B + (ks) * 7168 : L_A + ((ks) - 7) * 7168) + (n) * 1024)

DI void wait_flag(gu32* f, unsigned tag, bool active) {
  unsigned sp = 0;
  for (;;) {
    unsigned v = active ? __hip_atomic_load(f, RLX) : tag;
    if (__all(v == tag)) break;
    if (++sp > SPIN_CAP) break;
  }
  __builtin_amdgcn_fence(__ATOMIC_ACQUIRE, "wavefront");
}

#define PUBLISH(ph) do { DRAIN(); if (lane == 0) __hip_atomic_store(flg + ((ph) * 256 + cuid) * 32 + w, tag, RLX); } while (0)


__global__ __launch_bounds__(256, 1) void fused_block(const unsigned short* __restrict__ x, const unsigned short* __restrict__ wqkv,
                                                      const unsigned short* __restrict__ wo, const unsigned short* __restrict__ w1,
                                                      const unsigned short* __restrict__ w2, unsigned short* __restrict__ out,
                                                      char* __restrict__ wsp) {
  unsigned warm0;
  {
    unsigned long long pc;
    asm volatile("s_getpc_b64 %0" : "=s"(pc));
    const unsigned ln = min((((blockIdx.x >> 3) + 8u * (threadIdx.x >> 6)) & 31u) * 11u + min(threadIdx.x & 63u, 10u), (unsigned)CODE_LINES - 1u);
    asm volatile("global_load_dword %0, %1, off" : "=v"(warm0) : "v"((const GAS char*)((pc & ~127ull) + (unsigned long long)(ln * 128u))) : "memory");
  }
  __builtin_amdgcn_sched_barrier(0);
  extern __shared__ __attribute__((aligned(16))) char smem[];
  LAS char* lds = (LAS char*)smem;
  const unsigned ldsb = (unsigned)(size_t)lds;
  const int tid = threadIdx.x, lane = tid & 63;
  const int w = __builtin_amdgcn_readfirstlane(tid >> 6);
  const int g = lane >> 4, t16 = lane & 15;
  const int b = blockIdx.x, xcd = b & 7, sl = b >> 3;
  const int c2 = xcd & 1, P = 4 * (xcd >> 1) + (sl >> 3), m = 8 * c2 + (sl & 7);
  const int cuid = P * 16 + m;
  const int jl = (m & 7) >> 2, ct = 4 * c2 + (m & 3);
  const unsigned l16 = lane * 16;
  const rsrc_t ws = __builtin_amdgcn_make_buffer_rsrc(wsp, 0, WS_BYTES, 0x00020000);
  gu32* flg = (gu32*)(wsp + OFF_FLAG);
  v2u xv[5];
  {
    const GAS char* xp = (const GAS char*)(x + (size_t)(128 * P + 8 * m + 2 * w + (lane >> 5)) * 640) + (lane & 31) * 8;
    asm volatile("global_load_dwordx2 %0, %5, off\n\tglobal_load_dwordx2 %1, %5, off offset:256\n\tglobal_load_dwordx2 %2, %5, off offset:512\n\t"
                 "global_load_dwordx2 %3, %5, off offset:768\n\tglobal_load_dwordx2 %4, %5, off offset:1024"
                 : "=&v"(xv[0]), "=&v"(xv[1]), "=&v"(xv[2]), "=&v"(xv[3]), "=&v"(xv[4]) : "v"(xp) : "memory");
  }
  unsigned epoch;
  asm volatile("global_load_dword %0, %1, off sc1" : "=v"(epoch) : "v"((const GAS char*)wsp + OFF_EPOCH) : "memory");
  LAS char* lb0 = lds + l16; LAS char* lb1 = lb0 + 65536; LAS char* lb2 = lb0 + 131072;
  asm volatile("" : "+v"(lb1), "+v"(lb2));

  const int r8 = lane >> 3, ch8 = lane & 7;
#define P0B_INDEX() \
  int gv = cuid * 2 + (w & 1); asm volatile("" : "+s"(gv)); \
  const int rg1 = gv / 6, g6 = gv - 6 * rg1, cg1 = g6 < 3 ? 5 * g6 : 15 + 4 * (g6 - 3), n1 = gv < 480 ? (g6 < 3 ? 5 : 4) : 0; \
  const int rgA = gv < 432 ? gv >> 1 : gv - 432, cgA = gv < 432 ? 5 * (gv & 1) : 0, nAi = gv < 432 ? 5 : 10
  unsigned voA = 0, vo1 = 0;
  if (__builtin_expect(w < 2, 1)) {
    const rsrc_t rq = __builtin_amdgcn_make_buffer_rsrc((void*)wqkv, 0, 640 * 1920 * 2, 0x00020000);
    const int col = w == 0 ? (ch8 < 5 ? 640 + 40 * m + 8 * ch8 : 1280 + 40 * m + 8 * (ch8 - 5))
                           : (ch8 < 2 ? 1280 + 40 * m + 24 + 8 * ch8 : 40 * m + 8 * ((ch8 < 7 ? ch8 : 6) - 2));
    const unsigned vo = (unsigned)((40 * P + r8) * 1920 + col) * 2;
#pragma unroll
    for (int i = 0; i < 5; ++i) dma_item(rq, vo + i * 8 * 3840, ldsb + L_A + 16384 + (w * 5 + i) * 1024);
  } else {
    P0B_INDEX();
    (void)n1; (void)nAi;
    {
      const int sq = rgA >> 2, gg = rgA & 3;
      int row;
      if (gv < 432) {
        const int wq = sq < 28 ? sq / 14 : 2 + (sq - 28) / 13, k = sq - (wq < 2 ? 14 * wq : 28 + 13 * (wq - 2));
        int nA, nB;
        if (k < 12) {
          const int tt = 4 * (wq & 1) + k / 3, i = k % 3;
          nA = 54 * (wq >> 1) + (tt < 6 ? 7 * tt : 42 + 6 * (tt - 6)) + 2 * i; nB = nA + 1;
        } else {
          const int ci = (wq < 2 ? 2 * wq : wq + 2) + (k - 12), h2 = ci / 3, a3 = ci - 3 * h2;
          nA = 54 * h2 + 14 * a3 + 6; nB = nA + 7;
        }
        row = 16 * (r8 < 4 ? nA : nB) + 4 * gg + (r8 & 3);
      } else {
        row = sq < 16 ? 40 * sq + 5 * gg + (r8 < 5 ? r8 : 15 + r8) : 40 * (4 * (sq - 16) + (r8 >> 1)) + 23 + 5 * gg + (r8 & 1);
      }
      voA = (unsigned)(row * 640 + 64 * cgA + 8 * ch8) * 2;
    }
    {
      const int ks = rg1 >> 2, gg = rg1 & 3;
      const int nA = ks < 16 ? 5 * (ks >> 1) + 2 * (ks & 1) : 10 * (ks - 16) + 4, nB = ks < 16 ? nA + 1 : nA + 5;
      vo1 = (unsigned)((16 * (r8 < 4 ? nA : nB) + 4 * gg + (r8 & 3)) * 1728 + 64 * cg1 + 8 * ch8) * 2;
    }
  }
  asm volatile("s_cmp_lt_u32 %7, 2\n\ts_cbranch_scc1 1f\n\ts_waitcnt vmcnt(0)\n\ts_branch 2f\n1:\n\ts_waitcnt vmcnt(5)\n2:"
               : "+v"(xv[0]), "+v"(xv[1]), "+v"(xv[2]), "+v"(xv[3]), "+v"(xv[4]), "+v"(epoch), "+v"(warm0) : "s"(w) : "memory", "scc");
  if (__builtin_expect(w >= 2, 0)) {
    P0B_INDEX();
    (void)rg1; (void)cg1; (void)rgA; (void)cgA;
    const unsigned sb = ldsb + L_B + (w & 1) * 15360;
    const rsrc_t rA = __builtin_amdgcn_make_buffer_rsrc((void*)(gv < 432 ? w2 : wo), 0, gv < 432 ? 1728 * 640 * 2 : 640 * 640 * 2, 0x00020000);
    for (int i = 0; i < nAi; ++i) dma_item(rA, voA + i * 128, sb + i * 1024);
    const rsrc_t r1 = __builtin_amdgcn_make_buffer_rsrc((void*)w1, 0, 640 * 1728 * 2, 0x00020000);
    for (int i = 0; i < n1; ++i) dma_item(r1, vo1 + i * 128, sb + (10 + i) * 1024);
  }
  {
    float f[20];
#pragma unroll
    for (int i = 0; i < 5; ++i) { f[4 * i] = blo(xv[i].x); f[4 * i + 1] = bhi(xv[i].x); f[4 * i + 2] = blo(xv[i].y); f[4 * i + 3] = bhi(xv[i].y); }
    float s4[4];
#pragma unroll
    for (int j = 0; j < 4; ++j) s4[j] = (f[j] + f[4 + j]) + (f[8 + j] + f[12 + j]) + f[16 + j];
    const float mean = sum_x16(sum16((s4[0] + s4[1]) + (s4[2] + s4[3]))) * (1.f / 640.f);
#pragma unroll
    for (int i = 0; i < 20; ++i) f[i] -= mean;
#pragma unroll
    for (int j = 0; j < 4; ++j) s4[j] = (f[j] * f[j] + f[4 + j] * f[4 + j]) + (f[8 + j] * f[8 + j] + f[12 + j] * f[12 + j]) + f[16 + j] * f[16 + j];
    const float rstd = __builtin_amdgcn_rsqf(sum_x16(sum16((s4[0] + s4[1]) + (s4[2] + s4[3]))) * (1.f / 640.f) + 1e-5f);
    LAS char* sc = lds + L_A + ((lane & 31) >> 1) * 128 + (2 * w + (lane >> 5)) * 16 + (lane & 1) * 8;
#pragma unroll
    for (int i = 0; i < 5; ++i)
      *(LAS v2u*)(sc + i * 2048) = v2u{pk2(f[4 * i] * rstd, f[4 * i + 1] * rstd), pk2(f[4 * i + 2] * rstd, f[4 * i + 3] * rstd)};
  }
  BAR();
  unsigned tag = 0;
  if (__builtin_expect(w < 2, 1)) {
#pragma unroll
    for (int i = 0; i < 5; ++i) {
      const int L = 320 * w + 64 * i + lane, c = L >> 3, tt = 8 * (m & 1) + (L & 7);
      st128(*(LAS v4u*)(lds + L_A + L * 16), ws, OFF_XN + ((8 * P + (m >> 1)) * 20 + (c >> 2)) * 1024 + ((c & 3) * 16 + tt) * 16, 0);
    }
    asm volatile("s_waitcnt vmcnt(5)" ::: "memory");
    const int pos = 64 * w + lane, nb = pos >> 4, n = pos & 15;
#pragma unroll
    for (int i = 0; i < 5; ++i) {
      const v4u o = item_read(lds + L_A + 16384 + (w * 5 + i) * 1024, lane);
      const int rg = 5 * P + i, ks = rg >> 2, gg = rg & 3;
      if (pos < 120) st128(o, ws, OFF_WQKV + m * WQKV_STR + (nb < 7 ? (nb * 20 + ks) * 1024 + (gg * 16 + n) * 16 : 143360 + ks * 512 + (gg * 8 + n) * 16), 0);
    }
    DRAIN();
  }
  tag = __builtin_amdgcn_readfirstlane(epoch) * 2654435761u + 0x9E3779B9u;
  BAR();
  if (tid == 0) __hip_atomic_store(flg + (0 * 256 + cuid) * 32, tag, RLX);
  if (__builtin_expect(w < 2, 1)) *(LAS v4u*)(lds + L_IMG + tid * 32) = v4u{0u, 0u, 0u, 0u}, *(LAS v4u*)(lds + L_IMG + tid * 32 + 16) = v4u{0u, 0u, 0u, 0u};
  else {
    P0B_INDEX();
    LAS char* im = lds + L_B + (w & 1) * 15360;
    DRAIN();
    const unsigned baseA = (gv < 432 ? OFF_W2 : OFF_WO) + ((rgA >> 2) * 5 * 64 + (rgA & 3) * 16) * 16, strA = gv < 432 ? W2_STR : 102400u;
    for (int i = 0; i < nAi; ++i) {
      const int n = 64 * (cgA + i) + lane, c = n / 80, nl = n - 80 * c;
      st128(item_read(im + i * 1024, lane), ws, baseA + c * strA + (nl >> 4) * 1024 + (nl & 15) * 16, 0);
    }
    const unsigned base1 = OFF_W1 + ((rg1 >> 2) * 7 * 64 + (rg1 & 3) * 16) * 16;
    for (int i = 0; i < n1; ++i) {
      const int n = 64 * (cg1 + i) + lane;
      const int nb = n >> 4, h2 = nb >= 54 ? 1 : 0, nbp = nb - 54 * h2;
      const int tt = nbp < 42 ? nbp / 7 : 6 + (nbp - 42) / 6, nbl = nbp - (tt < 6 ? 7 * tt : 42 + 6 * (tt - 6));
      st128(item_read(im + (10 + i) * 1024, lane), ws, base1 + (8 * h2 + tt) * W1_STR + nbl * 1024 + (n & 15) * 16, 0);
    }
  }

  if (w == 0) {
    const int l = lane & 15;
    wait_flag(flg + (0 * 256 + (lane < 16 ? P * 16 + l : l * 16 + m)) * 32, tag, lane < 32);
  }
  BAR();
  v4u fb;
  v4u xn[2][20];
  f32x4 akv[2][5];
  v4u wqs[13];
  {
    constexpr int D1 = 5;
    for (int q = w; q < 25; q += 4) dma4(ws, l16, OFF_WQKV + m * WQKV_STR + q * 4096, ldsb + L_A + q * 4096);
    const unsigned xb = OFF_XN + (8 * P + 2 * w) * 20 * 1024;
#pragma unroll
    for (int ks = 0; ks < D1; ++ks) {
#pragma unroll
      for (int u = 0; u < 2; ++u) xn[u][ks] = LDF(xb, u * 20 + ks);
      SCHED();
    }
    asm volatile("s_waitcnt vmcnt(10)" ::: "memory");
    BAR();
    if (tid == 0) __hip_atomic_store((gu32*)(wsp + OFF_FLAGB) + cuid, tag, RLX);
#pragma unroll
    for (int u = 0; u < 2; ++u)
#pragma unroll
      for (int n = 0; n < 5; ++n) akv[u][n] = f32x4{0.f, 0.f, 0.f, 0.f};
    v4u wf[2][5];
#pragma unroll
    for (int n = 0; n < 5; ++n) wf[0][n] = LDSF(L_A + (n * 20) * 1024);
    SCHED();
    const unsigned wqb = OFF_WQKV + m * WQKV_STR + 102400 + w * 1024;
#pragma unroll
    for (int ks = 0; ks < 20; ++ks) {
      if (ks + D1 < 20) {
#pragma unroll
        for (int u = 0; u < 2; ++u) xn[u][ks + D1] = LDF(xb, u * 20 + ks + D1);
      }
      if (ks >= 4 && ks < 17) wqs[ks - 4] = ld128(ws, l16, wqb + (ks - 4 < 12 || w < 2 ? ks - 4 : 11) * 4096);
      if (ks == 17) fb = __builtin_amdgcn_raw_buffer_load_b128(ws, OFF_FLAGB + l16, 0, AUX_SC1);
      if (ks + 1 < 20) {
#pragma unroll
        for (int n = 0; n < 5; ++n) wf[(ks + 1) & 1][n] = LDSF(L_A + (n * 20 + ks + 1) * 1024);
      }
#pragma unroll
      for (int n = 0; n < 5; ++n)
#pragma unroll
        for (int u = 0; u < 2; ++u) akv[u][n] = mfma(xn[u][ks], wf[ks & 1][n], akv[u][n]);
#pragma unroll
      for (int n = 0; n < 5; ++n) { SGB(0x100, 1); SGB(0x8, 1); if (n < 3) SGB(0x20, 1); SGB(0x8, 1); }
      SCHED();
    }
  }
  if (!__all(fb.x == tag && fb.y == tag && fb.z == tag && fb.w == tag)) {
    unsigned sp = 0;
    for (;;) {
      asm volatile("" ::: "memory");
      fb = __builtin_amdgcn_raw_buffer_load_b128(ws, OFF_FLAGB + l16, 0, AUX_SC1);
      if (__all(fb.x == tag && fb.y == tag && fb.z == tag && fb.w == tag)) break;
      if (++sp > SPIN_CAP) break;
    }
  }
  v4u w2f[14][5];
  const int k4s = w < 2 ? 14 * w : 28 + 13 * (w - 2);
  const unsigned w2b = OFF_W2 + ct * W2_STR + k4s * 5 * 1024;
#define W2LD(f0, f1) _Pragma("unroll") for (int f_ = (f0); f_ < (f1); ++f_) w2f[f_ / 5][f_ % 5] = LDF(w2b, f_); SCHED()
  W2LD(0, 12);
  BAR();
#pragma unroll
  for (int u = 0; u < 2; ++u)
#pragma unroll
    for (int n = 0; n < 5; ++n) {
      f32x4 a = akv[u][n];
      if (n < 2) { a[0] = phi(a[0]); a[1] = phi(a[1]); a[2] = phi(a[2]); a[3] = phi(a[3]); }
      else if (n == 2) { if (t16 < 8) { a[0] = phi(a[0]); a[1] = phi(a[1]); a[2] = phi(a[2]); a[3] = phi(a[3]); } }
      *(LAS v2u*)(lds + L_A + (16 * n + t16) * EP + (32 * w + 16 * u + 4 * g) * 2) = v2u{pk2(a[0], a[1]), pk2(a[2], a[3])};
    }
  if (tid < 16) *(LAS v4u*)(lds + L_A + 80 * EP + tid * 16) = v4u{0x3F803F80u, 0x3F803F80u, 0x3F803F80u, 0x3F803F80u};
  BAR();
  W2LD(12, 24);
  {
    const int hd = w >> 1, dt = w & 1;
    f32x4 t0 = {0.f, 0.f, 0.f, 0.f}, t1 = t0;
    const unsigned ra = L_A + (20 * hd + 16 * dt + t16) * EP + g * 16;
    const unsigned rb0 = L_A + (40 + 20 * hd + t16) * EP + g * 16;
    const unsigned rb1 = L_A + (t16 < 4 ? 56 + 20 * hd + t16 : 80) * EP + g * 16;
    v4u fa[4], f0[4], f1[4];
#pragma unroll
    for (int s = 0; s < 4; ++s) { fa[s] = *(LAS v4u*)(lds + ra + s * 64); f0[s] = *(LAS v4u*)(lds + rb0 + s * 64); f1[s] = *(LAS v4u*)(lds + rb1 + s * 64); }
#pragma unroll
    for (int s = 0; s < 4; ++s) { t0 = mfma(fa[s], f0[s], t0); t1 = mfma(fa[s], f1[s], t1); }
    const unsigned pt = L_PT + hd * 1680;
    int dv = dt; asm volatile("" : "+v"(dv));
    const bool act = g < (dv ? 1 : 4);
    if (act) *(LAS f32x4*)(lds + pt + (dv ? 1344 + t16 * 16 : l16)) = t0;
    if (act && t16 < 5) *(LAS f32x4*)(lds + pt + (dv ? 1600 + t16 * 16 : 1024 + (g * 5 + t16) * 16)) = t1;
    if (tid >= 250) *(LAS v4u*)(lds + L_PT + 3360 + (tid - 250) * 16) = v4u{0u, 0u, 0u, 0u};
  }
  BAR();
  if (tid < 216) st128(*(LAS v4u*)(lds + L_PT + tid * 16), ws, tid * 16, OFF_PART + (m * 16 + P) * PART_STR);
#pragma unroll
  for (int i = 0; i < 13; ++i)
    if (i < 12 || w < 2) *(LAS v4u*)(lds + L_B + (w + 4 * i) * 1024 + l16) = wqs[i];
  PUBLISH(1);
  BAR();

  unsigned qp[2][3][2];
  {
    f32x4 aq[2][3];
#pragma unroll
    for (int u = 0; u < 2; ++u)
#pragma unroll
      for (int n = 0; n < 3; ++n) aq[u][n] = f32x4{0.f, 0.f, 0.f, 0.f};
    LAS char* lq7 = lds + L_B + 40960 + g * 128 + (t16 & 7) * 16;
    const unsigned wob = OFF_WO + ct * 102400 + w * 1024, wod = ldsb + L_A + w * 1024;
    v4u wf[3][3];
#pragma unroll
    for (int k = 0; k < 2; ++k) { wf[k][0] = LDSF(L_B + k * 1024); wf[k][1] = LDSF(L_B + (20 + k) * 1024); wf[k][2] = *(LAS v4u*)(lq7 + k * 512); }
    SCHED();
#pragma unroll
    for (int ks = 0; ks < 20; ++ks) {
      if (ks >= 4) dma1q(ws, l16, wob + (ks - 4) * 4096, wod + (ks - 4) * 4096);
      if (ks >= 11) dma1q(ws, l16, wob + (ks + 5) * 4096, wod + (ks + 5) * 4096);
      if (ks + 2 < 20) {
        wf[(ks + 2) % 3][0] = LDSF(L_B + (ks + 2) * 1024);
        wf[(ks + 2) % 3][1] = LDSF(L_B + (20 + ks + 2) * 1024);
        wf[(ks + 2) % 3][2] = *(LAS v4u*)(lq7 + (ks + 2) * 512);
      }
#pragma unroll
      for (int n = 0; n < 3; ++n)
#pragma unroll
        for (int u = 0; u < 2; ++u) aq[u][n] = mfma(wf[ks % 3][n], xn[u][ks], aq[u][n]);
#pragma unroll
      for (int n = 0; n < 3; ++n) { SGB(0x100, 1); SGB(0x8, 2); }
      SCHED();
    }
#pragma unroll
    for (int u = 0; u < 2; ++u)
#pragma unroll
      for (int n = 0; n < 3; ++n) {
        qp[u][n][0] = pk2(phi(aq[u][n][0]), phi(aq[u][n][1]));
        qp[u][n][1] = pk2(phi(aq[u][n][2]), phi(aq[u][n][3]));
      }
  }
  BAR();
  unsigned imgoff, imgoffz;
  {
    const int hd = tid >= 105 ? 1 : 0, u = tid - 105 * hd;
    int dq, e;
    if (u < 64) { dq = u >> 4; e = u & 15; }
    else if (u < 84) { const int v = u - 64; dq = v / 5; e = 16 + v - 5 * dq; }
    else if (u < 100) { dq = 4; e = u - 84; }
    else { dq = 4; e = 16 + u - 100; }
    int gq;
    if (hd == 0) gq = dq < 4 ? dq : 0; else gq = dq < 3 ? dq + 1 : (dq == 3 ? 0 : 1);
    const int j0 = dq < 4 ? 0 : 4, ge = e / 5, re = e - 5 * ge;
    const unsigned ib = L_IMG + hd * 2048 + gq * 256 + j0 * 2;
    imgoff = ib + (re < 4 ? (4 * ge + re) * 16 : 1024 + (4 * ge) * 16);
    imgoffz = e == 20 ? ib + 1024 + 16 : 0u;
  }

  wait_flag(flg + (1 * 256 + (lane >> 2) * 16 + m) * 32 + (lane & 3), tag, true);
  v4u pv[16];
  {
    const unsigned vo = (tid < 216 ? tid : 215) * 16, pb = OFF_PART + m * 16 * PART_STR;
#define PV8(o) asm volatile("buffer_load_dwordx4 %0, %8, %9, %10 offen sc1\n\tbuffer_load_dwordx4 %1, %8, %9, %11 offen sc1\n\t" \
      "buffer_load_dwordx4 %2, %8, %9, %12 offen sc1\n\tbuffer_load_dwordx4 %3, %8, %9, %13 offen sc1\n\t" \
      "buffer_load_dwordx4 %4, %8, %9, %14 offen sc1\n\tbuffer_load_dwordx4 %5, %8, %9, %15 offen sc1\n\t" \
      "buffer_load_dwordx4 %6, %8, %9, %16 offen sc1\n\tbuffer_load_dwordx4 %7, %8, %9, %17 offen sc1" \
      : "=&v"(pv[o]), "=&v"(pv[o + 1]), "=&v"(pv[o + 2]), "=&v"(pv[o + 3]), "=&v"(pv[o + 4]), "=&v"(pv[o + 5]), "=&v"(pv[o + 6]), "=&v"(pv[o + 7]) \
      : "v"(vo), "s"(ws), "s"(pb + (o) * PART_STR), "s"(pb + (o + 1) * PART_STR), "s"(pb + (o + 2) * PART_STR), "s"(pb + (o + 3) * PART_STR), \
        "s"(pb + (o + 4) * PART_STR), "s"(pb + (o + 5) * PART_STR), "s"(pb + (o + 6) * PART_STR), "s"(pb + (o + 7) * PART_STR) : "memory")
    PV8(0); PV8(8);
    for (int q = w; q < 12; q += 4) dma4(ws, l16, OFF_W1 + m * W1_STR + q * 4096, ldsb + L_B + q * 4096);
    if (w == 0) dma1(ws, l16, OFF_W1 + m * W1_STR + 49152, ldsb + L_B + 49152);
    asm volatile("s_waitcnt vmcnt(12)"
                 : "+v"(pv[0]), "+v"(pv[1]), "+v"(pv[2]), "+v"(pv[3]), "+v"(pv[4]), "+v"(pv[5]), "+v"(pv[6]), "+v"(pv[7]),
                   "+v"(pv[8]), "+v"(pv[9]), "+v"(pv[10]), "+v"(pv[11]), "+v"(pv[12]), "+v"(pv[13]), "+v"(pv[14]), "+v"(pv[15]) :: "memory");
  }
  if (tid < 210) {
    f32x4 s[4];
#pragma unroll
    for (int i = 0; i < 4; ++i)
      s[i] = (__builtin_bit_cast(f32x4, pv[4 * i]) + __builtin_bit_cast(f32x4, pv[4 * i + 1])) +
             (__builtin_bit_cast(f32x4, pv[4 * i + 2]) + __builtin_bit_cast(f32x4, pv[4 * i + 3]));
    const f32x4 st = (s[0] + s[1]) + (s[2] + s[3]);
    const v2u val = v2u{pk2(st[0], st[1]), pk2(st[2], st[3])};
    if (imgoffz == 0u) *(LAS v2u*)(lds + imgoff) = val;
    else {
#pragma unroll
      for (int ge = 0; ge < 4; ++ge) *(LAS v2u*)(lds + imgoffz + ge * 64) = val;
    }
  }
  BAR();
  {
    v4u ka[2][2];
#pragma unroll
    for (int hd = 0; hd < 2; ++hd)
#pragma unroll
      for (int bb = 0; bb < 2; ++bb) ka[hd][bb] = *(LAS v4u*)(lds + L_IMG + hd * 2048 + bb * 1024 + l16);
#pragma unroll
    for (int u = 0; u < 2; ++u) {
      v4u qa, qb;
      qa.x = qp[u][0][0]; qa.y = qp[u][0][1]; qa.z = g == 0 ? qp[u][1][0] : 0u; qa.w = g == 0 ? qp[u][1][1] : 0u;
      qb.x = g == 0 ? qp[u][2][0] : qp[u][1][0]; qb.y = g == 0 ? qp[u][2][1] : qp[u][1][1];
      qb.z = g == 1 ? qp[u][2][0] : 0u; qb.w = g == 1 ? qp[u][2][1] : 0u;
      const f32x4 z4 = {0.f, 0.f, 0.f, 0.f};
      const f32x4 a0 = mfma(ka[0][0], qa, z4), a1 = mfma(ka[0][1], qa, z4);
      const f32x4 b0 = mfma(ka[1][0], qb, z4), b1 = mfma(ka[1][1], qb, z4);
      const float ra = __builtin_amdgcn_rcpf(a1[1] + 1e-6f), rb = __builtin_amdgcn_rcpf(b1[1] + 1e-6f);
      const unsigned d0 = pk2(a0[0] * ra, a0[1] * ra), d1 = pk2(a0[2] * ra, a0[3] * ra), d2 = pk2(a1[0] * ra, b0[0] * rb),
                     d3 = pk2(b0[1] * rb, b0[2] * rb), d4 = pk2(b0[3] * rb, b1[0] * rb);
      const unsigned oi = (8 * P + 2 * w + u) * 16 + m;
      st128(v4u{d0, d1, d2, d3}, ws, l16, OFF_OM + oi * 1024);
      __builtin_amdgcn_raw_buffer_store_b32(d4, ws, lane * 4, OFF_OR + oi * 256, AUX_SC1);
    }
  }
  PUBLISH(2);
  BAR();
  v2u xr[5];
  {
    const GAS char* xp = (const GAS char*)x + (size_t)(64 * (2 * P + jl) + 16 * w + t16) * 1280 + (80 * ct + 4 * g) * 2;
#pragma unroll
    for (int n = 0; n < 5; ++n) xr[n] = *(const GAS v2u*)(xp + n * 32);
  }
  v4u w1r[23];
  const unsigned w1rb = OFF_W1 + m * W1_STR + 50176 + w * 1024;
#pragma unroll
  for (int i = 0; i < 12; ++i) w1r[i] = ld128(ws, l16, w1rb + i * 4096);
  SCHED();

  f32x4 cs7[7];
  const v4u ones = {0x3F803F80u, 0x3F803F80u, 0x3F803F80u, 0x3F803F80u};
  {
    v4u wv[13];
#pragma unroll
    for (int i = 0; i < 13; ++i) wv[i] = *(LAS v4u*)(lb0 + L_B + (w + 4 * (i < 12 || w == 0 ? i : 11)) * 1024);
#pragma unroll
    for (int j = 0; j < 7; ++j) cs7[j] = mfma(wv[j], ones, f32x4{0.f, 0.f, 0.f, 0.f});
#pragma unroll
    for (int i = 7; i < 12; ++i) cs7[i - 7] = mfma(wv[i], ones, cs7[i - 7]);
    if (w == 0) cs7[5] = mfma(wv[12], ones, cs7[5]);
  }
  wait_flag(flg + (2 * 256 + P * 16 + (lane & 15)) * 32 + ((4 * jl + w) >> 1), tag, lane < 16);
  unsigned hp[5][2];
  {
    constexpr int D2 = 8;
    v4u of[20];
    const unsigned ob = OFF_OM + (8 * P + 4 * jl + w) * 16 * 1024, orb = OFF_OR + (8 * P + 4 * jl + w) * 16 * 256;
#define LDO(ks) ((ks) < 16 ? LDF(ob, ks) : v4u{ld32(ws, lane * 4 + (4 * ((ks) - 16)) * 256, orb), ld32(ws, lane * 4 + (4 * ((ks) - 16) + 1) * 256, orb), \
                                              ld32(ws, lane * 4 + (4 * ((ks) - 16) + 2) * 256, orb), ld32(ws, lane * 4 + (4 * ((ks) - 16) + 3) * 256, orb)})
#pragma unroll
    for (int ks = 0; ks < D2; ++ks) { of[ks] = LDO(ks); SCHED(); }
    f32x4 ah[5];
#pragma unroll
    for (int n = 0; n < 5; ++n) ah[n] = f32x4{0.f, 0.f, 0.f, 0.f};
    v4u wf[3][5];
#pragma unroll
    for (int n = 0; n < 5; ++n) { wf[0][n] = LDSF(L_A + n * 1024); wf[1][n] = LDSF(L_A + (5 + n) * 1024); }
    SCHED();
#pragma unroll
    for (int ks = 0; ks < 20; ++ks) {
      if (ks + D2 < 20) of[ks + D2] = LDO(ks + D2);
      else {
        const int i0 = ks < 15 ? 12 + 2 * (ks - 12) : 15 + (ks - 12);
        w1r[i0] = ld128(ws, l16, w1rb + (i0 < 22 || w < 3 ? i0 : 21) * 4096);
        if (ks < 15) w1r[i0 + 1] = ld128(ws, l16, w1rb + (i0 + 1) * 4096);
      }
      if (ks + 2 < 20) {
#pragma unroll
        for (int n = 0; n < 5; ++n) wf[(ks + 2) % 3][n] = LDSF(L_A + ((ks + 2) * 5 + n) * 1024);
      }
#pragma unroll
      for (int n = 0; n < 5; ++n) ah[n] = mfma(wf[ks % 3][n], of[ks], ah[n]);
#pragma unroll
      for (int n = 0; n < 5; ++n) { SGB(0x100, 1); SGB(0x8, 1); if (n < 4) SGB(0x20, 1); }
      SCHED();
    }
    float hv[20];
#pragma unroll
    for (int n = 0; n < 5; ++n) {
      hp[n][0] = pk2(blo(xr[n].x) + ah[n][0], bhi(xr[n].x) + ah[n][1]);
      hp[n][1] = pk2(blo(xr[n].y) + ah[n][2], bhi(xr[n].y) + ah[n][3]);
    }
    const unsigned hi = (8 * P + 4 * jl + w) * 8 + ct;
    st128(v4u{hp[0][0], hp[0][1], hp[1][0], hp[1][1]}, ws, l16, OFF_HF + hi * 2048);
    st128(v4u{hp[2][0], hp[2][1], hp[3][0], hp[3][1]}, ws, l16 + 1024, OFF_HF + hi * 2048);
    st64(v2u{hp[4][0], hp[4][1]}, ws, lane * 8, OFF_HH + hi * 512);
    float s4[5];
#pragma unroll
    for (int n = 0; n < 5; ++n) {
      hv[4 * n] = blo(hp[n][0]); hv[4 * n + 1] = bhi(hp[n][0]); hv[4 * n + 2] = blo(hp[n][1]); hv[4 * n + 3] = bhi(hp[n][1]);
      s4[n] = (hv[4 * n] + hv[4 * n + 1]) + (hv[4 * n + 2] + hv[4 * n + 3]);
    }
    const float mi = sum_g((s4[0] + s4[1]) + (s4[2] + s4[3]) + s4[4]) * (1.f / 80.f);
#pragma unroll
    for (int n = 0; n < 5; ++n) {
      const float a = hv[4 * n] - mi, bq = hv[4 * n + 1] - mi, c = hv[4 * n + 2] - mi, d = hv[4 * n + 3] - mi;
      s4[n] = (a * a + bq * bq) + (c * c + d * d);
    }
    const float q = sum_g((s4[0] + s4[1]) + (s4[2] + s4[3]) + s4[4]);
    if (g == 0) st64(v2u{asu(mi), asu(q)}, ws, t16 * 8, OFF_ST + ((8 * P + 4 * jl + w) * 8 + ct) * 128);
  }
  PUBLISH(3);
  BAR();
#pragma unroll
  for (int i = 0; i < 23; ++i)
    if (i < 22 || w < 3) {
      *(LAS v4u*)(lds + L_A + (w + 4 * i) * 1024 + l16) = w1r[i];
      cs7[i % 7] = mfma(w1r[i], ones, cs7[i % 7]);
    }
  if (t16 == 0) {
#pragma unroll
    for (int j = 0; j < 7; ++j) *(LAS f32x4*)(lds + L_CS + (w * 7 + (w + 4 * j) % 7) * 64 + g * 16) = cs7[j];
  }
  W2LD(24, 36);

  BAR();
  {
    const int i = (lane & 15) >> 1;
    wait_flag(flg + (3 * 256 + P * 16 + 8 * (i >> 2) + 4 * (w >> 1) + (i & 3)) * 32 + ((2 * w + (lane & 1)) & 3), tag, lane < 16);
  }
  {
    constexpr int D3 = 5;
    v4u hf[2][20];
    const unsigned hb = OFF_HF + (8 * P + 2 * w) * 16 * 1024, hhb = OFF_HH + (8 * P + 2 * w) * 8 * 512;
    auto ldh = [&](int u, int ks) -> v4u {
      if (ks < 16) return LDF(hb, u * 16 + ks);
      const v2u a = ld64(ws, lane * 8 + (2 * (ks - 16)) * 512, hhb + u * 4096), c = ld64(ws, lane * 8 + (2 * (ks - 16) + 1) * 512, hhb + u * 4096);
      return v4u{a.x, a.y, c.x, c.y};
    };
    v2u stv[2][2];
#pragma unroll
    for (int ks = 0; ks < D3; ++ks) {
#pragma unroll
      for (int u = 0; u < 2; ++u) hf[u][ks] = ldh(u, ks);
      SCHED();
    }
#pragma unroll
    for (int u = 0; u < 2; ++u)
#pragma unroll
      for (int i = 0; i < 2; ++i) stv[u][i] = ld64(ws, t16 * 8 + (2 * g + i) * 128, OFF_ST + (8 * P + 2 * w + u) * 1024);
    f32x4 cs[7];
#pragma unroll
    for (int n = 0; n < 7; ++n) {
      f32x4 cw[4];
#pragma unroll
      for (int ww = 0; ww < 4; ++ww) cw[ww] = *(LAS f32x4*)(lds + L_CS + (ww * 7 + n) * 64 + g * 16);
      cs[n] = (cw[0] + cw[1]) + (cw[2] + cw[3]);
    }
    f32x4 a3[2][7];
#pragma unroll
    for (int u = 0; u < 2; ++u)
#pragma unroll
      for (int n = 0; n < 7; ++n) a3[u][n] = f32x4{0.f, 0.f, 0.f, 0.f};
    v4u wf[2][7];
#pragma unroll
    for (int n = 0; n < 7; ++n) wf[0][n] = LDSF(W1A(0, n));
    SCHED();
#pragma unroll
    for (int ks = 0; ks < 20; ++ks) {
      if (ks + D3 < 20) {
#pragma unroll
        for (int u = 0; u < 2; ++u) hf[u][ks + D3] = ldh(u, ks + D3);
      }
      w2f[(36 + ks) / 5][(36 + ks) % 5] = LDF(w2b, 36 + ks);
      if (ks + 1 < 20) {
#pragma unroll
        for (int n = 0; n < 7; ++n) wf[(ks + 1) & 1][n] = LDSF(W1A(ks + 1, n));
      }
#pragma unroll
      for (int n = 0; n < 7; ++n)
#pragma unroll
        for (int u = 0; u < 2; ++u) a3[u][n] = mfma(wf[ks & 1][n], hf[u][ks], a3[u][n]);
#pragma unroll
      for (int n = 0; n < 7; ++n) { SGB(0x100, 1); SGB(0x8, 1); if (n < 5) SGB(0x20, 1); SGB(0x8, 1); }
      SCHED();
    }
    float mean[2], rstd[2];
#pragma unroll
    for (int u = 0; u < 2; ++u) {
      const float m0 = asf(stv[u][0].x), q0 = asf(stv[u][0].y), m1 = asf(stv[u][1].x), q1 = asf(stv[u][1].y);
      const float mu = sum_g(m0 + m1) * 0.125f;
      const float M2 = sum_g(q0 + q1 + 80.f * ((m0 - mu) * (m0 - mu) + (m1 - mu) * (m1 - mu)));
      rstd[u] = __builtin_amdgcn_rsqf(M2 * (1.f / 640.f) + 1e-5f);
      mean[u] = mu * rstd[u];
    }
    unsigned hq[2][7][2];
#pragma unroll
    for (int n = 0; n < 7; ++n) {
#pragma unroll
      for (int u = 0; u < 2; ++u) {
        const f32x2 r2 = {rstd[u], rstd[u]}, m2 = {-mean[u], -mean[u]};
        const f32x2 lo = __builtin_elementwise_fma(f32x2{a3[u][n][0], a3[u][n][1]}, r2, f32x2{cs[n][0], cs[n][1]} * m2);
        const f32x2 hi2 = __builtin_elementwise_fma(f32x2{a3[u][n][2], a3[u][n][3]}, r2, f32x2{cs[n][2], cs[n][3]} * m2);
        const float v0 = fmaxf(lo[0], 0.f), v1 = fmaxf(lo[1], 0.f), v2 = fmaxf(hi2[0], 0.f), v3 = fmaxf(hi2[1], 0.f);
        hq[u][n][0] = pk2(v0, v1); hq[u][n][1] = pk2(v2, v3);
      }
    }
#pragma unroll
    for (int u = 0; u < 2; ++u) {
      const unsigned di = (8 * P + 2 * w + u) * 16 + m;
      st128(v4u{hq[u][0][0], hq[u][0][1], hq[u][1][0], hq[u][1][1]}, ws, l16, OFF_DF + di * 3072);
      st128(v4u{hq[u][2][0], hq[u][2][1], hq[u][3][0], hq[u][3][1]}, ws, l16 + 1024, OFF_DF + di * 3072);
      st128(v4u{hq[u][4][0], hq[u][4][1], hq[u][5][0], hq[u][5][1]}, ws, l16 + 2048, OFF_DF + di * 3072);
      if ((m & 7) < 6) st64(v2u{hq[u][6][0], hq[u][6][1]}, ws, lane * 8, OFF_DH + di * 512);
    }
  }
  PUBLISH(4);
  if (tid == 0 && cuid == 0) __hip_atomic_store((gu32*)(wsp + OFF_EPOCH), epoch + 1u, RLX);
  W2LD(56, 70);
  BAR();
  {
    const int l = lane & 15, j = (l - 8) >> 2, ci = (w < 2 ? 2 * w : w + 2) + (j > 0 && w < 2 ? 1 : 0);
    const int tile = l < 8 ? 4 * w + (l >> 1) : 8 * (ci >= 3 ? 1 : 0) + 2 * (ci >= 3 ? ci - 3 : ci) + ((l >> 1) & 1);
    wait_flag(flg + (4 * 256 + P * 16 + tile) * 32 + 2 * jl + (l & 1), tag, lane < 16);
  }
  {
    f32x4 a4[4][5];
#pragma unroll
    for (int u = 0; u < 4; ++u)
#pragma unroll
      for (int n = 0; n < 5; ++n) a4[u][n] = f32x4{0.f, 0.f, 0.f, 0.f};
    const unsigned hb = OFF_DF + ((8 * P + 4 * jl) * 48 + 12 * w) * 1024, dhb = OFF_DH + (8 * P + 4 * jl) * 16 * 512;
    const int ci0 = w < 2 ? 2 * w : w + 2;
    auto lda = [&](int u, int k) -> v4u {
      if (k < 12) return ld128(ws, l16 + (k & 3) * 1024, hb + u * 48 * 1024 + (k >> 2) * 4096);
      const int ci = ci0 + (k - 12), h2 = ci >= 3 ? 1 : 0, tA = 8 * h2 + 2 * (ci - 3 * h2);
      const v2u a = ld64(ws, lane * 8, dhb + (u * 16 + tA) * 512), c = ld64(ws, lane * 8 + 512, dhb + (u * 16 + tA) * 512);
      return v4u{a.x, a.y, c.x, c.y};
    };
    constexpr int DP = 3;
    v4u af[DP + 1][4];
#pragma unroll
    for (int k = 0; k < DP; ++k)
#pragma unroll
      for (int u = 0; u < 4; ++u) { af[k][u] = lda(u, k); SCHED(); }
#pragma unroll
    for (int k = 0; k < 14; ++k) {
      if (k + DP < 14) {
        if (k + DP < 13 || w < 2) {
#pragma unroll
          for (int u = 0; u < 4; ++u) af[(k + DP) % (DP + 1)][u] = lda(u, k + DP);
        }
      }
      if (k < 13 || w < 2) {
#pragma unroll
        for (int u = 0; u < 4; ++u)
#pragma unroll
          for (int n = 0; n < 5; ++n) a4[u][n] = mfma(w2f[k][n], af[k % (DP + 1)][u], a4[u][n]);
      }
#pragma unroll
      for (int u = 0; u < 4; ++u) { SGB(0x20, 2); SGB(0x8, 5); }
      SCHED();
    }
#pragma unroll
    for (int u = 0; u < 4; ++u) {
      if (u != w) {
        const int si = w < u ? w : w - 1;
#pragma unroll
        for (int n = 0; n < 5; ++n) *(LAS f32x4*)(lds + L_A + ((u * 3 + si) * 5 + n) * 1024 + l16) = a4[u][n];
      }
    }
    BAR();
    GAS char* op = (GAS char*)out + (size_t)(64 * (2 * P + jl) + 16 * w + t16) * 1280 + (80 * ct + 4 * g) * 2;
#pragma unroll
    for (int n = 0; n < 5; ++n) {
      f32x4 v = w == 0 ? a4[0][n] : (w == 1 ? a4[1][n] : (w == 2 ? a4[2][n] : a4[3][n]));
      f32x4 p[3];
#pragma unroll
      for (int si = 0; si < 3; ++si) p[si] = *(LAS f32x4*)(lds + L_A + ((w * 3 + si) * 5 + n) * 1024 + l16);
      v = (v + p[0]) + (p[1] + p[2]);
      const v2u o = {pk2(blo(hp[n][0]) + v[0], bhi(hp[n][0]) + v[1]), pk2(blo(hp[n][1]) + v[2], bhi(hp[n][1]) + v[3])};
      *(GAS v2u*)(op + n * 32) = o;
    }
  }
}

extern "C" void kernel_launch(void* const* d_in, const int* in_sizes, int n_in, void* d_out, int out_size, void* d_ws, size_t ws_size,
                              hipStream_t stream) {
  (void)in_sizes; (void)n_in; (void)out_size; (void)ws_size;
  hipFuncSetAttribute((const void*)fused_block, hipFuncAttributeMaxDynamicSharedMemorySize, LDS_BYTES);
  fused_block<<<256, 256, LDS_BYTES, stream>>>((const unsigned short*)d_in[0], (const unsigned short*)d_in[1], (const unsigned short*)d_in[2],
                                               (const unsigned short*)d_in[4], (const unsigned short*)d_in[5], (unsigned short*)d_out,
                                               (char*)d_ws);
}
```

```cpp
#include <hip/hip_runtime.h>
#include <hip/hip_bf16.h>
#include <stdint.h>

typedef unsigned v4u __attribute__((ext_vector_type(4)));
typedef unsigned v3u __attribute__((ext_vector_type(3)));
typedef unsigned v2u __attribute__((ext_vector_type(2)));
typedef float f32x4 __attribute__((ext_vector_type(4)));
typedef float f32x2 __attribute__((ext_vector_type(2)));
typedef short bf16x8 __attribute__((ext_vector_type(8)));
typedef short s16x4 __attribute__((ext_vector_type(4)));
typedef __bf16 bf2 __attribute__((ext_vector_type(2)));
typedef __amdgpu_buffer_rsrc_t rsrc_t;
#define GAS __attribute__((address_space(1)))
#define LAS __attribute__((address_space(3)))
#define DI __device__ __forceinline__
#define RLX __ATOMIC_RELAXED, __HIP_MEMORY_SCOPE_AGENT
typedef GAS unsigned gu32;

constexpr int CODE_LINES = 294;

constexpr unsigned OFF_EPOCH = 0;
constexpr unsigned OFF_FLAG = 4096;
constexpr unsigned OFF_FLAGB = OFF_FLAG + 5 * 256 * 128;
constexpr unsigned OFF_XN = 256 * 1024;
constexpr unsigned WQKV_STR = 153600;
constexpr unsigned OFF_WQKV = OFF_XN + 128 * 20 * 1024;
constexpr unsigned OFF_WO = OFF_WQKV + 16 * WQKV_STR;
constexpr unsigned W1_STR = 20 * 7 * 1024;
constexpr unsigned OFF_W1 = OFF_WO + 8 * 102400;
constexpr unsigned W2_STR = 54 * 5 * 1024;
constexpr unsigned OFF_W2 = OFF_W1 + 16 * W1_STR;
constexpr unsigned PART_STR = 3456;
constexpr unsigned OFF_PART = OFF_W2 + 8 * W2_STR;
constexpr unsigned OFF_OM = OFF_PART + 256 * PART_STR;
constexpr unsigned OFF_OR = OFF_OM + 128 * 16 * 1024;
constexpr unsigned OFF_HF = OFF_OR + 128 * 16 * 256;
constexpr unsigned OFF_HH = OFF_HF + 128 * 16 * 1024;
constexpr unsigned OFF_ST = OFF_HH + 128 * 8 * 512;
constexpr unsigned OFF_DF = OFF_ST + 128 * 8 * 128;
constexpr unsigned OFF_DH = OFF_DF + 128 * 48 * 1024;
constexpr unsigned WS_BYTES = OFF_DH + 128 * 16 * 512;

constexpr unsigned L_A = 0;
constexpr unsigned L_B = 102400;
constexpr unsigned L_M = 153600;
constexpr unsigned L_IMG = L_M;
constexpr unsigned L_CS = L_M + 4096;
constexpr unsigned L_PT = L_M + 6144;
constexpr unsigned LDS_BYTES = 163840;
constexpr unsigned EP = 272;

constexpr int AUX_SC1 = 16;
constexpr unsigned SPIN_CAP = 1u << 20;

DI f32x4 mfma(v4u a, v4u b, f32x4 c) {
  return __builtin_amdgcn_mfma_f32_16x16x32_bf16(__builtin_bit_cast(bf16x8, a), __builtin_bit_cast(bf16x8, b), c, 0, 0, 0);
}
DI unsigned pk2(float a, float b) { bf2 r = {(__bf16)a, (__bf16)b}; return __builtin_bit_cast(unsigned, r); }
DI float asf(unsigned u) { return __builtin_bit_cast(float, u); }
DI unsigned asu(float f) { return __builtin_bit_cast(unsigned, f); }
DI float blo(unsigned u) { return asf(u << 16); }
DI float bhi(unsigned u) { return asf(u & 0xffff0000u); }
DI float phi(float t) { return (t > 0.f ? t + 1.f : __expf(t)) + 1e-6f; }
template <int C> DI float dpp_add(float v) { return v + asf((unsigned)__builtin_amdgcn_update_dpp(0, (int)asu(v), C, 0xF, 0xF, true)); }
DI float sum16(float v) { v = dpp_add<0xB1>(v); v = dpp_add<0x4E>(v); v = dpp_add<0x141>(v); v = dpp_add<0x140>(v); return v; }
DI float sum_x16(float v) { const unsigned u = asu(v); auto r = __builtin_amdgcn_permlane16_swap(u, u, false, false); return asf(r[0]) + asf(r[1]); }
DI float sum_x32(float v) { const unsigned u = asu(v); auto r = __builtin_amdgcn_permlane32_swap(u, u, false, false); return asf(r[0]) + asf(r[1]); }
DI float sum_g(float v) { return sum_x32(sum_x16(v)); }
#define SCHED() __builtin_amdgcn_sched_barrier(0)
#define SGB(mask, n) __builtin_amdgcn_sched_group_barrier(mask, n, 0)
#define DRAIN() asm volatile("s_waitcnt vmcnt(0)" ::: "memory")
#define BAR() do { asm volatile("s_waitcnt lgkmcnt(0)" ::: "memory"); __builtin_amdgcn_s_barrier(); asm volatile("" ::: "memory"); } while (0)

DI v4u ld128(rsrc_t rs, unsigned voff, unsigned soff) { return __builtin_amdgcn_raw_buffer_load_b128(rs, voff, soff, AUX_SC1); }
DI void st128(v4u v, rsrc_t rs, unsigned voff, unsigned soff) { __builtin_amdgcn_raw_buffer_store_b128(v, rs, voff, soff, AUX_SC1); }
DI v2u ld64(rsrc_t rs, unsigned voff, unsigned soff) { return __builtin_amdgcn_raw_buffer_load_b64(rs, voff, soff, AUX_SC1); }
DI unsigned ld32(rsrc_t rs, unsigned voff, unsigned soff) { return __builtin_amdgcn_raw_buffer_load_b32(rs, voff, soff, AUX_SC1); }
DI void st64(v2u v, rsrc_t rs, unsigned voff, unsigned soff) { __builtin_amdgcn_raw_buffer_store_b64(v, rs, voff, soff, AUX_SC1); }

DI void dma4(rsrc_t rs, unsigned voff, unsigned soff, unsigned m0v) {
  asm volatile("s_nop 4\n\ts_mov_b32 m0, %0\n\ts_nop 0\n\t"
               "buffer_load_dwordx4 %1, %2, %3 offen sc1 lds\n\t"
               "buffer_load_dwordx4 %1, %2, %3 offen offset:1024 sc1 lds\n\t"
               "buffer_load_dwordx4 %1, %2, %3 offen offset:2048 sc1 lds\n\t"
               "buffer_load_dwordx4 %1, %2, %3 offen offset:3072 sc1 lds"
               :: "s"(m0v), "v"(voff), "s"(rs), "s"(soff) : "m0", "memory");
}
DI void dma1(rsrc_t rs, unsigned voff, unsigned soff, unsigned m0v) {
  asm volatile("s_nop 4\n\ts_mov_b32 m0, %0\n\ts_nop 0\n\t"
               "buffer_load_dwordx4 %1, %2, %3 offen sc1 lds"
               :: "s"(m0v), "v"(voff), "s"(rs), "s"(soff) : "m0", "memory");
}
DI void dma1q(rsrc_t rs, unsigned voff, unsigned soff, unsigned m0v) {
  asm volatile("s_mov_b32 m0, %0\n\ts_nop 0\n\tbuffer_load_dwordx4 %1, %2, %3 offen sc1 lds"
               :: "s"(m0v), "v"(voff), "s"(rs), "s"(soff) : "m0", "memory");
}
DI void dma_item(rsrc_t rs, unsigned voff, unsigned m0v) {
  asm volatile("s_nop 4\n\ts_mov_b32 m0, %0\n\ts_nop 0\n\t"
               "buffer_load_dwordx4 %1, %2, 0 offen lds"
               :: "s"(m0v), "v"(voff), "s"(rs) : "m0", "memory");
}
DI v4u item_read(LAS char* img, int lane) {
  LAS char* p = img + ((lane >> 2) & 3) * 128 + (lane >> 4) * 32 + (lane & 3) * 8;
  const s16x4 a = __builtin_amdgcn_ds_read_tr16_b64_v4i16((LAS s16x4*)p);
  const s16x4 b = __builtin_amdgcn_ds_read_tr16_b64_v4i16((LAS s16x4*)(p + 512));
  const v2u x = __builtin_bit_cast(v2u, a), y = __builtin_bit_cast(v2u, b);
  return v4u{x.x, x.y, y.x, y.y};
}
#define LDF(sbase, i) ld128(ws, l16 + ((i) & 3) * 1024, (sbase) + ((i) >> 2) * 4096)
#define LDSF(off) (*(LAS v4u*)(((off) < 65536 ? lb0 : ((off) < 131072 ? lb1 : lb2)) + ((off) & 65535)))
#define W1A(ks, n) (((ks) < 7 ? L_B + (ks) * 7168 : L_A + ((ks) - 7) * 7168) + (n) * 1024)

DI void wait_flag(gu32* f, unsigned tag, bool active) {
  unsigned sp = 0;
  for (;;) {
    unsigned v = active ? __hip_atomic_load(f, RLX) : tag;
    if (__all(v == tag)) break;
    if (++sp > SPIN_CAP) break;
  }
  __builtin_amdgcn_fence(__ATOMIC_ACQUIRE, "wavefront");
}

#define PUBLISH(ph) do { DRAIN(); if (lane == 0) __hip_atomic_store(flg + ((ph) * 256 + cuid) * 32 + w, tag, RLX); } while (0)


__global__ __launch_bounds__(256, 1) void fused_block(const unsigned short* __restrict__ x, const unsigned short* __restrict__ wqkv,
                                                      const unsigned short* __restrict__ wo, const unsigned short* __restrict__ w1,
                                                      const unsigned short* __restrict__ w2, unsigned short* __restrict__ out,
                                                      char* __restrict__ wsp) {
  extern __shared__ __attribute__((aligned(16))) char smem[];
  LAS char* lds = (LAS char*)smem;
  const unsigned ldsb = (unsigned)(size_t)lds;
  const int tid = threadIdx.x, lane = tid & 63;
  const int w = __builtin_amdgcn_readfirstlane(tid >> 6);
  const int g = lane >> 4, t16 = lane & 15;
  const int b = blockIdx.x, xcd = b & 7, sl = b >> 3;
  const int c2 = xcd & 1, P = 4 * (xcd >> 1) + (sl >> 3), m = 8 * c2 + (sl & 7);
  const int cuid = P * 16 + m;
  const int jl = (m & 7) >> 2, ct = 4 * c2 + (m & 3);
  const unsigned l16 = lane * 16;
  const rsrc_t ws = __builtin_amdgcn_make_buffer_rsrc(wsp, 0, WS_BYTES, 0x00020000);
  gu32* flg = (gu32*)(wsp + OFF_FLAG);
  v2u xv[5];
  {
    const GAS char* xp = (const GAS char*)(x + (size_t)(128 * P + 8 * m + 2 * w + (lane >> 5)) * 640) + (lane & 31) * 8;
    asm volatile("global_load_dwordx2 %0, %5, off\n\tglobal_load_dwordx2 %1, %5, off offset:256\n\tglobal_load_dwordx2 %2, %5, off offset:512\n\t"
                 "global_load_dwordx2 %3, %5, off offset:768\n\tglobal_load_dwordx2 %4, %5, off offset:1024"
                 : "=&v"(xv[0]), "=&v"(xv[1]), "=&v"(xv[2]), "=&v"(xv[3]), "=&v"(xv[4]) : "v"(xp) : "memory");
  }
  unsigned warm0;
  {
    unsigned long long pc;
    asm volatile("s_getpc_b64 %0" : "=s"(pc));
    const int ln = min(sl * 11 + min(w * 3 + min(lane, 2), 10), CODE_LINES - 1);
    asm volatile("global_load_dword %0, %1, off" : "=v"(warm0) : "v"((const GAS char*)((pc & ~127ull) + (unsigned long long)ln * 128ull)) : "memory");
  }
  unsigned epoch;
  asm volatile("global_load_dword %0, %1, off sc1" : "=v"(epoch) : "v"((const GAS char*)wsp + OFF_EPOCH) : "memory");
  LAS char* lb0 = lds + l16; LAS char* lb1 = lb0 + 65536; LAS char* lb2 = lb0 + 131072;
  asm volatile("" : "+v"(lb1), "+v"(lb2));

  const int r8 = lane >> 3, ch8 = lane & 7;
#define P0B_INDEX() \
  int gv = cuid * 2 + (w & 1); asm volatile("" : "+s"(gv)); \
  const int rg1 = gv / 6, g6 = gv - 6 * rg1, cg1 = g6 < 3 ? 5 * g6 : 15 + 4 * (g6 - 3), n1 = gv < 480 ? (g6 < 3 ? 5 : 4) : 0; \
  const int rgA = gv < 432 ? gv >> 1 : gv - 432, cgA = gv < 432 ? 5 * (gv & 1) : 0, nAi = gv < 432 ? 5 : 10
  unsigned voA = 0, vo1 = 0;
  if (__builtin_expect(w < 2, 1)) {
    const rsrc_t rq = __builtin_amdgcn_make_buffer_rsrc((void*)wqkv, 0, 640 * 1920 * 2, 0x00020000);
    const int col = w == 0 ? (ch8 < 5 ? 640 + 40 * m + 8 * ch8 : 1280 + 40 * m + 8 * (ch8 - 5))
                           : (ch8 < 2 ? 1280 + 40 * m + 24 + 8 * ch8 : 40 * m + 8 * ((ch8 < 7 ? ch8 : 6) - 2));
    const unsigned vo = (unsigned)((40 * P + r8) * 1920 + col) * 2;
#pragma unroll
    for (int i = 0; i < 5; ++i) dma_item(rq, vo + i * 8 * 3840, ldsb + L_A + 16384 + (w * 5 + i) * 1024);
  } else {
    P0B_INDEX();
    (void)n1; (void)nAi;
    {
      const int sq = rgA >> 2, gg = rgA & 3;
      int row;
      if (gv < 432) {
        const int wq = sq < 28 ? sq / 14 : 2 + (sq - 28) / 13, k = sq - (wq < 2 ? 14 * wq : 28 + 13 * (wq - 2));
        int nA, nB;
        if (k < 12) {
          const int tt = 4 * (wq & 1) + k / 3, i = k % 3;
          nA = 54 * (wq >> 1) + (tt < 6 ? 7 * tt : 42 + 6 * (tt - 6)) + 2 * i; nB = nA + 1;
        } else {
          const int ci = (wq < 2 ? 2 * wq : wq + 2) + (k - 12), h2 = ci / 3, a3 = ci - 3 * h2;
          nA = 54 * h2 + 14 * a3 + 6; nB = nA + 7;
        }
        row = 16 * (r8 < 4 ? nA : nB) + 4 * gg + (r8 & 3);
      } else {
        row = sq < 16 ? 40 * sq + 5 * gg + (r8 < 5 ? r8 : 15 + r8) : 40 * (4 * (sq - 16) + (r8 >> 1)) + 23 + 5 * gg + (r8 & 1);
      }
      voA = (unsigned)(row * 640 + 64 * cgA + 8 * ch8) * 2;
    }
    {
      const int ks = rg1 >> 2, gg = rg1 & 3;
      const int nA = ks < 16 ? 5 * (ks >> 1) + 2 * (ks & 1) : 10 * (ks - 16) + 4, nB = ks < 16 ? nA + 1 : nA + 5;
      vo1 = (unsigned)((16 * (r8 < 4 ? nA : nB) + 4 * gg + (r8 & 3)) * 1728 + 64 * cg1 + 8 * ch8) * 2;
    }
  }
  asm volatile("s_cmp_lt_u32 %7, 2\n\ts_cbranch_scc1 1f\n\ts_waitcnt vmcnt(0)\n\ts_branch 2f\n1:\n\ts_waitcnt vmcnt(5)\n2:"
               : "+v"(xv[0]), "+v"(xv[1]), "+v"(xv[2]), "+v"(xv[3]), "+v"(xv[4]), "+v"(epoch), "+v"(warm0) : "s"(w) : "memory", "scc");
  if (__builtin_expect(w >= 2, 0)) {
    P0B_INDEX();
    (void)rg1; (void)cg1; (void)rgA; (void)cgA;
    const unsigned sb = ldsb + L_B + (w & 1) * 15360;
    const rsrc_t rA = __builtin_amdgcn_make_buffer_rsrc((void*)(gv < 432 ? w2 : wo), 0, gv < 432 ? 1728 * 640 * 2 : 640 * 640 * 2, 0x00020000);
    for (int i = 0; i < nAi; ++i) dma_item(rA, voA + i * 128, sb + i * 1024);
    const rsrc_t r1 = __builtin_amdgcn_make_buffer_rsrc((void*)w1, 0, 640 * 1728 * 2, 0x00020000);
    for (int i = 0; i < n1; ++i) dma_item(r1, vo1 + i * 128, sb + (10 + i) * 1024);
  }
  {
    float f[20];
#pragma unroll
    for (int i = 0; i < 5; ++i) { f[4 * i] = blo(xv[i].x); f[4 * i + 1] = bhi(xv[i].x); f[4 * i + 2] = blo(xv[i].y); f[4 * i + 3] = bhi(xv[i].y); }
    float s4[4];
#pragma unroll
    for (int j = 0; j < 4; ++j) s4[j] = (f[j] + f[4 + j]) + (f[8 + j] + f[12 + j]) + f[16 + j];
    const float mean = sum_x16(sum16((s4[0] + s4[1]) + (s4[2] + s4[3]))) * (1.f / 640.f);
#pragma unroll
    for (int i = 0; i < 20; ++i) f[i] -= mean;
#pragma unroll
    for (int j = 0; j < 4; ++j) s4[j] = (f[j] * f[j] + f[4 + j] * f[4 + j]) + (f[8 + j] * f[8 + j] + f[12 + j] * f[12 + j]) + f[16 + j] * f[16 + j];
    const float rstd = __builtin_amdgcn_rsqf(sum_x16(sum16((s4[0] + s4[1]) + (s4[2] + s4[3]))) * (1.f / 640.f) + 1e-5f);
    LAS char* sc = lds + L_A + ((lane & 31) >> 1) * 128 + (2 * w + (lane >> 5)) * 16 + (lane & 1) * 8;
#pragma unroll
    for (int i = 0; i < 5; ++i)
      *(LAS v2u*)(sc + i * 2048) = v2u{pk2(f[4 * i] * rstd, f[4 * i + 1] * rstd), pk2(f[4 * i + 2] * rstd, f[4 * i + 3] * rstd)};
  }
  BAR();
  unsigned tag = 0;
  if (__builtin_expect(w < 2, 1)) {
#pragma unroll
    for (int i = 0; i < 5; ++i) {
      const int L = 320 * w + 64 * i + lane, c = L >> 3, tt = 8 * (m & 1) + (L & 7);
      st128(*(LAS v4u*)(lds + L_A + L * 16), ws, OFF_XN + ((8 * P + (m >> 1)) * 20 + (c >> 2)) * 1024 + ((c & 3) * 16 + tt) * 16, 0);
    }
    asm volatile("s_waitcnt vmcnt(5)" ::: "memory");
    const int pos = 64 * w + lane, nb = pos >> 4, n = pos & 15;
#pragma unroll
    for (int i = 0; i < 5; ++i) {
      const v4u o = item_read(lds + L_A + 16384 + (w * 5 + i) * 1024, lane);
      const int rg = 5 * P + i, ks = rg >> 2, gg = rg & 3;
      if (pos < 120) st128(o, ws, OFF_WQKV + m * WQKV_STR + (nb < 7 ? (nb * 20 + ks) * 1024 + (gg * 16 + n) * 16 : 143360 + ks * 512 + (gg * 8 + n) * 16), 0);
    }
    DRAIN();
  }
  tag = __builtin_amdgcn_readfirstlane(epoch) * 2654435761u + 0x9E3779B9u;
  BAR();
  if (tid == 0) __hip_atomic_store(flg + (0 * 256 + cuid) * 32, tag, RLX);
  if (__builtin_expect(w < 2, 1)) *(LAS v4u*)(lds + L_IMG + tid * 32) = v4u{0u, 0u, 0u, 0u}, *(LAS v4u*)(lds + L_IMG + tid * 32 + 16) = v4u{0u, 0u, 0u, 0u};
  else {
    P0B_INDEX();
    LAS char* im = lds + L_B + (w & 1) * 15360;
    DRAIN();
    const unsigned baseA = (gv < 432 ? OFF_W2 : OFF_WO) + ((rgA >> 2) * 5 * 64 + (rgA & 3) * 16) * 16, strA = gv < 432 ? W2_STR : 102400u;
    for (int i = 0; i < nAi; ++i) {
      const int n = 64 * (cgA + i) + lane, c = n / 80, nl = n - 80 * c;
      st128(item_read(im + i * 1024, lane), ws, baseA + c * strA + (nl >> 4) * 1024 + (nl & 15) * 16, 0);
    }
    const unsigned base1 = OFF_W1 + ((rg1 >> 2) * 7 * 64 + (rg1 & 3) * 16) * 16;
    for (int i = 0; i < n1; ++i) {
      const int n = 64 * (cg1 + i) + lane;
      const int nb = n >> 4, h2 = nb >= 54 ? 1 : 0, nbp = nb - 54 * h2;
      const int tt = nbp < 42 ? nbp / 7 : 6 + (nbp - 42) / 6, nbl = nbp - (tt < 6 ? 7 * tt : 42 + 6 * (tt - 6));
      st128(item_read(im + (10 + i) * 1024, lane), ws, base1 + (8 * h2 + tt) * W1_STR + nbl * 1024 + (n & 15) * 16, 0);
    }
  }

  if (w == 0) {
    const int l = lane & 15;
    wait_flag(flg + (0 * 256 + (lane < 16 ? P * 16 + l : l * 16 + m)) * 32, tag, lane < 32);
  }
  BAR();
  v4u fb;
  v4u xn[2][20];
  f32x4 akv[2][5];
  v4u wqs[13];
  {
    constexpr int D1 = 5;
    for (int q = w; q < 25; q += 4) dma4(ws, l16, OFF_WQKV + m * WQKV_STR + q * 4096, ldsb + L_A + q * 4096);
    const unsigned xb = OFF_XN + (8 * P + 2 * w) * 20 * 1024;
#pragma unroll
    for (int ks = 0; ks < D1; ++ks) {
#pragma unroll
      for (int u = 0; u < 2; ++u) xn[u][ks] = LDF(xb, u * 20 + ks);
      SCHED();
    }
    asm volatile("s_waitcnt vmcnt(10)" ::: "memory");
    BAR();
    if (tid == 0) __hip_atomic_store((gu32*)(wsp + OFF_FLAGB) + cuid, tag, RLX);
#pragma unroll
    for (int u = 0; u < 2; ++u)
#pragma unroll
      for (int n = 0; n < 5; ++n) akv[u][n] = f32x4{0.f, 0.f, 0.f, 0.f};
    v4u wf[2][5];
#pragma unroll
    for (int n = 0; n < 5; ++n) wf[0][n] = LDSF(L_A + (n * 20) * 1024);
    SCHED();
    const unsigned wqb = OFF_WQKV + m * WQKV_STR + 102400 + w * 1024;
#pragma unroll
    for (int ks = 0; ks < 20; ++ks) {
      if (ks + D1 < 20) {
#pragma unroll
        for (int u = 0; u < 2; ++u) xn[u][ks + D1] = LDF(xb, u * 20 + ks + D1);
      }
      if (ks >= 4 && ks < 17) wqs[ks - 4] = ld128(ws, l16, wqb + (ks - 4 < 12 || w < 2 ? ks - 4 : 11) * 4096);
      if (ks == 17) fb = __builtin_amdgcn_raw_buffer_load_b128(ws, OFF_FLAGB + l16, 0, AUX_SC1);
      if (ks + 1 < 20) {
#pragma unroll
        for (int n = 0; n < 5; ++n) wf[(ks + 1) & 1][n] = LDSF(L_A + (n * 20 + ks + 1) * 1024);
      }
#pragma unroll
      for (int n = 0; n < 5; ++n)
#pragma unroll
        for (int u = 0; u < 2; ++u) akv[u][n] = mfma(xn[u][ks], wf[ks & 1][n], akv[u][n]);
#pragma unroll
      for (int n = 0; n < 5; ++n) { SGB(0x100, 1); SGB(0x8, 1); if (n < 3) SGB(0x20, 1); SGB(0x8, 1); }
      SCHED();
    }
  }
  if (!__all(fb.x == tag && fb.y == tag && fb.z == tag && fb.w == tag)) {
    unsigned sp = 0;
    for (;;) {
      asm volatile("" ::: "memory");
      fb = __builtin_amdgcn_raw_buffer_load_b128(ws, OFF_FLAGB + l16, 0, AUX_SC1);
      if (__all(fb.x == tag && fb.y == tag && fb.z == tag && fb.w == tag)) break;
      if (++sp > SPIN_CAP) break;
    }
  }
  v4u w2f[14][5];
  const int k4s = w < 2 ? 14 * w : 28 + 13 * (w - 2);
  const unsigned w2b = OFF_W2 + ct * W2_STR + k4s * 5 * 1024;
#define W2LD(f0, f1) _Pragma("unroll") for (int f_ = (f0); f_ < (f1); ++f_) w2f[f_ / 5][f_ % 5] = LDF(w2b, f_); SCHED()
  W2LD(0, 12);
  BAR();
#pragma unroll
  for (int u = 0; u < 2; ++u)
#pragma unroll
    for (int n = 0; n < 5; ++n) {
      f32x4 a = akv[u][n];
      if (n < 2) { a[0] = phi(a[0]); a[1] = phi(a[1]); a[2] = phi(a[2]); a[3] = phi(a[3]); }
      else if (n == 2) { if (t16 < 8) { a[0] = phi(a[0]); a[1] = phi(a[1]); a[2] = phi(a[2]); a[3] = phi(a[3]); } }
      *(LAS v2u*)(lds + L_A + (16 * n + t16) * EP + (32 * w + 16 * u + 4 * g) * 2) = v2u{pk2(a[0], a[1]), pk2(a[2], a[3])};
    }
  if (tid < 16) *(LAS v4u*)(lds + L_A + 80 * EP + tid * 16) = v4u{0x3F803F80u, 0x3F803F80u, 0x3F803F80u, 0x3F803F80u};
  BAR();
  W2LD(12, 24);
  {
    const int hd = w >> 1, dt = w & 1;
    f32x4 t0 = {0.f, 0.f, 0.f, 0.f}, t1 = t0;
    const unsigned ra = L_A + (20 * hd + 16 * dt + t16) * EP + g * 16;
    const unsigned rb0 = L_A + (40 + 20 * hd + t16) * EP + g * 16;
    const unsigned rb1 = L_A + (t16 < 4 ? 56 + 20 * hd + t16 : 80) * EP + g * 16;
    v4u fa[4], f0[4], f1[4];
#pragma unroll
    for (int s = 0; s < 4; ++s) { fa[s] = *(LAS v4u*)(lds + ra + s * 64); f0[s] = *(LAS v4u*)(lds + rb0 + s * 64); f1[s] = *(LAS v4u*)(lds + rb1 + s * 64); }
#pragma unroll
    for (int s = 0; s < 4; ++s) { t0 = mfma(fa[s], f0[s], t0); t1 = mfma(fa[s], f1[s], t1); }
    const unsigned pt = L_PT + hd * 1680;
    int dv = dt; asm volatile("" : "+v"(dv));
    const bool act = g < (dv ? 1 : 4);
    if (act) *(LAS f32x4*)(lds + pt + (dv ? 1344 + t16 * 16 : l16)) = t0;
    if (act && t16 < 5) *(LAS f32x4*)(lds + pt + (dv ? 1600 + t16 * 16 : 1024 + (g * 5 + t16) * 16)) = t1;
    if (tid >= 250) *(LAS v4u*)(lds + L_PT + 3360 + (tid - 250) * 16) = v4u{0u, 0u, 0u, 0u};
  }
  BAR();
  if (tid < 216) st128(*(LAS v4u*)(lds + L_PT + tid * 16), ws, tid * 16, OFF_PART + (m * 16 + P) * PART_STR);
#pragma unroll
  for (int i = 0; i < 13; ++i)
    if (i < 12 || w < 2) *(LAS v4u*)(lds + L_B + (w + 4 * i) * 1024 + l16) = wqs[i];
  PUBLISH(1);
  BAR();

  unsigned qp[2][3][2];
  {
    f32x4 aq[2][3];
#pragma unroll
    for (int u = 0; u < 2; ++u)
#pragma unroll
      for (int n = 0; n < 3; ++n) aq[u][n] = f32x4{0.f, 0.f, 0.f, 0.f};
    LAS char* lq7 = lds + L_B + 40960 + g * 128 + (t16 & 7) * 16;
    const unsigned wob = OFF_WO + ct * 102400 + w * 1024, wod = ldsb + L_A + w * 1024;
    v4u wf[3][3];
#pragma unroll
    for (int k = 0; k < 2; ++k) { wf[k][0] = LDSF(L_B + k * 1024); wf[k][1] = LDSF(L_B + (20 + k) * 1024); wf[k][2] = *(LAS v4u*)(lq7 + k * 512); }
    SCHED();
#pragma unroll
    for (int ks = 0; ks < 20; ++ks) {
      if (ks >= 4) dma1q(ws, l16, wob + (ks - 4) * 4096, wod + (ks - 4) * 4096);
      if (ks >= 11) dma1q(ws, l16, wob + (ks + 5) * 4096, wod + (ks + 5) * 4096);
      if (ks + 2 < 20) {
        wf[(ks + 2) % 3][0] = LDSF(L_B + (ks + 2) * 1024);
        wf[(ks + 2) % 3][1] = LDSF(L_B + (20 + ks + 2) * 1024);
        wf[(ks + 2) % 3][2] = *(LAS v4u*)(lq7 + (ks + 2) * 512);
      }
#pragma unroll
      for (int n = 0; n < 3; ++n)
#pragma unroll
        for (int u = 0; u < 2; ++u) aq[u][n] = mfma(wf[ks % 3][n], xn[u][ks], aq[u][n]);
#pragma unroll
      for (int n = 0; n < 3; ++n) { SGB(0x100, 1); SGB(0x8, 2); }
      SCHED();
    }
#pragma unroll
    for (int u = 0; u < 2; ++u)
#pragma unroll
      for (int n = 0; n < 3; ++n) {
        qp[u][n][0] = pk2(phi(aq[u][n][0]), phi(aq[u][n][1]));
        qp[u][n][1] = pk2(phi(aq[u][n][2]), phi(aq[u][n][3]));
      }
  }
  BAR();
  unsigned imgoff, imgoffz;
  {
    const int hd = tid >= 105 ? 1 : 0, u = tid - 105 * hd;
    int dq, e;
    if (u < 64) { dq = u >> 4; e = u & 15; }
    else if (u < 84) { const int v = u - 64; dq = v / 5; e = 16 + v - 5 * dq; }
    else if (u < 100) { dq = 4; e = u - 84; }
    else { dq = 4; e = 16 + u - 100; }
    int gq;
    if (hd == 0) gq = dq < 4 ? dq : 0; else gq = dq < 3 ? dq + 1 : (dq == 3 ? 0 : 1);
    const int j0 = dq < 4 ? 0 : 4, ge = e / 5, re = e - 5 * ge;
    const unsigned ib = L_IMG + hd * 2048 + gq * 256 + j0 * 2;
    imgoff = ib + (re < 4 ? (4 * ge + re) * 16 : 1024 + (4 * ge) * 16);
    imgoffz = e == 20 ? ib + 1024 + 16 : 0u;
  }

  wait_flag(flg + (1 * 256 + (lane >> 2) * 16 + m) * 32 + (lane & 3), tag, true);
  v4u pv[16];
  {
    const unsigned vo = (tid < 216 ? tid : 215) * 16, pb = OFF_PART + m * 16 * PART_STR;
#define PV8(o) asm volatile("buffer_load_dwordx4 %0, %8, %9, %10 offen sc1\n\tbuffer_load_dwordx4 %1, %8, %9, %11 offen sc1\n\t" \
      "buffer_load_dwordx4 %2, %8, %9, %12 offen sc1\n\tbuffer_load_dwordx4 %3, %8, %9, %13 offen sc1\n\t" \
      "buffer_load_dwordx4 %4, %8, %9, %14 offen sc1\n\tbuffer_load_dwordx4 %5, %8, %9, %15 offen sc1\n\t" \
      "buffer_load_dwordx4 %6, %8, %9, %16 offen sc1\n\tbuffer_load_dwordx4 %7, %8, %9, %17 offen sc1" \
      : "=&v"(pv[o]), "=&v"(pv[o + 1]), "=&v"(pv[o + 2]), "=&v"(pv[o + 3]), "=&v"(pv[o + 4]), "=&v"(pv[o + 5]), "=&v"(pv[o + 6]), "=&v"(pv[o + 7]) \
      : "v"(vo), "s"(ws), "s"(pb + (o) * PART_STR), "s"(pb + (o + 1) * PART_STR), "s"(pb + (o + 2) * PART_STR), "s"(pb + (o + 3) * PART_STR), \
        "s"(pb + (o + 4) * PART_STR), "s"(pb + (o + 5) * PART_STR), "s"(pb + (o + 6) * PART_STR), "s"(pb + (o + 7) * PART_STR) : "memory")
    PV8(0); PV8(8);
    for (int q = w; q < 12; q += 4) dma4(ws, l16, OFF_W1 + m * W1_STR + q * 4096, ldsb + L_B + q * 4096);
    if (w == 0) dma1(ws, l16, OFF_W1 + m * W1_STR + 49152, ldsb + L_B + 49152);
    asm volatile("s_waitcnt vmcnt(12)"
                 : "+v"(pv[0]), "+v"(pv[1]), "+v"(pv[2]), "+v"(pv[3]), "+v"(pv[4]), "+v"(pv[5]), "+v"(pv[6]), "+v"(pv[7]),
                   "+v"(pv[8]), "+v"(pv[9]), "+v"(pv[10]), "+v"(pv[11]), "+v"(pv[12]), "+v"(pv[13]), "+v"(pv[14]), "+v"(pv[15]) :: "memory");
  }
  if (tid < 210) {
    f32x4 s[4];
#pragma unroll
    for (int i = 0; i < 4; ++i)
      s[i] = (__builtin_bit_cast(f32x4, pv[4 * i]) + __builtin_bit_cast(f32x4, pv[4 * i + 1])) +
             (__builtin_bit_cast(f32x4, pv[4 * i + 2]) + __builtin_bit_cast(f32x4, pv[4 * i + 3]));
    const f32x4 st = (s[0] + s[1]) + (s[2] + s[3]);
    const v2u val = v2u{pk2(st[0], st[1]), pk2(st[2], st[3])};
    if (imgoffz == 0u) *(LAS v2u*)(lds + imgoff) = val;
    else {
#pragma unroll
      for (int ge = 0; ge < 4; ++ge) *(LAS v2u*)(lds + imgoffz + ge * 64) = val;
    }
  }
  BAR();
  {
    v4u ka[2][2];
#pragma unroll
    for (int hd = 0; hd < 2; ++hd)
#pragma unroll
      for (int bb = 0; bb < 2; ++bb) ka[hd][bb] = *(LAS v4u*)(lds + L_IMG + hd * 2048 + bb * 1024 + l16);
#pragma unroll
    for (int u = 0; u < 2; ++u) {
      v4u qa, qb;
      qa.x = qp[u][0][0]; qa.y = qp[u][0][1]; qa.z = g == 0 ? qp[u][1][0] : 0u; qa.w = g == 0 ? qp[u][1][1] : 0u;
      qb.x = g == 0 ? qp[u][2][0] : qp[u][1][0]; qb.y = g == 0 ? qp[u][2][1] : qp[u][1][1];
      qb.z = g == 1 ? qp[u][2][0] : 0u; qb.w = g == 1 ? qp[u][2][1] : 0u;
      const f32x4 z4 = {0.f, 0.f, 0.f, 0.f};
      const f32x4 a0 = mfma(ka[0][0], qa, z4), a1 = mfma(ka[0][1], qa, z4);
      const f32x4 b0 = mfma(ka[1][0], qb, z4), b1 = mfma(ka[1][1], qb, z4);
      const float ra = __builtin_amdgcn_rcpf(a1[1] + 1e-6f), rb = __builtin_amdgcn_rcpf(b1[1] + 1e-6f);
      const unsigned d0 = pk2(a0[0] * ra, a0[1] * ra), d1 = pk2(a0[2] * ra, a0[3] * ra), d2 = pk2(a1[0] * ra, b0[0] * rb),
                     d3 = pk2(b0[1] * rb, b0[2] * rb), d4 = pk2(b0[3] * rb, b1[0] * rb);
      const unsigned oi = (8 * P + 2 * w + u) * 16 + m;
      st128(v4u{d0, d1, d2, d3}, ws, l16, OFF_OM + oi * 1024);
      __builtin_amdgcn_raw_buffer_store_b32(d4, ws, lane * 4, OFF_OR + oi * 256, AUX_SC1);
    }
  }
  PUBLISH(2);
  BAR();
  v2u xr[5];
  {
    const GAS char* xp = (const GAS char*)x + (size_t)(64 * (2 * P + jl) + 16 * w + t16) * 1280 + (80 * ct + 4 * g) * 2;
#pragma unroll
    for (int n = 0; n < 5; ++n) xr[n] = *(const GAS v2u*)(xp + n * 32);
  }
  v4u w1r[23];
  const unsigned w1rb = OFF_W1 + m * W1_STR + 50176 + w * 1024;
#pragma unroll
  for (int i = 0; i < 12; ++i) w1r[i] = ld128(ws, l16, w1rb + i * 4096);
  SCHED();

  f32x4 cs7[7];
  const v4u ones = {0x3F803F80u, 0x3F803F80u, 0x3F803F80u, 0x3F803F80u};
  {
    v4u wv[13];
#pragma unroll
    for (int i = 0; i < 13; ++i) wv[i] = *(LAS v4u*)(lb0 + L_B + (w + 4 * (i < 12 || w == 0 ? i : 11)) * 1024);
#pragma unroll
    for (int j = 0; j < 7; ++j) cs7[j] = mfma(wv[j], ones, f32x4{0.f, 0.f, 0.f, 0.f});
#pragma unroll
    for (int i = 7; i < 12; ++i) cs7[i - 7] = mfma(wv[i], ones, cs7[i - 7]);
    if (w == 0) cs7[5] = mfma(wv[12], ones, cs7[5]);
  }
  wait_flag(flg + (2 * 256 + P * 16 + (lane & 15)) * 32 + ((4 * jl + w) >> 1), tag, lane < 16);
  unsigned hp[5][2];
  {
    constexpr int D2 = 8;
    v4u of[20];
    const unsigned ob = OFF_OM + (8 * P + 4 * jl + w) * 16 * 1024, orb = OFF_OR + (8 * P + 4 * jl + w) * 16 * 256;
#define LDO(ks) ((ks) < 16 ? LDF(ob, ks) : v4u{ld32(ws, lane * 4 + (4 * ((ks) - 16)) * 256, orb), ld32(ws, lane * 4 + (4 * ((ks) - 16) + 1) * 256, orb), \
                                              ld32(ws, lane * 4 + (4 * ((ks) - 16) + 2) * 256, orb), ld32(ws, lane * 4 + (4 * ((ks) - 16) + 3) * 256, orb)})
#pragma unroll
    for (int ks = 0; ks < D2; ++ks) { of[ks] = LDO(ks); SCHED(); }
    f32x4 ah[5];
#pragma unroll
    for (int n = 0; n < 5; ++n) ah[n] = f32x4{0.f, 0.f, 0.f, 0.f};
    v4u wf[3][5];
#pragma unroll
    for (int n = 0; n < 5; ++n) { wf[0][n] = LDSF(L_A + n * 1024); wf[1][n] = LDSF(L_A + (5 + n) * 1024); }
    SCHED();
#pragma unroll
    for (int ks = 0; ks < 20; ++ks) {
      if (ks + D2 < 20) of[ks + D2] = LDO(ks + D2);
      else {
        const int i0 = ks < 15 ? 12 + 2 * (ks - 12) : 15 + (ks - 12);
        w1r[i0] = ld128(ws, l16, w1rb + (i0 < 22 || w < 3 ? i0 : 21) * 4096);
        if (ks < 15) w1r[i0 + 1] = ld128(ws, l16, w1rb + (i0 + 1) * 4096);
      }
      if (ks + 2 < 20) {
#pragma unroll
        for (int n = 0; n < 5; ++n) wf[(ks + 2) % 3][n] = LDSF(L_A + ((ks + 2) * 5 + n) * 1024);
      }
#pragma unroll
      for (int n = 0; n < 5; ++n) ah[n] = mfma(wf[ks % 3][n], of[ks], ah[n]);
#pragma unroll
      for (int n = 0; n < 5; ++n) { SGB(0x100, 1); SGB(0x8, 1); if (n < 4) SGB(0x20, 1); }
      SCHED();
    }
    float hv[20];
#pragma unroll
    for (int n = 0; n < 5; ++n) {
      hp[n][0] = pk2(blo(xr[n].x) + ah[n][0], bhi(xr[n].x) + ah[n][1]);
      hp[n][1] = pk2(blo(xr[n].y) + ah[n][2], bhi(xr[n].y) + ah[n][3]);
    }
    const unsigned hi = (8 * P + 4 * jl + w) * 8 + ct;
    st128(v4u{hp[0][0], hp[0][1], hp[1][0], hp[1][1]}, ws, l16, OFF_HF + hi * 2048);
    st128(v4u{hp[2][0], hp[2][1], hp[3][0], hp[3][1]}, ws, l16 + 1024, OFF_HF + hi * 2048);
    st64(v2u{hp[4][0], hp[4][1]}, ws, lane * 8, OFF_HH + hi * 512);
    float s4[5];
#pragma unroll
    for (int n = 0; n < 5; ++n) {
      hv[4 * n] = blo(hp[n][0]); hv[4 * n + 1] = bhi(hp[n][0]); hv[4 * n + 2] = blo(hp[n][1]); hv[4 * n + 3] = bhi(hp[n][1]);
      s4[n] = (hv[4 * n] + hv[4 * n + 1]) + (hv[4 * n + 2] + hv[4 * n + 3]);
    }
    const float mi = sum_g((s4[0] + s4[1]) + (s4[2] + s4[3]) + s4[4]) * (1.f / 80.f);
#pragma unroll
    for (int n = 0; n < 5; ++n) {
      const float a = hv[4 * n] - mi, bq = hv[4 * n + 1] - mi, c = hv[4 * n + 2] - mi, d = hv[4 * n + 3] - mi;
      s4[n] = (a * a + bq * bq) + (c * c + d * d);
    }
    const float q = sum_g((s4[0] + s4[1]) + (s4[2] + s4[3]) + s4[4]);
    if (g == 0) st64(v2u{asu(mi), asu(q)}, ws, t16 * 8, OFF_ST + ((8 * P + 4 * jl + w) * 8 + ct) * 128);
  }
  PUBLISH(3);
  BAR();
#pragma unroll
  for (int i = 0; i < 23; ++i)
    if (i < 22 || w < 3) {
      *(LAS v4u*)(lds + L_A + (w + 4 * i) * 1024 + l16) = w1r[i];
      cs7[i % 7] = mfma(w1r[i], ones, cs7[i % 7]);
    }
  if (t16 == 0) {
#pragma unroll
    for (int j = 0; j < 7; ++j) *(LAS f32x4*)(lds + L_CS + (w * 7 + (w + 4 * j) % 7) * 64 + g * 16) = cs7[j];
  }
  W2LD(24, 36);

  BAR();
  {
    const int i = (lane & 15) >> 1;
    wait_flag(flg + (3 * 256 + P * 16 + 8 * (i >> 2) + 4 * (w >> 1) + (i & 3)) * 32 + ((2 * w + (lane & 1)) & 3), tag, lane < 16);
  }
  {
    constexpr int D3 = 5;
    v4u hf[2][20];
    const unsigned hb = OFF_HF + (8 * P + 2 * w) * 16 * 1024, hhb = OFF_HH + (8 * P + 2 * w) * 8 * 512;
    auto ldh = [&](int u, int ks) -> v4u {
      if (ks < 16) return LDF(hb, u * 16 + ks);
      const v2u a = ld64(ws, lane * 8 + (2 * (ks - 16)) * 512, hhb + u * 4096), c = ld64(ws, lane * 8 + (2 * (ks - 16) + 1) * 512, hhb + u * 4096);
      return v4u{a.x, a.y, c.x, c.y};
    };
    v2u stv[2][2];
#pragma unroll
    for (int ks = 0; ks < D3; ++ks) {
#pragma unroll
      for (int u = 0; u < 2; ++u) hf[u][ks] = ldh(u, ks);
      SCHED();
    }
#pragma unroll
    for (int u = 0; u < 2; ++u)
#pragma unroll
      for (int i = 0; i < 2; ++i) stv[u][i] = ld64(ws, t16 * 8 + (2 * g + i) * 128, OFF_ST + (8 * P + 2 * w + u) * 1024);
    f32x4 cs[7];
#pragma unroll
    for (int n = 0; n < 7; ++n) {
      f32x4 cw[4];
#pragma unroll
      for (int ww = 0; ww < 4; ++ww) cw[ww] = *(LAS f32x4*)(lds + L_CS + (ww * 7 + n) * 64 + g * 16);
      cs[n] = (cw[0] + cw[1]) + (cw[2] + cw[3]);
    }
    f32x4 a3[2][7];
#pragma unroll
    for (int u = 0; u < 2; ++u)
#pragma unroll
      for (int n = 0; n < 7; ++n) a3[u][n] = f32x4{0.f, 0.f, 0.f, 0.f};
    v4u wf[2][7];
#pragma unroll
    for (int n = 0; n < 7; ++n) wf[0][n] = LDSF(W1A(0, n));
    SCHED();
#pragma unroll
    for (int ks = 0; ks < 20; ++ks) {
      if (ks + D3 < 20) {
#pragma unroll
        for (int u = 0; u < 2; ++u) hf[u][ks + D3] = ldh(u, ks + D3);
      }
      w2f[(36 + ks) / 5][(36 + ks) % 5] = LDF(w2b, 36 + ks);
      if (ks + 1 < 20) {
#pragma unroll
        for (int n = 0; n < 7; ++n) wf[(ks + 1) & 1][n] = LDSF(W1A(ks + 1, n));
      }
#pragma unroll
      for (int n = 0; n < 7; ++n)
#pragma unroll
        for (int u = 0; u < 2; ++u) a3[u][n] = mfma(wf[ks & 1][n], hf[u][ks], a3[u][n]);
#pragma unroll
      for (int n = 0; n < 7; ++n) { SGB(0x100, 1); SGB(0x8, 1); if (n < 5) SGB(0x20, 1); SGB(0x8, 1); }
      SCHED();
    }
    float mean[2], rstd[2];
#pragma unroll
    for (int u = 0; u < 2; ++u) {
      const float m0 = asf(stv[u][0].x), q0 = asf(stv[u][0].y), m1 = asf(stv[u][1].x), q1 = asf(stv[u][1].y);
      const float mu = sum_g(m0 + m1) * 0.125f;
      const float M2 = sum_g(q0 + q1 + 80.f * ((m0 - mu) * (m0 - mu) + (m1 - mu) * (m1 - mu)));
      rstd[u] = __builtin_amdgcn_rsqf(M2 * (1.f / 640.f) + 1e-5f);
      mean[u] = mu * rstd[u];
    }
    unsigned hq[2][7][2];
#pragma unroll
    for (int n = 0; n < 7; ++n) {
#pragma unroll
      for (int u = 0; u < 2; ++u) {
        const f32x2 r2 = {rstd[u], rstd[u]}, m2 = {-mean[u], -mean[u]};
        const f32x2 lo = __builtin_elementwise_fma(f32x2{a3[u][n][0], a3[u][n][1]}, r2, f32x2{cs[n][0], cs[n][1]} * m2);
        const f32x2 hi2 = __builtin_elementwise_fma(f32x2{a3[u][n][2], a3[u][n][3]}, r2, f32x2{cs[n][2], cs[n][3]} * m2);
        const float v0 = fmaxf(lo[0], 0.f), v1 = fmaxf(lo[1], 0.f), v2 = fmaxf(hi2[0], 0.f), v3 = fmaxf(hi2[1], 0.f);
        hq[u][n][0] = pk2(v0, v1); hq[u][n][1] = pk2(v2, v3);
      }
    }
#pragma unroll
    for (int u = 0; u < 2; ++u) {
      const unsigned di = (8 * P + 2 * w + u) * 16 + m;
      st128(v4u{hq[u][0][0], hq[u][0][1], hq[u][1][0], hq[u][1][1]}, ws, l16, OFF_DF + di * 3072);
      st128(v4u{hq[u][2][0], hq[u][2][1], hq[u][3][0], hq[u][3][1]}, ws, l16 + 1024, OFF_DF + di * 3072);
      st128(v4u{hq[u][4][0], hq[u][4][1], hq[u][5][0], hq[u][5][1]}, ws, l16 + 2048, OFF_DF + di * 3072);
      if ((m & 7) < 6) st64(v2u{hq[u][6][0], hq[u][6][1]}, ws, lane * 8, OFF_DH + di * 512);
    }
  }
  PUBLISH(4);
  if (tid == 0 && cuid == 0) __hip_atomic_store((gu32*)(wsp + OFF_EPOCH), epoch + 1u, RLX);
  W2LD(56, 70);
  BAR();
  {
    const int l = lane & 15, j = (l - 8) >> 2, ci = (w < 2 ? 2 * w : w + 2) + (j > 0 && w < 2 ? 1 : 0);
    const int tile = l < 8 ? 4 * w + (l >> 1) : 8 * (ci >= 3 ? 1 : 0) + 2 * (ci >= 3 ? ci - 3 : ci) + ((l >> 1) & 1);
    wait_flag(flg + (4 * 256 + P * 16 + tile) * 32 + 2 * jl + (l & 1), tag, lane < 16);
  }
  {
    f32x4 a4[4][5];
#pragma unroll
    for (int u = 0; u < 4; ++u)
#pragma unroll
      for (int n = 0; n < 5; ++n) a4[u][n] = f32x4{0.f, 0.f, 0.f, 0.f};
    const unsigned hb = OFF_DF + ((8 * P + 4 * jl) * 48 + 12 * w) * 1024, dhb = OFF_DH + (8 * P + 4 * jl) * 16 * 512;
    const int ci0 = w < 2 ? 2 * w : w + 2;
    auto lda = [&](int u, int k) -> v4u {
      if (k < 12) return ld128(ws, l16 + (k & 3) * 1024, hb + u * 48 * 1024 + (k >> 2) * 4096);
      const int ci = ci0 + (k - 12), h2 = ci >= 3 ? 1 : 0, tA = 8 * h2 + 2 * (ci - 3 * h2);
      const v2u a = ld64(ws, lane * 8, dhb + (u * 16 + tA) * 512), c = ld64(ws, lane * 8 + 512, dhb + (u * 16 + tA) * 512);
      return v4u{a.x, a.y, c.x, c.y};
    };
    constexpr int DP = 3;
    v4u af[DP + 1][4];
#pragma unroll
    for (int k = 0; k < DP; ++k)
#pragma unroll
      for (int u = 0; u < 4; ++u) { af[k][u] = lda(u, k); SCHED(); }
#pragma unroll
    for (int k = 0; k < 14; ++k) {
      if (k + DP < 14) {
        if (k + DP < 13 || w < 2) {
#pragma unroll
          for (int u = 0; u < 4; ++u) af[(k + DP) % (DP + 1)][u] = lda(u, k + DP);
        }
      }
      if (k < 13 || w < 2) {
#pragma unroll
        for (int u = 0; u < 4; ++u)
#pragma unroll
          for (int n = 0; n < 5; ++n) a4[u][n] = mfma(w2f[k][n], af[k % (DP + 1)][u], a4[u][n]);
      }
#pragma unroll
      for (int u = 0; u < 4; ++u) { SGB(0x20, 2); SGB(0x8, 5); }
      SCHED();
    }
#pragma unroll
    for (int u = 0; u < 4; ++u) {
      if (u != w) {
        const int si = w < u ? w : w - 1;
#pragma unroll
        for (int n = 0; n < 5; ++n) *(LAS f32x4*)(lds + L_A + ((u * 3 + si) * 5 + n) * 1024 + l16) = a4[u][n];
      }
    }
    BAR();
    GAS char* op = (GAS char*)out + (size_t)(64 * (2 * P + jl) + 16 * w + t16) * 1280 + (80 * ct + 4 * g) * 2;
#pragma unroll
    for (int n = 0; n < 5; ++n) {
      f32x4 v = w == 0 ? a4[0][n] : (w == 1 ? a4[1][n] : (w == 2 ? a4[2][n] : a4[3][n]));
      f32x4 p[3];
#pragma unroll
      for (int si = 0; si < 3; ++si) p[si] = *(LAS f32x4*)(lds + L_A + ((w * 3 + si) * 5 + n) * 1024 + l16);
      v = (v + p[0]) + (p[1] + p[2]);
      const v2u o = {pk2(blo(hp[n][0]) + v[0], bhi(hp[n][0]) + v[1]), pk2(blo(hp[n][1]) + v[2], bhi(hp[n][1]) + v[3])};
      *(GAS v2u*)(op + n * 32) = o;
    }
  }
}

extern "C" void kernel_launch(void* const* d_in, const int* in_sizes, int n_in, void* d_out, int out_size, void* d_ws, size_t ws_size,
                              hipStream_t stream) {
  (void)in_sizes; (void)n_in; (void)out_size; (void)ws_size;
  hipFuncSetAttribute((const void*)fused_block, hipFuncAttributeMaxDynamicSharedMemorySize, LDS_BYTES);
  fused_block<<<256, 256, LDS_BYTES, stream>>>((const unsigned short*)d_in[0], (const unsigned short*)d_in[1], (const unsigned short*)d_in[2],
                                               (const unsigned short*)d_in[4], (const unsigned short*)d_in[5], (unsigned short*)d_out,
                                               (char*)d_ws);
}
```
